```python
import torch

def _softmax(x, axis=-1):
    return torch.softmax(x, dim=axis)

def _gelu(x):
    return torch.nn.functional.gelu(x, approximate='tanh')

def _silu(x):
    return torch.nn.functional.silu(x)

def _sigmoid(x):
    return torch.sigmoid(x)

def _layernorm(x, axis=-1, eps=1e-05):
    m = x.mean(dim=axis, keepdim=True)
    v = x.var(dim=axis, keepdim=True, unbiased=False)
    return (x - m) / torch.sqrt(v + eps)

def _scatter_add(tgt, idx, src, axis=0):
    return tgt.clone().index_add_(axis, idx, src)

def _segment_sum(src, seg, n):
    out = torch.zeros((n,) + src.shape[1:], dtype=src.dtype)
    return out.index_add_(0, seg, src)

def _pad(x, pw):
    flat = tuple((v for pair in reversed(pw) for v in pair))
    return torch.nn.functional.pad(x, flat)
kernel_name = 'sequential_preLN__memory_aug_recurrent_mem_tr__dense_activation_relu__prompt'

def setup_inputs():

    _w = lambda a: (lambda t: t.to(torch.bfloat16) if t.is_floating_point() else t)(torch.as_tensor(a))

    def _A_setup():
        B, S, Dm = (1, 2048, 640)
        H, Dh = (32, 128)
        M, seg = (8, 32)
        seg = min(seg, S)
        S = S // seg * seg
        return {'x': _w((lambda *s: torch.randn(*s, dtype=torch.bfloat16) * 0.05)(B, S, Dm)), 'mem0': _w((lambda *s: torch.randn(*s, dtype=torch.bfloat16) * 0.2)(B, M, Dm)), 'Wq': _w((lambda *s: torch.randn(*s, dtype=torch.bfloat16) * 1.6)(Dm, H, Dh) * Dm ** (-0.5)), 'Wk': _w((lambda *s: torch.randn(*s, dtype=torch.bfloat16) * 1.6)(Dm, H, Dh) * Dm ** (-0.5)), 'Wv': _w((lambda *s: torch.randn(*s, dtype=torch.bfloat16) * 0.2)(Dm, H, Dh) * Dm ** (-0.5)), 'Wo': _w((lambda *s: torch.randn(*s, dtype=torch.bfloat16) * 0.2)(H, Dh, Dm) * (H * Dh) ** (-0.5)), 'seg_len': seg}
    _A_in = _A_setup()

    def _B_setup():
        B, S, D, Dff = (1, 2048, 640, 1728)
        return {'x': _w((lambda *s: torch.randn(*s, dtype=torch.bfloat16) * 0.05)(B, S, D)), 'w1': _w((lambda *s: torch.randn(*s, dtype=torch.bfloat16) * 0.05)(D, Dff)), 'w2': _w((lambda *s: torch.randn(*s, dtype=torch.bfloat16) * 0.05)(Dff, D))}
    _B_in = _B_setup()
    return {'A_x': _A_in['x'], 'A_mem0': _A_in['mem0'], 'A_Wq': _A_in['Wq'], 'A_Wk': _A_in['Wk'], 'A_Wv': _A_in['Wv'], 'A_Wo': _A_in['Wo'], 'A_seg_len': _A_in['seg_len'], 'B_x': _B_in['x'], 'B_w1': _B_in['w1'], 'B_w2': _B_in['w2']}

def reference(A_x, A_mem0, A_Wq, A_Wk, A_Wv, A_Wo, A_seg_len, B_x, B_w1, B_w2):

    def _A_ref(x, mem0, Wq, Wk, Wv, Wo, seg_len):
        B, S, Dm = x.shape
        H, Dh = (Wq.shape[1], Wq.shape[2])
        M = mem0.shape[1]
        scale = Dh ** (-0.5)
        n_seg = S // seg_len
        L = M + seg_len
        idx = torch.arange(L)
        mask = (idx[:, None] < M) | (idx[:, None] >= idx[None, :])
        mem = mem0
        outs = []
        for s in range(n_seg):
            xs = x[:, s * seg_len:(s + 1) * seg_len, :].clone()
            h = torch.concatenate([mem, xs], axis=1)
            q = torch.einsum('btd,dhe->bhte', h, Wq)
            k = torch.einsum('btd,dhe->bhte', h, Wk)
            v = torch.einsum('btd,dhe->bhte', h, Wv)
            scores = torch.einsum('bhqe,bhke->bhqk', q, k) * scale
            scores = torch.where(mask[None, None], scores, -1000000000.0)
            p = _softmax(scores, axis=-1)
            o = torch.einsum('bhqk,bhke->bhqe', p, v)
            o = torch.einsum('bhte,hed->btd', o, Wo)
            mem = o[:, :M, :].clone()
            outs.append(o[:, M:, :])
        return torch.concatenate(outs, axis=1)

    def _B_ref(x, w1, w2):
        h = torch.einsum('bsd,df->bsf', x, w1)
        zero = torch.zeros_like(h)
        h = torch.maximum(h, zero)
        return torch.einsum('bsf,fd->bsd', h, w2)
    x = A_x
    h = x + _A_ref(_layernorm(x, axis=-1), A_mem0, A_Wq, A_Wk, A_Wv, A_Wo, A_seg_len)
    h2 = h + _B_ref(_layernorm(h, axis=-1), B_w1, B_w2)
    return h2
```

```cpp
#include <hip/hip_runtime.h>
#include <hip/hip_bf16.h>
#include <stdint.h>
#include <utility>

typedef __bf16 bf16x2 __attribute__((ext_vector_type(2)));
typedef __bf16 bf16x8 __attribute__((ext_vector_type(8)));
typedef __bf16 bf16x16 __attribute__((ext_vector_type(16)));
typedef float f32x2 __attribute__((ext_vector_type(2)));
typedef float f32x4 __attribute__((ext_vector_type(4)));
typedef short s16x4 __attribute__((ext_vector_type(4)));
typedef unsigned u32x2 __attribute__((ext_vector_type(2)));
typedef unsigned u32x4 __attribute__((ext_vector_type(4)));
typedef unsigned u32x8 __attribute__((ext_vector_type(8)));
#define LAS __attribute__((address_space(3)))
#define DEVI __device__ __forceinline__

constexpr int SEQ = 2048, DM = 640, NH = 32, DH = 128, NSEG = 64, DFF = 1728;
constexpr size_t MB = 1u << 20;
constexpr size_t OFF_MISC = 12 * MB, OFF_XN = 13 * MB, OFF_KM = 16 * MB, OFF_Q = 20 * MB, OFF_K = 36 * MB, OFF_V = 52 * MB,
                 OFF_O = 68 * MB, OFF_WOF = 84 * MB, OFF_W1F = 90 * MB, OFF_W2F = 93 * MB, OFF_HP = 96 * MB, OFF_H = 120 * MB,
                 OFF_HN = 126 * MB, OFF_U = 129 * MB, OFF_DBG = 140 * MB;
constexpr unsigned PAGE = 2u << 20;
constexpr unsigned POISON = 0xFFFFFFFFu;
constexpr int SC1 = 16;

template <int N, class F> DEVI void static_for(F&& f) {
  [&]<int... I>(std::integer_sequence<int, I...>) { (f(std::integral_constant<int, I>{}), ...); }(std::make_integer_sequence<int, N>{});
}
DEVI unsigned pk(float lo, float hi) { f32x2 v = {lo, hi}; return __builtin_bit_cast(unsigned, __builtin_convertvector(v, bf16x2)); }
DEVI u32x2 pk4(f32x4 v) { u32x2 r; r.x = pk(v[0], v[1]); r.y = pk(v[2], v[3]); return r; }
DEVI float bflo(unsigned u) { return __uint_as_float(u << 16); }
DEVI float bfhi(unsigned u) { return __uint_as_float(u & 0xffff0000u); }
DEVI f32x4 mma32(u32x4 a, u32x4 b, f32x4 c) { return __builtin_amdgcn_mfma_f32_16x16x32_bf16(__builtin_bit_cast(bf16x8, a), __builtin_bit_cast(bf16x8, b), c, 0, 0, 0); }
DEVI f32x4 mma16(u32x2 a, u32x2 b, f32x4 c) { return __builtin_amdgcn_mfma_f32_16x16x16bf16_1k(__builtin_bit_cast(s16x4, a), __builtin_bit_cast(s16x4, b), c, 0, 0, 0); }
DEVI f32x4 smma(u32x4 a, u32x8 b, f32x4 c, int idx) { return __builtin_amdgcn_smfmac_f32_16x16x64_bf16(__builtin_bit_cast(bf16x8, a), __builtin_bit_cast(bf16x16, b), c, idx, 0, 0); }
DEVI u32x2 trd(LAS char* p) { return __builtin_bit_cast(u32x2, __builtin_amdgcn_ds_read_tr16_b64_v4i16((LAS s16x4*)p)); }
DEVI unsigned ror8(unsigned v) { return (unsigned)__builtin_amdgcn_mov_dpp((int)v, 0x128, 0xf, 0xf, true); }
DEVI float ror8f(float v) { return __uint_as_float(ror8(__float_as_uint(v))); }
DEVI float sum32(float a, float b) { auto r = __builtin_amdgcn_permlane32_swap(__float_as_uint(a), __float_as_uint(b), false, false); return __uint_as_float(r[0]) + __uint_as_float(r[1]); }
DEVI float sum16(float a) { auto r = __builtin_amdgcn_permlane16_swap(__float_as_uint(a), __float_as_uint(a), false, false); return __uint_as_float(r[0]) + __uint_as_float(r[1]); }
DEVI float max32(float a) { auto r = __builtin_amdgcn_permlane32_swap(__float_as_uint(a), __float_as_uint(a), false, false); return fmaxf(__uint_as_float(r[0]), __uint_as_float(r[1])); }
DEVI float max16(float a) { auto r = __builtin_amdgcn_permlane16_swap(__float_as_uint(a), __float_as_uint(a), false, false); return fmaxf(__uint_as_float(r[0]), __uint_as_float(r[1])); }
DEVI void glds(const void* g, unsigned ldsaddr) {
  unsigned sv;
  asm volatile("s_mov_b32 %0, m0\n\ts_mov_b32 m0, %2\n\ts_nop 0\n\tglobal_load_lds_dwordx4 %1, off\n\ts_mov_b32 m0, %0" : "=&s"(sv) : "v"(g), "s"(ldsaddr) : "memory");
}
DEVI void glds_sc1(const void* g, unsigned ldsaddr) {
  unsigned sv;
  asm volatile("s_mov_b32 %0, m0\n\ts_mov_b32 m0, %2\n\ts_nop 0\n\tglobal_load_lds_dwordx4 %1, off sc1\n\ts_mov_b32 m0, %0" : "=&s"(sv) : "v"(g), "s"(ldsaddr) : "memory");
}
template <int N> DEVI void wait_vm() { asm volatile("s_waitcnt vmcnt(%0)" :: "n"(N) : "memory"); }
DEVI void wait_lgkm0() { asm volatile("s_waitcnt lgkmcnt(0)" ::: "memory"); }
DEVI void bar() { asm volatile("s_waitcnt lgkmcnt(0)\n\ts_barrier" ::: "memory"); }
DEVI void delay(int n) {
  if (n <= 0) return;
  int cnt;
  asm volatile("s_mov_b32 %0, %1\n 1: s_nop 4\n s_sub_u32 %0, %0, 1\n s_cmp_lg_u32 %0, 0\n s_cbranch_scc1 1b" : "=&s"(cnt) : "s"(n) : "scc", "memory");
}
DEVI unsigned max4(u32x4 v) { return max(max(v.x, v.y), max(v.z, v.w)); }
DEVI unsigned lds_off(LAS char* p) { return (unsigned)(uintptr_t)p; }

struct ProArgs {
  const unsigned short* x; const unsigned short* mem0; const unsigned short* Wo; const unsigned short* w1; const unsigned short* w2;
  char* wb;
};
DEVI int pi_slot(int g, int j) { return j < 4 ? 4 * g + j : 16 + 4 * g + (j - 4); }

__global__ void __launch_bounds__(256) k_pro(ProArgs p) {
  __shared__ float red[2][4][16];
  const int b = blockIdx.x, tid = threadIdx.x, lane = tid & 63, w = tid >> 6, g = lane >> 4, t = lane & 15;
  if (b < 128) {
    u32x4 v[5]; float s = 0.f;
    for (int i = 0; i < 5; ++i) {
      v[i] = *(const u32x4*)(p.x + (size_t)(b * 16 + t) * DM + (5 * w + i) * 32 + 8 * g);
      for (int k = 0; k < 4; ++k) s += bflo(v[i][k]) + bfhi(v[i][k]);
    }
    s = sum16(s); s = sum32(s, s);
    if (g == 0) red[0][w][t] = s;
    __syncthreads();
    const float mean = (red[0][0][t] + red[0][1][t] + red[0][2][t] + red[0][3][t]) * (1.f / DM);
    float q = 0.f;
    for (int i = 0; i < 5; ++i) for (int k = 0; k < 4; ++k) { float a = bflo(v[i][k]) - mean, c = bfhi(v[i][k]) - mean; q += a * a + c * c; }
    q = sum16(q); q = sum32(q, q);
    if (g == 0) red[1][w][t] = q;
    __syncthreads();
    const float rstd = 1.f / sqrtf((red[1][0][t] + red[1][1][t] + red[1][2][t] + red[1][3][t]) * (1.f / DM) + 1e-5f);
    for (int i = 0; i < 5; ++i) {
      u32x4 o; for (int k = 0; k < 4; ++k) o[k] = pk((bflo(v[i][k]) - mean) * rstd, (bfhi(v[i][k]) - mean) * rstd);
      *(u32x4*)(p.wb + OFF_XN + ((size_t)(b * 20 + 5 * w + i) * 64 + lane) * 16) = o;
    }
    return;
  }
  if (b < 136) {
    if (b == 128 || b == 129) {
      const int page = (b - 128) * 4 + w;
      if (page < 6 && lane == 0) {
        unsigned best[2] = {1u << 30, 1u << 30};
        for (int r = 0; r < 3; ++r)
          for (int hf = 0; hf < 2; ++hf) {
            char* a = p.wb + (size_t)page * PAGE + (PAGE - 16384) + hf * 8192 + 4096 + r * 256;
            unsigned v = r;
            asm volatile("global_store_dword %0, %1, off sc1\n s_waitcnt vmcnt(0)" :: "v"(a), "v"(v) : "memory");
            delay(30);
            unsigned long long c0 = __builtin_amdgcn_s_memtime();
            asm volatile("global_load_dword %0, %1, off sc1\n s_waitcnt vmcnt(0)" : "=v"(v) : "v"(a) : "memory");
            unsigned long long c1 = __builtin_amdgcn_s_memtime();
            unsigned d = (unsigned)(c1 - c0); if (d < best[hf]) best[hf] = d;
          }
        ((int*)(p.wb + OFF_MISC))[page] = best[1] < best[0] ? 1 : 0;
      }
    } else if (b == 130) {
      for (int i = tid; i < 640; i += 256) {
        const int ks = i >> 5, gg = (i >> 3) & 3, row = i & 7;
        u32x4 v = *(const u32x4*)(p.mem0 + row * DM + ks * 32 + gg * 8);
        for (int k = 0; k < 4; ++k) v[k] = min(v[k], POISON - 1);
        const unsigned o = (ks < 10 ? ks * 512 : 16384 + (ks - 10) * 512) + gg * 128 + row * 16;
        *(u32x4*)(p.wb + o) = v; *(u32x4*)(p.wb + o + 8192) = v;
      }
    }
    return;
  }
  if (b < 136 + 96) {
    const u32x4 po = {POISON, POISON, POISON, POISON};
    const int n0 = 4096, n1 = 81920, n2 = 16384;
    for (int i = (b - 136) * 256 + tid; i < n0 + n1 + n2; i += 96 * 256) {
      size_t o = i < n0 ? 32768 + (size_t)i * 16 : i < n0 + n1 ? PAGE + (size_t)(i - n0) * 16 : 2 * (size_t)PAGE + (size_t)(i - n0 - n1) * 16;
      *(u32x4*)(p.wb + o) = po;
    }
    return;
  }
  const int nWo = 40 * 128 * 64, nW1 = 112 * 20 * 64, nW2 = 40 * 54 * 64;
  for (int i = (b - 232) * 256 + tid; i < nWo + nW1 + nW2; i += (gridDim.x - 232) * 256) {
    unsigned short e[8]; char* dst;
    if (i < nWo) {
      const int l = i & 63, kb = (i >> 6) & 127, dt = i >> 13;
      for (int j = 0; j < 8; ++j) e[j] = p.Wo[(size_t)(32 * kb + pi_slot(l >> 4, j)) * DM + 16 * dt + (l & 15)];
      dst = p.wb + OFF_WOF + (size_t)i * 16;
    } else if (i < nWo + nW1) {
      const int ii = i - nWo, l = ii & 63, kb = (ii >> 6) % 20, ft = (ii >> 6) / 20;
      for (int j = 0; j < 8; ++j) e[j] = ft < 108 ? p.w1[(size_t)(32 * kb + 8 * (l >> 4) + j) * DFF + 16 * ft + (l & 15)] : (unsigned short)0;
      dst = p.wb + OFF_W1F + (size_t)ii * 16;
    } else {
      const int ii = i - nWo - nW1, l = ii & 63, kb = (ii >> 6) % 54, dt = (ii >> 6) / 54;
      for (int j = 0; j < 8; ++j) e[j] = p.w2[(size_t)(32 * kb + pi_slot(l >> 4, j)) * DM + 16 * dt + (l & 15)];
      dst = p.wb + OFF_W2F + (size_t)ii * 16;
    }
    u32x4 o; for (int k = 0; k < 4; ++k) o[k] = e[2 * k] | ((unsigned)e[2 * k + 1] << 16);
    *(u32x4*)dst = o;
  }
}

struct QkvArgs { const unsigned short* Wq; const unsigned short* Wk; const unsigned short* Wv; char* wb; };

DEVI void qkv_work(LAS char* lds, const QkvArgs& p, int c, int r0, int r1) {
  const int tid = threadIdx.x, lane = tid & 63, w = __builtin_amdgcn_readfirstlane(tid >> 6), g = lane >> 4;
  const int h = c >> 2, jq = c & 3;
  const unsigned lb = lds_off(lds);
  for (int i = w; i < 120; i += 4) {
    const int mat = i / 40, d = (i % 40) * 16 + (lane >> 2), pc = (lane & 3) ^ (((d >> 3) & 1) << 1);
    const unsigned short* W = mat == 0 ? p.Wq : mat == 1 ? p.Wk : p.Wv;
    glds(W + (size_t)d * 4096 + h * 128 + jq * 32 + pc * 8, lb + i * 1024);
  }
  wait_vm<0>();
  bar();
  const int i16 = lane & 15;
  LAS char* wa[2];
  for (int et = 0; et < 2; ++et) wa[et] = lds + (8 * g + (i16 >> 2)) * 64 + ((et ^ (g & 1)) * 32) + (i16 & 3) * 8;
  const char* xn = p.wb + OFF_XN + lane * 16;
  for (int r = r0; r < r1; ++r) {
    const int tb = r * 16 + w * 4;
    f32x4 acc[3][2][4];
    for (int m = 0; m < 3; ++m) for (int e = 0; e < 2; ++e) for (int n = 0; n < 4; ++n) acc[m][e][n] = (f32x4){0.f, 0.f, 0.f, 0.f};
    u32x4 bf[4][4];
#pragma unroll
    for (int s = 0; s < 3; ++s)
#pragma unroll
      for (int n = 0; n < 4; ++n) bf[s][n] = *(const u32x4*)(xn + (size_t)((tb + n) * 20 + s) * 1024);
#pragma unroll
    for (int ks = 0; ks < 20; ++ks) {
      if (ks + 3 < 20) {
#pragma unroll
        for (int n = 0; n < 4; ++n) bf[(ks + 3) & 3][n] = *(const u32x4*)(xn + (size_t)((tb + n) * 20 + ks + 3) * 1024);
      }
#pragma unroll
      for (int m = 0; m < 3; ++m)
#pragma unroll
        for (int e = 0; e < 2; ++e) {
          u32x2 lo = trd(wa[e] + m * 40960 + ks * 2048), hi = trd(wa[e] + m * 40960 + ks * 2048 + 256);
          u32x4 a = {lo.x, lo.y, hi.x, hi.y};
#pragma unroll
          for (int n = 0; n < 4; ++n) acc[m][e][n] = mma32(a, bf[ks & 3][n], acc[m][e][n]);
        }
    }
#pragma unroll
    for (int m = 0; m < 3; ++m)
#pragma unroll
      for (int n = 0; n < 4; ++n) {
        u32x2 lo = pk4(acc[m][0][n]), hi = pk4(acc[m][1][n]);
        u32x4 o = {lo.x, lo.y, hi.x, hi.y};
        const int tt = tb + n;
        char* dst = p.wb + OFF_Q + (size_t)m * 16 * MB + ((size_t)((tt >> 1) * 32 + h) * 8 + (tt & 1) * 4 + jq) * 1024 + lane * 16;
        *(u32x4*)dst = o;
      }
  }
}
__global__ void __launch_bounds__(256, 1) k_qkv(QkvArgs p) {
  extern __shared__ __attribute__((aligned(16))) char smem[];
  const int b = blockIdx.x;
  const int c = (b & 7) * 16 + ((b >> 3) & 15), hf = b >> 7;
  qkv_work((LAS char*)smem, p, c, hf * 4, hf * 4 + 4);
}

struct RecArgs { const unsigned short* Wq; const unsigned short* Wk; const unsigned short* Wv; const unsigned short* Wo; char* wb; int d1, d2, d3, dbg; };
constexpr int L_MST = 0, L_VP = 10240, L_SP = L_VP + 4 * 8 * 144, L_KX = 22528, L_VX = L_KX + 16384, L_WV = L_VX + 16384, L_WO = L_WV + 40960;
constexpr int SP_ROW = 208, VP_ROW = 144;
constexpr int SPIN_MAX = 1 << 18;
#define ST(i)

DEVI void rec_work(LAS char* lds, const RecArgs& p, int h, int j) {
  const int tid = threadIdx.x, lane = tid & 63, w = __builtin_amdgcn_readfirstlane(tid >> 6);
  const int G = lane >> 4, c = lane & 15, eta = (lane >> 3) & 1, r8 = lane & 7;
  const unsigned lb = lds_off(lds);
  const int role = h * 4 + j;
  u32x8 wq[20], wk[20], wv[5], wop[4]; u32x4 wo2[4];
  {
    const unsigned rb = lb + w * 36864;
    LAS char* rp = lds + w * 36864;
    const int n0 = 160 * j + 40 * w;
    auto issue = [&](auto U) {
      constexpr int u = U; const unsigned dst = rb + (u % 9) * 4096;
      if constexpr (u < 20) {
        const int ks = 5 * ((w + u / 5) & 3) + u % 5;
        const size_t o = (size_t)(32 * ks + (lane >> 2)) * 4096 + h * 128 + 32 * w + (lane & 3) * 8;
        glds(p.Wq + o, dst); glds(p.Wq + o + 16 * 4096, dst + 1024); glds(p.Wk + o, dst + 2048); glds(p.Wk + o + 16 * 4096, dst + 3072);
      } else if constexpr (u < 23) {
        const int ks = 5 * w + 2 * (u - 20);
        const size_t o = (size_t)(32 * ks + (lane >> 2)) * 4096 + h * 128 + 32 * j + (lane & 3) * 8;
        glds(p.Wv + o, dst); glds(p.Wv + o + 16 * 4096, dst + 1024);
        if constexpr (u < 22) { glds(p.Wv + o + 32 * 4096, dst + 2048); glds(p.Wv + o + 48 * 4096, dst + 3072); }
      } else if constexpr (u < 27) {
#pragma unroll
        for (int t = 0; t < 3; ++t) {
          const int idx = t * 64 + lane, row = idx / 6, pc = min(idx % 6, 4);
          glds(p.Wo + (size_t)(h * 128 + 32 * (u - 23) + row) * DM + n0 + pc * 8, dst + t * 1024);
        }
      }
    };
    constexpr auto cnt = [](int u) { return u < 22 ? 4 : u == 22 ? 2 : u < 27 ? 3 : 0; };
    static_for<8>([&](auto U) { issue(U); });
    const int q4 = c >> 2, p4 = c & 3;
    const int a64 = (4 * G + (q4 & 1)) * 64 + (q4 >> 1) * 32 + p4 * 8;
    const int a96 = (16 * (G & 1) + 4 * (G >> 1) + (q4 & 1)) * 96 + (q4 >> 1) * 32 + p4 * 8;
    const int a96d = (4 * G + q4) * 96 + 64 + p4 * 8;
    auto rd64 = [&](LAS char* b) { u32x2 x0 = trd(b + a64), x1 = trd(b + a64 + 128), x2 = trd(b + a64 + 1024), x3 = trd(b + a64 + 1152);
      return (u32x8){x0.x, x0.y, x1.x, x1.y, x2.x, x2.y, x3.x, x3.y}; };
    static_for<27>([&](auto U) {
      constexpr int u = U;
      constexpr int later = cnt(u + 1) + cnt(u + 2) + cnt(u + 3) + cnt(u + 4) + cnt(u + 5) + cnt(u + 6) + cnt(u + 7);
      wait_vm<later>();
      LAS char* b = rp + (u % 9) * 4096;
      if constexpr (u < 20) {
        wq[u] = rd64(b); wk[u] = rd64(b + 2048);
        if constexpr (u < 15) { asm volatile("" : "+a"(wq[u])); asm volatile("" : "+a"(wk[u])); }
      } else if constexpr (u < 23) {
        wv[2 * (u - 20)] = rd64(b);
        if constexpr (u < 22) wv[2 * (u - 20) + 1] = rd64(b + 2048);
      } else {
        u32x2 x0 = trd(b + a96), x1 = trd(b + a96 + 2 * 96), x2 = trd(b + a96 + 8 * 96), x3 = trd(b + a96 + 10 * 96);
        wop[u - 23] = (u32x8){x0.x, x0.y, x1.x, x1.y, x2.x, x2.y, x3.x, x3.y};
        u32x2 y0 = trd(b + a96d), y1 = trd(b + a96d + 16 * 96);
        wo2[u - 23] = (u32x4){y0.x, y0.y, y1.x, y1.y};
      }
      wait_lgkm0();
      issue(std::integral_constant<int, u + 8>{});
    });
  }
  bar();
  LAS char* wvL = lds + L_WV + w * 10240 + lane * 16;
#pragma unroll
  for (int i = 0; i < 5; ++i) { *(LAS u32x4*)(wvL + i * 2048) = wv[i].lo; *(LAS u32x4*)(wvL + i * 2048 + 1024) = wv[i].hi; }
  LAS char* woL = lds + L_WO + w * 12288 + lane * 16;
#pragma unroll
  for (int i = 0; i < 4; ++i) { *(LAS u32x4*)(woL + i * 3072) = wop[i].lo; *(LAS u32x4*)(woL + i * 3072 + 1024) = wop[i].hi; *(LAS u32x4*)(woL + i * 3072 + 2048) = wo2[i]; }
  auto load_wo = [&]() {
#pragma unroll
    for (int i = 0; i < 4; ++i) { wop[i].lo = *(LAS u32x4*)(woL + i * 3072); wop[i].hi = *(LAS u32x4*)(woL + i * 3072 + 1024); wo2[i] = *(LAS u32x4*)(woL + i * 3072 + 2048); }
  };
  const __amdgpu_buffer_rsrc_t rs = __builtin_amdgcn_make_buffer_rsrc(p.wb, 0, 12 * MB, 0x00020000);
  const int* selp = (const int*)(p.wb + OFF_MISC);
  const unsigned selM = selp[0] * 8192u, selP = PAGE + selp[1] * 8192u;
  const unsigned selV = (unsigned)(selp[2] | (selp[3] << 1) | (selp[4] << 2) | (selp[5] << 3));
  const int idxH = eta ? 0xEEEE : 0x4444;
  const int frag = G * 128 + r8 * 16;
  const unsigned gso = selM + (w >> 1) * 16384 + (w & 1) * 2560;
  LAS char* mstW = lds + L_MST + w * 2560 + frag;
  LAS char* mstR[3];
#pragma unroll
  for (int q = 0; q < 3; ++q) mstR[q] = lds + L_MST + ((w + q + 1) & 3) * 2560 + frag;
  LAS char* vpW = lds + L_VP + (w * 8 + 4 * (G & 1)) * VP_ROW + (16 * (G >> 1) + c) * 4;
  LAS char* vpR = lds + L_VP + r8 * VP_ROW + (16 * eta + 4 * G) * 4;
  const unsigned vmSt = (h & 3) * 2048 + j * 512 + G * 128 + r8 * 16 + eta * 8;
  const unsigned vmLd = (h & 3) * 2048 + frag;
  LAS char* spW = lds + L_SP + (w * 8 + r8) * SP_ROW + G * 16;
  LAS char* spRx = lds + L_SP + r8 * SP_ROW + (8 * G + 4 * eta) * 4;
  LAS char* spRm = lds + L_SP + r8 * SP_ROW + (32 + 4 * (G >> 1) + 2 * eta) * 4;
  const int csw = (c & 9) | ((c & 2) << 1) | ((c & 4) >> 1);
  LAS char* kxR = lds + L_KX + w * 1024 + G * 256 + csw * 16;
  LAS char* vxR = lds + L_VX + lane * 16;
  u32x2 ident; { const int d = c - 4 * G; ident.x = (d == 0 ? 0x3f80u : 0u) | (d == 1 ? 0x3f800000u : 0u); ident.y = (d == 2 ? 0x3f80u : 0u) | (d == 3 ? 0x3f800000u : 0u); }
  u32x4 selm; { const unsigned v = (r8 & 1) ? 0x3f800000u : 0x3f80u; for (int k = 0; k < 4; ++k) selm[k] = (r8 >> 1) == k ? v : 0u; }
  unsigned pst01, pst2;
  { const int inner = (((c & 1) << 1) | ((c >> 1) & 1)) * 16 + ((c >> 2) & 1) * 8;
    int ch = (20 * j + 5 * w + 2 * (G >> 1) + eta) * 2 + (G & 1); pst01 = (ch >> 2) * 16384 + (ch & 3) * 2048 + h * 64 + inner;
    ch = (20 * j + 5 * w + 4) * 2 + (G & 1); pst2 = (ch >> 2) * 16384 + (ch & 3) * 2048 + h * 64 + inner; }
  const int red = w == 0 ? role : (w == 1 && j == 0) ? 128 + h : -1;
  const unsigned rld = (red >> 2) * 16384 + (red & 3) * 2048 + lane * 16;
  unsigned mst; { const int cb = red >> 1, ks = cb >> 2; mst = (ks < 10 ? ks * 512 : 16384 + (ks - 10) * 512) + (cb & 3) * 128 + (4 * (red & 1) + (lane & 3)) * 16 + ((lane >> 2) & 1) * 8 + (lane >> 5) * 4; }
  const char* kxg = p.wb + OFF_K + (size_t)h * 8192 + lane * 16;
  const char* vxg = p.wb + OFF_V + (size_t)h * 8192 + lane * 16;
  char* kmg = p.wb + OFF_KM + (size_t)h * 2048 + w * 512 + frag;
  const float cs = 0.08838834764831845f * 1.4426950408889634f;
  int fail = 0;

  auto stage_kv = [&](int s) {
    if (w >= 2) {
      const char* src = (w == 2 ? kxg : vxg) + (size_t)s * 262144;
      const unsigned dst = lb + (w == 2 ? L_KX : L_VX) + (s & 1) * 8192;
#pragma unroll
      for (int i = 0; i < 8; ++i) glds_sc1(src + i * 1024, dst + i * 1024);
    }
  };
  u32x2 zx[2][3];
  auto make_zx = [&](int s) {
#pragma unroll
    for (int T = 0; T < 2; ++T) {
      f32x4 z0 = {0.f, 0.f, 0.f, 0.f}, z1 = z0, z2 = z0;
#pragma unroll
      for (int ks = 0; ks < 4; ++ks) {
        u32x4 a = *(LAS u32x4*)(vxR + (s & 1) * 8192 + (T * 4 + ks) * 1024);
        z0 = smma(a, wop[ks], z0, 0x4444); z1 = smma(a, wop[ks], z1, 0xEEEE); z2 = mma32(a, wo2[ks], z2);
      }
      zx[T][0] = pk4(z0); zx[T][1] = pk4(z1); zx[T][2] = pk4(z2);
    }
  };
  stage_kv(0); stage_kv(1);
  wait_vm<0>();
  bar();
  make_zx(0);

  for (int s = 0; s < NSEG; ++s) {
    ST(0)
    u32x4 am[5];
#pragma unroll
    for (int i = 0; i < 5; ++i) { wv[i].lo = *(LAS u32x4*)(wvL + i * 2048); wv[i].hi = *(LAS u32x4*)(wvL + i * 2048 + 1024); }
    { const unsigned so = gso + s * 32768u;
      for (int sp = 0;; ++sp) {
        asm volatile("" ::: "memory");
        unsigned m = 0;
#pragma unroll
        for (int i = 0; i < 5; ++i) am[i] = __builtin_amdgcn_raw_buffer_load_b128(rs, frag + i * 512, so, SC1);
#pragma unroll
        for (int i = 0; i < 5; ++i) m = max(m, max4(am[i]));
        if (!__any(m == POISON) || fail) break;
        if (sp > SPIN_MAX) fail = 1;
      } }
    ST(1)
    if (!eta) {
#pragma unroll
      for (int i = 0; i < 5; ++i) *(LAS u32x4*)(mstW + i * 512) = am[i];
    }
    f32x4 av = {0.f, 0.f, 0.f, 0.f}, aq = av, ak = av;
#pragma unroll
    for (int i = 0; i < 5; ++i) av = smma(am[i], wv[i], av, idxH);
#pragma unroll
    for (int i = 0; i < 5; ++i) { ak = smma(am[i], wk[i], ak, idxH); aq = smma(am[i], wq[i], aq, idxH); }
#pragma unroll
    for (int r = 0; r < 4; ++r) *(LAS float*)(vpW + r * VP_ROW) = av[r];
    bar();
    ST(2)
    const unsigned vso = (2 + (s >> 4)) * PAGE + ((s & 15) * 8 + (h >> 2)) * 16384u + ((selV >> (s >> 4)) & 1) * 8192u;
    if (w == 3) {
      f32x4 a0 = *(LAS f32x4*)(vpR), a1 = *(LAS f32x4*)(vpR + 8 * VP_ROW), a2 = *(LAS f32x4*)(vpR + 16 * VP_ROW), a3 = *(LAS f32x4*)(vpR + 24 * VP_ROW);
      u32x2 o = pk4((a0 + a1) + (a2 + a3)); o.x = min(o.x, POISON - 1); o.y = min(o.y, POISON - 1);
      __builtin_amdgcn_raw_buffer_store_b64(o, rs, vmSt, vso, SC1);
      if (s + 2 < NSEG) {
        const int s2 = s + 2;
        const unsigned vso2 = (2 + (s2 >> 4)) * PAGE + ((s2 & 15) * 8 + (h >> 2)) * 16384u + ((selV >> (s2 >> 4)) & 1) * 8192u;
        __builtin_amdgcn_raw_buffer_store_b64((u32x2){POISON, POISON}, rs, vmSt, vso2, SC1);
      }
    }
    u32x4 vm[4];
#pragma unroll
    for (int q = 0; q < 3; ++q) {
#pragma unroll
      for (int i = 0; i < 5; ++i) {
        u32x4 a = *(LAS u32x4*)(mstR[q] + i * 512);
        ak = smma(a, wk[5 * q + 5 + i], ak, idxH); aq = smma(a, wq[5 * q + 5 + i], aq, idxH);
      }
      if (q == 1) {
        asm volatile("" ::: "memory");
#pragma unroll
        for (int i = 0; i < 4; ++i) vm[i] = __builtin_amdgcn_raw_buffer_load_b128(rs, vmLd + i * 512, vso, SC1);
      }
    }
    u32x4 kx0 = *(LAS u32x4*)(kxR + (s & 1) * 8192), kx1 = *(LAS u32x4*)(kxR + (s & 1) * 8192 + 4096);
    const f32x4 z4 = {0.f, 0.f, 0.f, 0.f};
    u32x2 kt = pk4(mma16(pk4(ak), ident, z4)), qt = pk4(mma16(pk4(aq), ident, z4));
    u32x4 kf = {kt.x, kt.y, ror8(kt.x), ror8(kt.y)}, qf = {qt.x, qt.y, ror8(qt.x), ror8(qt.y)};
    if (s == NSEG - 1) {
      if (w == j && !eta) *(u32x4*)(kmg + (size_t)s * 65536) = kf;
      break;
    }
    ST(3)
    f32x4 s0 = mma32(kx0, qf, z4), s1 = mma32(kx1, qf, z4), sm = mma32(kf, qf, z4);
    if (!eta) { *(LAS f32x4*)(spW) = s0; *(LAS f32x4*)(spW + 64) = s1; *(LAS f32x4*)(spW + 128) = sm; }
    bar();
    ST(4)
    load_wo();
    f32x4 sx; f32x2 sy;
    { f32x4 x0 = *(LAS f32x4*)(spRx), x1 = *(LAS f32x4*)(spRx + 8 * SP_ROW), x2 = *(LAS f32x4*)(spRx + 16 * SP_ROW), x3 = *(LAS f32x4*)(spRx + 24 * SP_ROW);
      f32x2 y0 = *(LAS f32x2*)(spRm), y1 = *(LAS f32x2*)(spRm + 8 * SP_ROW), y2 = *(LAS f32x2*)(spRm + 16 * SP_ROW), y3 = *(LAS f32x2*)(spRm + 24 * SP_ROW);
      sx = (x0 + x1) + (x2 + x3); sy = (y0 + y1) + (y2 + y3); }
    float mx = fmaxf(fmaxf(fmaxf(sx[0], sx[1]), fmaxf(sx[2], sx[3])), fmaxf(sy[0], sy[1]));
    mx = fmaxf(mx, ror8f(mx)); mx = max16(mx); mx = max32(mx);
    const float mc = -mx * cs;
    float e0 = __builtin_amdgcn_exp2f(fmaf(sx[0], cs, mc)), e1 = __builtin_amdgcn_exp2f(fmaf(sx[1], cs, mc)), e2 = __builtin_amdgcn_exp2f(fmaf(sx[2], cs, mc)),
          e3 = __builtin_amdgcn_exp2f(fmaf(sx[3], cs, mc)), e4 = __builtin_amdgcn_exp2f(fmaf(sy[0], cs, mc)), e5 = __builtin_amdgcn_exp2f(fmaf(sy[1], cs, mc));
    float l = ((e0 + e1) + (e2 + e3)) + ((G & 1) ? 0.f : e4 + e5);
    l += ror8f(l); l = sum16(l); l = sum32(l, l);
    const float rl = __builtin_amdgcn_rcpf(l);
    const unsigned pm = pk(e4 * rl, e5 * rl);
    const u32x4 pa = {pk(e0 * rl, e1 * rl), G < 2 ? pm : 0u, pk(e2 * rl, e3 * rl), G < 2 ? 0u : pm};
    ST(5)
    for (int sp = 0;; ++sp) {
      unsigned m = max(max(max4(vm[0]), max4(vm[1])), max(max4(vm[2]), max4(vm[3])));
      if (!__any(m == POISON) || fail) break;
      if (sp > SPIN_MAX) fail = 1;
      asm volatile("" ::: "memory");
#pragma unroll
      for (int i = 0; i < 4; ++i) vm[i] = __builtin_amdgcn_raw_buffer_load_b128(rs, vmLd + i * 512, vso, SC1);
    }
    f32x4 zp = z4, z2 = z4;
#pragma unroll
    for (int i = 0; i < 4; ++i) { zp = smma(vm[i], wop[i], zp, idxH); z2 = mma32(vm[i], wo2[i], z2); }
    const u32x2 zpp = pk4(zp), z2p = pk4(z2);
    const u32x8 Z0 = {zx[0][0].x, zx[0][0].y, (G == 0 ? zpp.x : 0u), (G == 0 ? zpp.y : 0u), zx[1][0].x, zx[1][0].y, (G == 1 ? zpp.x : 0u), (G == 1 ? zpp.y : 0u)};
    const u32x8 Z1 = {zx[0][1].x, zx[0][1].y, (G == 2 ? zpp.x : 0u), (G == 2 ? zpp.y : 0u), zx[1][1].x, zx[1][1].y, (G == 3 ? zpp.x : 0u), (G == 3 ? zpp.y : 0u)};
    const u32x8 Z2 = {zx[0][2].x, zx[0][2].y, (G == 0 ? z2p.x : 0u), (G == 0 ? z2p.y : 0u), zx[1][2].x, zx[1][2].y, (G == 1 ? z2p.x : 0u), (G == 1 ? z2p.y : 0u)};
    f32x4 o0 = smma(pa, Z0, z4, idxH), o1 = smma(pa, Z1, z4, idxH), o2 = smma(pa, Z2, z4, idxH);
    f32x4 oa, ob;
#pragma unroll
    for (int r = 0; r < 4; ++r) { oa[r] = sum32(o0[r], o1[r]); ob[r] = sum32(o2[r], o2[r]); }
    u32x2 pa01 = pk4(oa), pb2 = pk4(ob);
    pa01.x = min(pa01.x, POISON - 1); pa01.y = min(pa01.y, POISON - 1); pb2.x = min(pb2.x, POISON - 1); pb2.y = min(pb2.y, POISON - 1);
    const unsigned pso = selP + (s & 1) * (40u * 16384u);
    wait_vm<0>();
    __builtin_amdgcn_raw_buffer_store_b64(pa01, rs, pst01, pso, SC1);
    if (lane < 32 && !eta) __builtin_amdgcn_raw_buffer_store_b64(pb2, rs, pst2, pso, SC1);
    ST(6)
    if (w == j && !eta) *(u32x4*)(kmg + (size_t)s * 65536) = kf;
    make_zx(s + 1);
    ST(7)
    if (red >= 0) {
      delay(p.d1);
      u32x4 v0, v1;
      for (int sp = 0;; ++sp) {
        asm volatile("" ::: "memory");
        v0 = __builtin_amdgcn_raw_buffer_load_b128(rs, rld, pso, SC1);
        v1 = __builtin_amdgcn_raw_buffer_load_b128(rs, rld + 1024, pso, SC1);
        if (!__any(max(max4(v0), max4(v1)) == POISON) || fail) break;
        if (sp > SPIN_MAX) fail = 1;
      }
      ST(8)
      f32x4 d = mma32(v0, selm, z4); d = mma32(v1, selm, d);
      float x = sum16(sum32(d[0], d[1])), y = sum16(sum32(d[2], d[3]));
      const unsigned o = min(pk(x, y), POISON - 1);
      if ((lane & 31) < 8) {
        __builtin_amdgcn_raw_buffer_store_b32(o, rs, mst, selM + (s + 1) * 32768u, SC1);
        if (s + 3 < NSEG) __builtin_amdgcn_raw_buffer_store_b32(POISON, rs, mst, selM + (s + 3) * 32768u, SC1);
      }
      const u32x4 po = {POISON, POISON, POISON, POISON};
      __builtin_amdgcn_raw_buffer_store_b128(po, rs, rld, pso, SC1);
      __builtin_amdgcn_raw_buffer_store_b128(po, rs, rld + 1024, pso, SC1);
    }
    bar();
    if (s + 2 < NSEG) stage_kv(s + 2);
    ST(9)
    delay(p.d2);
    ST(10)
  }
}
__global__ void __launch_bounds__(256, 1) k_rec(RecArgs p) {
  extern __shared__ __attribute__((aligned(16))) char smem[];
  const int b = blockIdx.x;
  if ((b & 7) >= 4) return;
  const int t = b >> 3;
  rec_work((LAS char*)smem, p, (t >> 2) * 4 + (b & 3), t & 3);
}

DEVI void attn_work(char* wb, int s, int h, unsigned selV) {
  const int lane = threadIdx.x & 63, g = lane >> 4, n = lane & 15;
  const char* qb = wb + OFF_Q + ((size_t)s * 32 + h) * 8192 + lane * 16;
  u32x4 q[2][4], k[3][4], v[3][4];
  const char* kmb = wb + OFF_KM + ((size_t)s * 32 + h) * 2048 + g * 128 + (n & 7) * 16;
  const char* vmb = wb + (size_t)(2 + (s >> 4)) * PAGE + ((s & 15) * 8 + (h >> 2)) * 16384u + ((selV >> (s >> 4)) & 1) * 8192u + (h & 3) * 2048 + g * 128 + (n & 7) * 16;
#pragma unroll
  for (int ks = 0; ks < 4; ++ks) {
    k[2][ks] = *(const u32x4*)(kmb + ks * 512);
#pragma unroll
    for (int T = 0; T < 2; ++T) { q[T][ks] = *(const u32x4*)(qb + (T * 4 + ks) * 1024); k[T][ks] = *(const u32x4*)(qb + 16 * MB + (T * 4 + ks) * 1024); }
  }
#pragma unroll
  for (int ks = 0; ks < 4; ++ks) {
    v[2][ks] = *(const u32x4*)(vmb + ks * 512);
#pragma unroll
    for (int T = 0; T < 2; ++T) v[T][ks] = *(const u32x4*)(qb + 32 * MB + (T * 4 + ks) * 1024);
  }
  const f32x4 z4 = {0.f, 0.f, 0.f, 0.f};
  const float cs = 0.08838834764831845f * 1.4426950408889634f;
  const float NEG = -__builtin_inff();
  u32x4 px[2]; u32x2 pm[2]; float rl[2];
#pragma unroll
  for (int qt = 0; qt < 2; ++qt) {
    f32x4 sm = z4, s0 = z4, s1 = z4;
#pragma unroll
    for (int ks = 0; ks < 4; ++ks) { sm = mma32(k[2][ks], q[qt][ks], sm); s0 = mma32(k[0][ks], q[qt][ks], s0); if (qt == 1) s1 = mma32(k[1][ks], q[qt][ks], s1); }
    float mx = NEG;
#pragma unroll
    for (int r = 0; r < 4; ++r) {
      if (g >= 2) sm[r] = NEG;
      const bool vis = 4 * g + r <= n;
      if (qt == 0) { if (!vis) s0[r] = NEG; s1[r] = NEG; } else if (!vis) s1[r] = NEG;
      mx = fmaxf(mx, fmaxf(sm[r], fmaxf(s0[r], s1[r])));
    }
    mx = max16(mx); mx = max32(mx);
    const float mc = -mx * cs; float l = 0.f;
#pragma unroll
    for (int r = 0; r < 4; ++r) {
      sm[r] = __builtin_amdgcn_exp2f(fmaf(sm[r], cs, mc)); s0[r] = __builtin_amdgcn_exp2f(fmaf(s0[r], cs, mc)); s1[r] = qt ? __builtin_amdgcn_exp2f(fmaf(s1[r], cs, mc)) : 0.f;
      l += sm[r] + s0[r] + s1[r];
    }
    l = sum16(l); l = sum32(l, l);
    const float il = __builtin_amdgcn_rcpf(l);
    u32x2 a = pk4(s0 * il), b = pk4(s1 * il);
    px[qt] = (u32x4){a.x, a.y, b.x, b.y}; pm[qt] = pk4(sm * il); rl[qt] = il;
  }
  u32x2 ident; { const int d = n - 4 * g; ident.x = (d == 0 ? 0x3f80u : 0u) | (d == 1 ? 0x3f800000u : 0u); ident.y = (d == 2 ? 0x3f80u : 0u) | (d == 3 ? 0x3f800000u : 0u); }
#pragma unroll
  for (int ks = 0; ks < 4; ++ks) {
    f32x4 o[2][2];
#pragma unroll
    for (int hf = 0; hf < 2; ++hf) {
      u32x2 t0 = pk4(mma16(hf ? (u32x2){v[0][ks].z, v[0][ks].w} : (u32x2){v[0][ks].x, v[0][ks].y}, ident, z4));
      u32x2 t1 = pk4(mma16(hf ? (u32x2){v[1][ks].z, v[1][ks].w} : (u32x2){v[1][ks].x, v[1][ks].y}, ident, z4));
      u32x2 tm = pk4(mma16(hf ? (u32x2){v[2][ks].z, v[2][ks].w} : (u32x2){v[2][ks].x, v[2][ks].y}, ident, z4));
      const u32x4 vt = {t0.x, t0.y, t1.x, t1.y};
#pragma unroll
      for (int qt = 0; qt < 2; ++qt) o[qt][hf] = mma32((u32x4){tm.x, tm.y, 0u, 0u}, (u32x4){pm[qt].x, pm[qt].y, 0u, 0u}, mma32(vt, px[qt], z4));
    }
#pragma unroll
    for (int qt = 0; qt < 2; ++qt) {
      u32x2 a = pk4(o[qt][0]), b = pk4(o[qt][1]);
      *(u32x4*)(wb + OFF_O + ((size_t)(s * 2 + qt) * 128 + h * 4 + ks) * 1024 + lane * 16) = (u32x4){a.x, a.y, b.x, b.y};
    }
  }
}
__global__ void __launch_bounds__(256) k_attn(char* wb) {
  const int* selp = (const int*)(wb + OFF_MISC);
  const unsigned selV = (unsigned)(selp[2] | (selp[3] << 1) | (selp[4] << 2) | (selp[5] << 3));
  const int id = blockIdx.x * 4 + (threadIdx.x >> 6);
  attn_work(wb, id >> 5, id & 31, selV);
}

template <int WM, int WN, int NS, class EPI>
DEVI void fgemm(LAS char* lds, const char* A, int akb, const char* B, int bkb, int nkb, EPI epi) {
  constexpr int NF = 2 * WM + 2 * WN, PER = (NF + 3) / 4;
  const int tid = threadIdx.x, lane = tid & 63, w = __builtin_amdgcn_readfirstlane(tid >> 6), wm = w >> 1, wn = w & 1;
  const unsigned lb = lds_off(lds);
  const char* src[PER];
#pragma unroll
  for (int i = 0; i < PER; ++i) { const int f = min(w * PER + i, NF - 1); src[i] = (f < 2 * WM ? A + (size_t)f * akb * 1024 : B + (size_t)(f - 2 * WM) * bkb * 1024) + lane * 16; }
  auto issue = [&](int kb) {
    const int kk = min(kb, nkb - 1);
#pragma unroll
    for (int i = 0; i < PER; ++i) glds(src[i] + (size_t)kk * 1024, lb + ((kb % NS) * (4 * PER) + w * PER + i) * 1024);
  };
  f32x4 acc[WM][WN];
#pragma unroll
  for (int m = 0; m < WM; ++m)
#pragma unroll
    for (int n = 0; n < WN; ++n) acc[m][n] = (f32x4){0.f, 0.f, 0.f, 0.f};
#pragma unroll
  for (int s = 0; s < NS - 1; ++s) issue(s);
  for (int kb = 0; kb < nkb; ++kb) {
    wait_vm<(NS - 2) * PER>();
    bar();
    issue(kb + NS - 1);
    LAS char* st = lds + (kb % NS) * (4 * PER) * 1024 + lane * 16;
    u32x4 a[WM], b[WN];
#pragma unroll
    for (int m = 0; m < WM; ++m) a[m] = *(LAS u32x4*)(st + (wm * WM + m) * 1024);
#pragma unroll
    for (int n = 0; n < WN; ++n) b[n] = *(LAS u32x4*)(st + (2 * WM + wn * WN + n) * 1024);
#pragma unroll
    for (int m = 0; m < WM; ++m)
#pragma unroll
      for (int n = 0; n < WN; ++n) acc[m][n] = mma32(a[m], b[n], acc[m][n]);
  }
  wait_vm<0>();
  epi(acc, wm, wn, lane);
}

__global__ void __launch_bounds__(256) k_wo(char* wb) {
  extern __shared__ __attribute__((aligned(16))) char smem[];
  const int b = blockIdx.x, kq = b & 3, mq = (b >> 2) & 3, nb = b >> 4;
  fgemm<5, 4, 4>((LAS char*)smem, wb + OFF_WOF + ((size_t)mq * 10 * 128 + kq * 32) * 1024, 128, wb + OFF_O + ((size_t)nb * 8 * 128 + kq * 32) * 1024, 128, 32,
    [&](f32x4 (&acc)[5][4], int wm, int wn, int lane) {
      float* hp = (float*)(wb + OFF_HP) + (size_t)kq * SEQ * DM;
#pragma unroll
      for (int m = 0; m < 5; ++m)
#pragma unroll
        for (int n = 0; n < 4; ++n)
          *(f32x4*)(hp + (size_t)(nb * 128 + (wn * 4 + n) * 16 + (lane & 15)) * DM + mq * 160 + (wm * 5 + m) * 16 + (lane >> 4) * 4) = acc[m][n];
    });
}
__global__ void __launch_bounds__(256) k_ln2(const unsigned short* x, char* wb) {
  __shared__ float red[2][4][16];
  const int b = blockIdx.x, tid = threadIdx.x, lane = tid & 63, w = tid >> 6, g = lane >> 4, t = lane & 15;
  float v[5][8]; float s = 0.f;
#pragma unroll
  for (int i = 0; i < 5; ++i) {
    const size_t o = (size_t)(b * 16 + t) * DM + (5 * w + i) * 32 + 8 * g;
    u32x4 xv = *(const u32x4*)(x + o);
    f32x4 lo = {0.f, 0.f, 0.f, 0.f}, hi = lo;
#pragma unroll
    for (int q = 0; q < 4; ++q) { const float* hp = (const float*)(wb + OFF_HP) + (size_t)q * SEQ * DM + o; lo += *(const f32x4*)hp; hi += *(const f32x4*)(hp + 4); }
    u32x2 a = pk4(lo), c = pk4(hi);
    const unsigned at[4] = {a.x, a.y, c.x, c.y};
    u32x4 hv;
#pragma unroll
    for (int k = 0; k < 4; ++k) { hv[k] = pk(bflo(xv[k]) + bflo(at[k]), bfhi(xv[k]) + bfhi(at[k])); v[i][2 * k] = bflo(hv[k]); v[i][2 * k + 1] = bfhi(hv[k]); s += v[i][2 * k] + v[i][2 * k + 1]; }
    *(u32x4*)((unsigned short*)(wb + OFF_H) + o) = hv;
  }
  s = sum16(s); s = sum32(s, s);
  if (g == 0) red[0][w][t] = s;
  __syncthreads();
  const float mean = (red[0][0][t] + red[0][1][t] + red[0][2][t] + red[0][3][t]) * (1.f / DM);
  float q = 0.f;
#pragma unroll
  for (int i = 0; i < 5; ++i) for (int k = 0; k < 8; ++k) { const float a = v[i][k] - mean; q += a * a; }
  q = sum16(q); q = sum32(q, q);
  if (g == 0) red[1][w][t] = q;
  __syncthreads();
  const float rstd = 1.f / sqrtf((red[1][0][t] + red[1][1][t] + red[1][2][t] + red[1][3][t]) * (1.f / DM) + 1e-5f);
#pragma unroll
  for (int i = 0; i < 5; ++i) {
    u32x4 o; for (int k = 0; k < 4; ++k) o[k] = pk((v[i][2 * k] - mean) * rstd, (v[i][2 * k + 1] - mean) * rstd);
    *(u32x4*)(wb + OFF_HN + ((size_t)(b * 20 + 5 * w + i) * 64 + lane) * 16) = o;
  }
}
__global__ void __launch_bounds__(256) k_ffn1(char* wb) {
  extern __shared__ __attribute__((aligned(16))) char smem[];
  const int b = blockIdx.x, mg = b % 14, nb = b / 14;
  fgemm<4, 4, 4>((LAS char*)smem, wb + OFF_W1F + (size_t)mg * 8 * 20 * 1024, 20, wb + OFF_HN + (size_t)nb * 8 * 20 * 1024, 20, 20,
    [&](f32x4 (&acc)[4][4], int wm, int wn, int lane) {
#pragma unroll
      for (int m = 0; m < 2; ++m)
#pragma unroll
        for (int n = 0; n < 4; ++n) {
          f32x4 a = acc[2 * m][n], c = acc[2 * m + 1][n];
          for (int r = 0; r < 4; ++r) { a[r] = fmaxf(a[r], 0.f); c[r] = fmaxf(c[r], 0.f); }
          u32x2 lo = pk4(a), hi = pk4(c);
          *(u32x4*)(wb + OFF_U + ((size_t)(nb * 8 + wn * 4 + n) * 56 + mg * 4 + wm * 2 + m) * 1024 + lane * 16) = (u32x4){lo.x, lo.y, hi.x, hi.y};
        }
    });
}
__global__ void __launch_bounds__(256) k_ffn2(char* wb, unsigned short* out) {
  extern __shared__ __attribute__((aligned(16))) char smem[];
  const int b = blockIdx.x, mq = b & 3, nb = b >> 2;
  fgemm<5, 1, 6>((LAS char*)smem, wb + OFF_W2F + (size_t)mq * 10 * 54 * 1024, 54, wb + OFF_U + (size_t)nb * 2 * 56 * 1024, 56, 54,
    [&](f32x4 (&acc)[5][1], int wm, int wn, int lane) {
#pragma unroll
      for (int m = 0; m < 5; ++m) {
        const size_t o = (size_t)(nb * 32 + wn * 16 + (lane & 15)) * DM + mq * 160 + (wm * 5 + m) * 16 + (lane >> 4) * 4;
        const u32x2 hv = *(const u32x2*)((const unsigned short*)(wb + OFF_H) + o);
        const u32x2 f = pk4(acc[m][0]);
        u32x2 r; r.x = pk(bflo(hv.x) + bflo(f.x), bfhi(hv.x) + bfhi(f.x)); r.y = pk(bflo(hv.y) + bflo(f.y), bfhi(hv.y) + bfhi(f.y));
        *(u32x2*)(out + o) = r;
      }
    });
}

#define REC_D1 4
#define REC_D2 20
extern "C" void kernel_launch(void* const* d_in, const int* in_sizes, int n_in, void* d_out, int out_size, void* d_ws, size_t ws_size,
                              hipStream_t stream) {
  const unsigned short* x = (const unsigned short*)d_in[0];
  const unsigned short* mem0 = (const unsigned short*)d_in[1];
  const unsigned short* Wq = (const unsigned short*)d_in[2];
  const unsigned short* Wk = (const unsigned short*)d_in[3];
  const unsigned short* Wv = (const unsigned short*)d_in[4];
  const unsigned short* Wo = (const unsigned short*)d_in[5];
  const unsigned short* w1 = (const unsigned short*)d_in[8];
  const unsigned short* w2 = (const unsigned short*)d_in[9];
  char* wb = (char*)(((uintptr_t)d_ws + PAGE - 1) & ~(uintptr_t)(PAGE - 1));
  (void)hipFuncSetAttribute((const void*)k_qkv, hipFuncAttributeMaxDynamicSharedMemorySize, 120 * 1024);
  (void)hipFuncSetAttribute((const void*)k_rec, hipFuncAttributeMaxDynamicSharedMemorySize, 144 * 1024);
  (void)hipFuncSetAttribute((const void*)k_wo, hipFuncAttributeMaxDynamicSharedMemorySize, 80 * 1024);
  (void)hipFuncSetAttribute((const void*)k_ffn1, hipFuncAttributeMaxDynamicSharedMemorySize, 64 * 1024);
  (void)hipFuncSetAttribute((const void*)k_ffn2, hipFuncAttributeMaxDynamicSharedMemorySize, 72 * 1024);
  ProArgs pa = {x, mem0, Wo, w1, w2, wb};
  k_pro<<<232 + 792, 256, 0, stream>>>(pa);
  QkvArgs qa = {Wq, Wk, Wv, wb};
  k_qkv<<<256, 256, 120 * 1024, stream>>>(qa);
  RecArgs ra = {Wq, Wk, Wv, Wo, wb, REC_D1, REC_D2, 0, 0};
  k_rec<<<256, 256, 144 * 1024, stream>>>(ra);
  k_attn<<<512, 256, 0, stream>>>(wb);
  k_wo<<<256, 256, 80 * 1024, stream>>>(wb);
  k_ln2<<<128, 256, 0, stream>>>(x, wb);
  k_ffn1<<<224, 256, 64 * 1024, stream>>>(wb);
  k_ffn2<<<256, 256, 72 * 1024, stream>>>(wb, (unsigned short*)d_out);
}
```

```cpp
#include <hip/hip_runtime.h>
#include <hip/hip_bf16.h>
#include <stdint.h>
#include <utility>

typedef __bf16 bf16x2 __attribute__((ext_vector_type(2)));
typedef __bf16 bf16x8 __attribute__((ext_vector_type(8)));
typedef float f32x2 __attribute__((ext_vector_type(2)));
typedef float f32x4 __attribute__((ext_vector_type(4)));
typedef short s16x4 __attribute__((ext_vector_type(4)));
typedef unsigned u32x2 __attribute__((ext_vector_type(2)));
typedef unsigned u32x4 __attribute__((ext_vector_type(4)));
typedef unsigned short us16;
#define LAS __attribute__((address_space(3)))
#define DEVI __device__ __forceinline__

constexpr int SEQ = 2048, DM = 640, NSEG = 64, DFF = 1728;
constexpr unsigned MB = 1u << 20;
constexpr unsigned OFF_FLAG = 6 * MB, OFF_XN = 7 * MB, OFF_KM = 10 * MB, OFF_Q = 14 * MB, OFF_K = 30 * MB, OFF_PT = 46 * MB, OFF_H = 51 * MB,
                   OFF_U2 = 54 * MB, OFF_ZM = 62 * MB, OFF_UW = 82 * MB, OFF_Z = 108 * MB, OFF_M = 192 * MB, OFF_P = 200 * MB, OFF_DBG = 220 * MB;
constexpr unsigned F_QK = 0, F_VZ = 16384, F_A = 32768, F_B = 49152, F_T1 = 90112, F_F1 = 94208, F_H = 102400, F_SEL = 112640, F_END = 114176;
constexpr unsigned OFF_ST = OFF_FLAG + MB / 2;
constexpr unsigned PAGE = 2u << 20;
constexpr unsigned POISON = 0xFFFFFFFFu, OOB = 0x80000000u;
constexpr int SC1 = 16;
constexpr int LDS_BYTES = 155648;
constexpr int SPIN_MAX = 1 << 17;

DEVI char* page_up(char* q) { return (char*)(((uintptr_t)q + PAGE - 1) & ~(uintptr_t)(PAGE - 1)); }
DEVI unsigned pk(float lo, float hi) { f32x2 v = {lo, hi}; return __builtin_bit_cast(unsigned, __builtin_convertvector(v, bf16x2)); }
DEVI u32x2 pk4(f32x4 v) { u32x2 r; r.x = pk(v[0], v[1]); r.y = pk(v[2], v[3]); return r; }
DEVI u32x4 pk8(f32x4 a, f32x4 b) { u32x2 x = pk4(a), y = pk4(b); return (u32x4){x.x, x.y, y.x, y.y}; }
DEVI float bflo(unsigned u) { return __uint_as_float(u << 16); }
DEVI float bfhi(unsigned u) { return __uint_as_float(u & 0xffff0000u); }
DEVI f32x4 mma32(u32x4 a, u32x4 b, f32x4 c) { return __builtin_amdgcn_mfma_f32_16x16x32_bf16(__builtin_bit_cast(bf16x8, a), __builtin_bit_cast(bf16x8, b), c, 0, 0, 0); }
DEVI u32x2 trd(LAS char* p) { return __builtin_bit_cast(u32x2, __builtin_amdgcn_ds_read_tr16_b64_v4i16((LAS s16x4*)p)); }
DEVI float sum32(float a) { auto r = __builtin_amdgcn_permlane32_swap(__float_as_uint(a), __float_as_uint(a), false, false); return __uint_as_float(r[0]) + __uint_as_float(r[1]); }
DEVI float sum16(float a) { auto r = __builtin_amdgcn_permlane16_swap(__float_as_uint(a), __float_as_uint(a), false, false); return __uint_as_float(r[0]) + __uint_as_float(r[1]); }
DEVI float max32(float a) { auto r = __builtin_amdgcn_permlane32_swap(__float_as_uint(a), __float_as_uint(a), false, false); return fmaxf(__uint_as_float(r[0]), __uint_as_float(r[1])); }
DEVI float max16(float a) { auto r = __builtin_amdgcn_permlane16_swap(__float_as_uint(a), __float_as_uint(a), false, false); return fmaxf(__uint_as_float(r[0]), __uint_as_float(r[1])); }
DEVI void glds(const void* g, unsigned ldsaddr) {
  unsigned sv;
  asm volatile("s_mov_b32 %0, m0\n\ts_mov_b32 m0, %2\n\ts_nop 0\n\tglobal_load_lds_dwordx4 %1, off\n\ts_mov_b32 m0, %0" : "=&s"(sv) : "v"(g), "s"(ldsaddr) : "memory");
}
DEVI void glds_sc1(const void* g, unsigned ldsaddr) {
  unsigned sv;
  asm volatile("s_mov_b32 %0, m0\n\ts_mov_b32 m0, %2\n\ts_nop 0\n\tglobal_load_lds_dwordx4 %1, off sc1\n\ts_mov_b32 m0, %0" : "=&s"(sv) : "v"(g), "s"(ldsaddr) : "memory");
}
template <int N> DEVI void wait_vm() { asm volatile("s_waitcnt vmcnt(%0)" :: "n"(N) : "memory"); }
DEVI void bar() { asm volatile("s_waitcnt lgkmcnt(0)\n\ts_barrier" ::: "memory"); }
DEVI void fence() { asm volatile("" ::: "memory"); __builtin_amdgcn_sched_barrier(0); }
DEVI void delay(int n) {
  int cnt;
  asm volatile("s_mov_b32 %0, %1\n 1: s_nop 4\n s_sub_u32 %0, %0, 1\n s_cmp_lg_u32 %0, 0\n s_cbranch_scc1 1b" : "=&s"(cnt) : "s"(n) : "scc", "memory");
}
DEVI int lane_id() { int l; asm volatile("v_mbcnt_lo_u32_b32 %0, -1, 0\n\tv_mbcnt_hi_u32_b32 %0, -1, %0" : "=v"(l)); return l; }
typedef __amdgpu_buffer_rsrc_t rsrc_t;
DEVI u32x4 ld16(rsrc_t r, unsigned vo, unsigned so) { return __builtin_amdgcn_raw_buffer_load_b128(r, vo, so, SC1); }
DEVI void st16(u32x4 v, rsrc_t r, unsigned vo, unsigned so) { __builtin_amdgcn_raw_buffer_store_b128(v, r, vo + so, 0, SC1); }
DEVI void poll(rsrc_t r, unsigned vo, unsigned need, int& budget) {
  for (;;) {
    asm volatile("" ::: "memory");
    const unsigned v = __builtin_amdgcn_raw_buffer_load_b32(r, vo, OFF_FLAG, SC1);
    if (__all(v >= need) || budget <= 0) break;
    --budget;
    __builtin_amdgcn_s_sleep(2);
  }
  asm volatile("" ::: "memory");
}
DEVI void pollc(rsrc_t r, unsigned vo, unsigned need, unsigned& seen, int& budget) {
  asm volatile("" ::: "memory");
  while (!__all(seen >= need) && budget > 0) {
    seen = __builtin_amdgcn_raw_buffer_load_b32(r, vo, OFF_FLAG, SC1);
    --budget;
    if (!__all(seen >= need)) __builtin_amdgcn_s_sleep(2);
    asm volatile("" ::: "memory");
  }
}
DEVI void publish(rsrc_t r, unsigned vo, unsigned val) {
  wait_vm<0>();
  __builtin_amdgcn_raw_buffer_store_b32(val, r, vo, OFF_FLAG, SC1);
}

#define ASM_QK \
  "s_nop 4\n" \
  "s_mov_b64 s[40:41], %[ws]\n" \
  "s_mov_b32 s42, 0x0ff00000\n" \
  "s_mov_b32 s43, 0x20000\n" \
  "s_lshl_b32 s54, %[h], 8\n" \
  "s_lshl_b32 s55, %[w], 6\n" \
  "s_add_u32 s54, s54, s55\n" \
  "s_mov_b64 s[44:45], %[wa]\n" \
  "s_add_u32 s44, s44, s54\n" \
  "s_addc_u32 s45, s45, 0\n" \
  "s_mov_b32 s46, 0x00500000\n" \
  "s_mov_b32 s47, 0x20000\n" \
  "s_mov_b64 s[48:49], %[wb]\n" \
  "s_add_u32 s48, s48, s54\n" \
  "s_addc_u32 s49, s49, 0\n" \
  "s_mov_b32 s50, 0x00500000\n" \
  "s_mov_b32 s51, 0x20000\n" \
  "s_sub_u32 s46, s46, s54\n" \
  "s_sub_u32 s50, s50, s54\n" \
  "s_mul_i32 s52, %[w], 28672\n" \
  "s_add_u32 s52, s52, 40960\n" \
  "s_mul_i32 s56, %[w], 10240\n" \
  "s_add_u32 s57, s56, 7340032\n" \
  "s_mov_b32 s53, 0\n" \
  "s_add_u32 m0, s52, 0\n" \
  "s_nop 0\n" \
  "buffer_load_dwordx4 %[wo], s[44:47], s53 offen lds\n" \
  "s_mov_b32 s53, 131072\n" \
  "s_add_u32 m0, s52, 1024\n" \
  "s_nop 0\n" \
  "buffer_load_dwordx4 %[wo], s[44:47], s53 offen lds\n" \
  "s_mov_b32 s53, 0\n" \
  "s_add_u32 m0, s52, 2048\n" \
  "s_nop 0\n" \
  "buffer_load_dwordx4 %[wo], s[48:51], s53 offen lds\n" \
  "s_mov_b32 s53, 131072\n" \
  "s_add_u32 m0, s52, 3072\n" \
  "s_nop 0\n" \
  "buffer_load_dwordx4 %[wo], s[48:51], s53 offen lds\n" \
  "s_mov_b32 s53, 262144\n" \
  "s_add_u32 m0, s52, 4096\n" \
  "s_nop 0\n" \
  "buffer_load_dwordx4 %[wo], s[44:47], s53 offen lds\n" \
  "s_mov_b32 s53, 393216\n" \
  "s_add_u32 m0, s52, 5120\n" \
  "s_nop 0\n" \
  "buffer_load_dwordx4 %[wo], s[44:47], s53 offen lds\n" \
  "s_mov_b32 s53, 262144\n" \
  "s_add_u32 m0, s52, 6144\n" \
  "s_nop 0\n" \
  "buffer_load_dwordx4 %[wo], s[48:51], s53 offen lds\n" \
  "s_mov_b32 s53, 393216\n" \
  "s_add_u32 m0, s52, 7168\n" \
  "s_nop 0\n" \
  "buffer_load_dwordx4 %[wo], s[48:51], s53 offen lds\n" \
  "s_mov_b32 s53, 524288\n" \
  "s_add_u32 m0, s52, 8192\n" \
  "s_nop 0\n" \
  "buffer_load_dwordx4 %[wo], s[44:47], s53 offen lds\n" \
  "s_mov_b32 s53, 655360\n" \
  "s_add_u32 m0, s52, 9216\n" \
  "s_nop 0\n" \
  "buffer_load_dwordx4 %[wo], s[44:47], s53 offen lds\n" \
  "s_mov_b32 s53, 524288\n" \
  "s_add_u32 m0, s52, 10240\n" \
  "s_nop 0\n" \
  "buffer_load_dwordx4 %[wo], s[48:51], s53 offen lds\n" \
  "s_mov_b32 s53, 655360\n" \
  "s_add_u32 m0, s52, 11264\n" \
  "s_nop 0\n" \
  "buffer_load_dwordx4 %[wo], s[48:51], s53 offen lds\n" \
  "s_mov_b32 s53, 786432\n" \
  "s_add_u32 m0, s52, 12288\n" \
  "s_nop 0\n" \
  "buffer_load_dwordx4 %[wo], s[44:47], s53 offen lds\n" \
  "s_mov_b32 s53, 917504\n" \
  "s_add_u32 m0, s52, 13312\n" \
  "s_nop 0\n" \
  "buffer_load_dwordx4 %[wo], s[44:47], s53 offen lds\n" \
  "s_mov_b32 s53, 786432\n" \
  "s_add_u32 m0, s52, 14336\n" \
  "s_nop 0\n" \
  "buffer_load_dwordx4 %[wo], s[48:51], s53 offen lds\n" \
  "s_mov_b32 s53, 917504\n" \
  "s_add_u32 m0, s52, 15360\n" \
  "s_nop 0\n" \
  "buffer_load_dwordx4 %[wo], s[48:51], s53 offen lds\n" \
  "s_mov_b32 s53, 1048576\n" \
  "s_add_u32 m0, s52, 16384\n" \
  "s_nop 0\n" \
  "buffer_load_dwordx4 %[wo], s[44:47], s53 offen lds\n" \
  "s_mov_b32 s53, 1179648\n" \
  "s_add_u32 m0, s52, 17408\n" \
  "s_nop 0\n" \
  "buffer_load_dwordx4 %[wo], s[44:47], s53 offen lds\n" \
  "s_mov_b32 s53, 1048576\n" \
  "s_add_u32 m0, s52, 18432\n" \
  "s_nop 0\n" \
  "buffer_load_dwordx4 %[wo], s[48:51], s53 offen lds\n" \
  "s_mov_b32 s53, 1179648\n" \
  "s_add_u32 m0, s52, 19456\n" \
  "s_nop 0\n" \
  "buffer_load_dwordx4 %[wo], s[48:51], s53 offen lds\n" \
  "s_mov_b32 s53, 1310720\n" \
  "s_add_u32 m0, s52, 20480\n" \
  "s_nop 0\n" \
  "buffer_load_dwordx4 %[wo], s[44:47], s53 offen lds\n" \
  "s_mov_b32 s53, 1441792\n" \
  "s_add_u32 m0, s52, 21504\n" \
  "s_nop 0\n" \
  "buffer_load_dwordx4 %[wo], s[44:47], s53 offen lds\n" \
  "s_mov_b32 s53, 1310720\n" \
  "s_add_u32 m0, s52, 22528\n" \
  "s_nop 0\n" \
  "buffer_load_dwordx4 %[wo], s[48:51], s53 offen lds\n" \
  "s_mov_b32 s53, 1441792\n" \
  "s_add_u32 m0, s52, 23552\n" \
  "s_nop 0\n" \
  "buffer_load_dwordx4 %[wo], s[48:51], s53 offen lds\n" \
  "s_add_u32 m0, s56, 0\n" \
  "s_add_u32 s53, s57, 0\n" \
  "buffer_load_dwordx4 %[l16], s[40:43], s53 offen offset:0 lds\n" \
  "buffer_load_dwordx4 %[l16], s[40:43], s53 offen offset:1024 lds\n" \
  "buffer_load_dwordx4 %[l16], s[40:43], s53 offen offset:2048 lds\n" \
  "buffer_load_dwordx4 %[l16], s[40:43], s53 offen offset:3072 lds\n" \
  "s_add_u32 m0, s56, 4096\n" \
  "s_add_u32 s53, s57, 4096\n" \
  "buffer_load_dwordx4 %[l16], s[40:43], s53 offen offset:0 lds\n" \
  "buffer_load_dwordx4 %[l16], s[40:43], s53 offen offset:1024 lds\n" \
  "buffer_load_dwordx4 %[l16], s[40:43], s53 offen offset:2048 lds\n" \
  "buffer_load_dwordx4 %[l16], s[40:43], s53 offen offset:3072 lds\n" \
  "s_add_u32 m0, s56, 8192\n" \
  "s_add_u32 s53, s57, 8192\n" \
  "buffer_load_dwordx4 %[l16], s[40:43], s53 offen offset:0 lds\n" \
  "buffer_load_dwordx4 %[l16], s[40:43], s53 offen offset:1024 lds\n" \
  "s_waitcnt vmcnt(30)\n" \
  "ds_read_b64_tr_b16 a[0:1], %[tr] offset:0\n" \
  "ds_read_b64_tr_b16 a[2:3], %[tr] offset:256\n" \
  "ds_read_b64_tr_b16 a[4:5], %[tr] offset:32\n" \
  "ds_read_b64_tr_b16 a[6:7], %[tr] offset:288\n" \
  "ds_read_b64_tr_b16 a[8:9], %[tr] offset:2048\n" \
  "ds_read_b64_tr_b16 a[10:11], %[tr] offset:2304\n" \
  "ds_read_b64_tr_b16 a[12:13], %[tr] offset:2080\n" \
  "ds_read_b64_tr_b16 a[14:15], %[tr] offset:2336\n" \
  "s_mov_b32 s53, 1572864\n" \
  "s_add_u32 m0, s52, 24576\n" \
  "s_nop 0\n" \
  "buffer_load_dwordx4 %[wo], s[44:47], s53 offen lds\n" \
  "s_mov_b32 s53, 1703936\n" \
  "s_add_u32 m0, s52, 25600\n" \
  "s_nop 0\n" \
  "buffer_load_dwordx4 %[wo], s[44:47], s53 offen lds\n" \
  "s_mov_b32 s53, 1572864\n" \
  "s_add_u32 m0, s52, 26624\n" \
  "s_nop 0\n" \
  "buffer_load_dwordx4 %[wo], s[48:51], s53 offen lds\n" \
  "s_mov_b32 s53, 1703936\n" \
  "s_add_u32 m0, s52, 27648\n" \
  "s_nop 0\n" \
  "buffer_load_dwordx4 %[wo], s[48:51], s53 offen lds\n" \
  "s_waitcnt vmcnt(30)\n" \
  "ds_read_b64_tr_b16 a[16:17], %[tr] offset:4096\n" \
  "ds_read_b64_tr_b16 a[18:19], %[tr] offset:4352\n" \
  "ds_read_b64_tr_b16 a[20:21], %[tr] offset:4128\n" \
  "ds_read_b64_tr_b16 a[22:23], %[tr] offset:4384\n" \
  "ds_read_b64_tr_b16 a[24:25], %[tr] offset:6144\n" \
  "ds_read_b64_tr_b16 a[26:27], %[tr] offset:6400\n" \
  "ds_read_b64_tr_b16 a[28:29], %[tr] offset:6176\n" \
  "s_waitcnt lgkmcnt(14)\n" \
  "ds_read_b64_tr_b16 a[30:31], %[tr] offset:6432\n" \
  "s_waitcnt lgkmcnt(8)\n" \
  "s_mov_b32 s53, 1835008\n" \
  "s_add_u32 m0, s52, 0\n" \
  "s_nop 0\n" \
  "buffer_load_dwordx4 %[wo], s[44:47], s53 offen lds\n" \
  "s_mov_b32 s53, 1966080\n" \
  "s_add_u32 m0, s52, 1024\n" \
  "s_nop 0\n" \
  "buffer_load_dwordx4 %[wo], s[44:47], s53 offen lds\n" \
  "s_mov_b32 s53, 1835008\n" \
  "s_add_u32 m0, s52, 2048\n" \
  "s_nop 0\n" \
  "buffer_load_dwordx4 %[wo], s[48:51], s53 offen lds\n" \
  "s_mov_b32 s53, 1966080\n" \
  "s_add_u32 m0, s52, 3072\n" \
  "s_nop 0\n" \
  "buffer_load_dwordx4 %[wo], s[48:51], s53 offen lds\n" \
  "s_waitcnt vmcnt(30)\n" \
  "ds_read_b64_tr_b16 a[32:33], %[tr] offset:8192\n" \
  "ds_read_b64_tr_b16 a[34:35], %[tr] offset:8448\n" \
  "ds_read_b64_tr_b16 a[36:37], %[tr] offset:8224\n" \
  "ds_read_b64_tr_b16 a[38:39], %[tr] offset:8480\n" \
  "ds_read_b64_tr_b16 a[40:41], %[tr] offset:10240\n" \
  "ds_read_b64_tr_b16 a[42:43], %[tr] offset:10496\n" \
  "ds_read_b64_tr_b16 a[44:45], %[tr] offset:10272\n" \
  "s_waitcnt lgkmcnt(14)\n" \
  "ds_read_b64_tr_b16 a[46:47], %[tr] offset:10528\n" \
  "s_waitcnt lgkmcnt(8)\n" \
  "s_mov_b32 s53, 2097152\n" \
  "s_add_u32 m0, s52, 4096\n" \
  "s_nop 0\n" \
  "buffer_load_dwordx4 %[wo], s[44:47], s53 offen lds\n" \
  "s_mov_b32 s53, 2228224\n" \
  "s_add_u32 m0, s52, 5120\n" \
  "s_nop 0\n" \
  "buffer_load_dwordx4 %[wo], s[44:47], s53 offen lds\n" \
  "s_mov_b32 s53, 2097152\n" \
  "s_add_u32 m0, s52, 6144\n" \
  "s_nop 0\n" \
  "buffer_load_dwordx4 %[wo], s[48:51], s53 offen lds\n" \
  "s_mov_b32 s53, 2228224\n" \
  "s_add_u32 m0, s52, 7168\n" \
  "s_nop 0\n" \
  "buffer_load_dwordx4 %[wo], s[48:51], s53 offen lds\n" \
  "s_waitcnt vmcnt(30)\n" \
  "ds_read_b64_tr_b16 a[48:49], %[tr] offset:12288\n" \
  "ds_read_b64_tr_b16 a[50:51], %[tr] offset:12544\n" \
  "ds_read_b64_tr_b16 a[52:53], %[tr] offset:12320\n" \
  "ds_read_b64_tr_b16 a[54:55], %[tr] offset:12576\n" \
  "ds_read_b64_tr_b16 a[56:57], %[tr] offset:14336\n" \
  "ds_read_b64_tr_b16 a[58:59], %[tr] offset:14592\n" \
  "ds_read_b64_tr_b16 a[60:61], %[tr] offset:14368\n" \
  "s_waitcnt lgkmcnt(14)\n" \
  "ds_read_b64_tr_b16 a[62:63], %[tr] offset:14624\n" \
  "s_waitcnt lgkmcnt(8)\n" \
  "s_mov_b32 s53, 2359296\n" \
  "s_add_u32 m0, s52, 8192\n" \
  "s_nop 0\n" \
  "buffer_load_dwordx4 %[wo], s[44:47], s53 offen lds\n" \
  "s_mov_b32 s53, 2490368\n" \
  "s_add_u32 m0, s52, 9216\n" \
  "s_nop 0\n" \
  "buffer_load_dwordx4 %[wo], s[44:47], s53 offen lds\n" \
  "s_mov_b32 s53, 2359296\n" \
  "s_add_u32 m0, s52, 10240\n" \
  "s_nop 0\n" \
  "buffer_load_dwordx4 %[wo], s[48:51], s53 offen lds\n" \
  "s_mov_b32 s53, 2490368\n" \
  "s_add_u32 m0, s52, 11264\n" \
  "s_nop 0\n" \
  "buffer_load_dwordx4 %[wo], s[48:51], s53 offen lds\n" \
  "s_waitcnt vmcnt(30)\n" \
  "ds_read_b64_tr_b16 a[64:65], %[tr] offset:16384\n" \
  "ds_read_b64_tr_b16 a[66:67], %[tr] offset:16640\n" \
  "ds_read_b64_tr_b16 a[68:69], %[tr] offset:16416\n" \
  "ds_read_b64_tr_b16 a[70:71], %[tr] offset:16672\n" \
  "ds_read_b64_tr_b16 a[72:73], %[tr] offset:18432\n" \
  "ds_read_b64_tr_b16 a[74:75], %[tr] offset:18688\n" \
  "ds_read_b64_tr_b16 a[76:77], %[tr] offset:18464\n" \
  "s_waitcnt lgkmcnt(14)\n" \
  "ds_read_b64_tr_b16 a[78:79], %[tr] offset:18720\n" \
  "s_waitcnt lgkmcnt(8)\n" \
  "s_mov_b32 s53, 2621440\n" \
  "s_add_u32 m0, s52, 12288\n" \
  "s_nop 0\n" \
  "buffer_load_dwordx4 %[wo], s[44:47], s53 offen lds\n" \
  "s_mov_b32 s53, 2752512\n" \
  "s_add_u32 m0, s52, 13312\n" \
  "s_nop 0\n" \
  "buffer_load_dwordx4 %[wo], s[44:47], s53 offen lds\n" \
  "s_mov_b32 s53, 2621440\n" \
  "s_add_u32 m0, s52, 14336\n" \
  "s_nop 0\n" \
  "buffer_load_dwordx4 %[wo], s[48:51], s53 offen lds\n" \
  "s_mov_b32 s53, 2752512\n" \
  "s_add_u32 m0, s52, 15360\n" \
  "s_nop 0\n" \
  "buffer_load_dwordx4 %[wo], s[48:51], s53 offen lds\n" \
  "s_waitcnt vmcnt(30)\n" \
  "ds_read_b64_tr_b16 a[80:81], %[tr] offset:20480\n" \
  "ds_read_b64_tr_b16 a[82:83], %[tr] offset:20736\n" \
  "ds_read_b64_tr_b16 a[84:85], %[tr] offset:20512\n" \
  "ds_read_b64_tr_b16 a[86:87], %[tr] offset:20768\n" \
  "ds_read_b64_tr_b16 a[88:89], %[tr] offset:22528\n" \
  "ds_read_b64_tr_b16 a[90:91], %[tr] offset:22784\n" \
  "ds_read_b64_tr_b16 a[92:93], %[tr] offset:22560\n" \
  "s_waitcnt lgkmcnt(14)\n" \
  "ds_read_b64_tr_b16 a[94:95], %[tr] offset:22816\n" \
  "s_waitcnt lgkmcnt(8)\n" \
  "s_mov_b32 s53, 2883584\n" \
  "s_add_u32 m0, s52, 16384\n" \
  "s_nop 0\n" \
  "buffer_load_dwordx4 %[wo], s[44:47], s53 offen lds\n" \
  "s_mov_b32 s53, 3014656\n" \
  "s_add_u32 m0, s52, 17408\n" \
  "s_nop 0\n" \
  "buffer_load_dwordx4 %[wo], s[44:47], s53 offen lds\n" \
  "s_mov_b32 s53, 2883584\n" \
  "s_add_u32 m0, s52, 18432\n" \
  "s_nop 0\n" \
  "buffer_load_dwordx4 %[wo], s[48:51], s53 offen lds\n" \
  "s_mov_b32 s53, 3014656\n" \
  "s_add_u32 m0, s52, 19456\n" \
  "s_nop 0\n" \
  "buffer_load_dwordx4 %[wo], s[48:51], s53 offen lds\n" \
  "s_waitcnt vmcnt(20)\n" \
  "ds_read_b64_tr_b16 a[96:97], %[tr] offset:24576\n" \
  "ds_read_b64_tr_b16 a[98:99], %[tr] offset:24832\n" \
  "ds_read_b64_tr_b16 a[100:101], %[tr] offset:24608\n" \
  "ds_read_b64_tr_b16 a[102:103], %[tr] offset:24864\n" \
  "ds_read_b64_tr_b16 a[104:105], %[tr] offset:26624\n" \
  "ds_read_b64_tr_b16 a[106:107], %[tr] offset:26880\n" \
  "ds_read_b64_tr_b16 a[108:109], %[tr] offset:26656\n" \
  "s_waitcnt lgkmcnt(14)\n" \
  "ds_read_b64_tr_b16 a[110:111], %[tr] offset:26912\n" \
  "s_waitcnt lgkmcnt(8)\n" \
  "s_mov_b32 s53, 3145728\n" \
  "s_add_u32 m0, s52, 20480\n" \
  "s_nop 0\n" \
  "buffer_load_dwordx4 %[wo], s[44:47], s53 offen lds\n" \
  "s_mov_b32 s53, 3276800\n" \
  "s_add_u32 m0, s52, 21504\n" \
  "s_nop 0\n" \
  "buffer_load_dwordx4 %[wo], s[44:47], s53 offen lds\n" \
  "s_mov_b32 s53, 3145728\n" \
  "s_add_u32 m0, s52, 22528\n" \
  "s_nop 0\n" \
  "buffer_load_dwordx4 %[wo], s[48:51], s53 offen lds\n" \
  "s_mov_b32 s53, 3276800\n" \
  "s_add_u32 m0, s52, 23552\n" \
  "s_nop 0\n" \
  "buffer_load_dwordx4 %[wo], s[48:51], s53 offen lds\n" \
  "s_waitcnt vmcnt(20)\n" \
  "ds_read_b64_tr_b16 a[112:113], %[tr] offset:0\n" \
  "ds_read_b64_tr_b16 a[114:115], %[tr] offset:256\n" \
  "ds_read_b64_tr_b16 a[116:117], %[tr] offset:32\n" \
  "ds_read_b64_tr_b16 a[118:119], %[tr] offset:288\n" \
  "ds_read_b64_tr_b16 a[120:121], %[tr] offset:2048\n" \
  "ds_read_b64_tr_b16 a[122:123], %[tr] offset:2304\n" \
  "ds_read_b64_tr_b16 a[124:125], %[tr] offset:2080\n" \
  "s_waitcnt lgkmcnt(14)\n" \
  "ds_read_b64_tr_b16 a[126:127], %[tr] offset:2336\n" \
  "s_waitcnt lgkmcnt(8)\n" \
  "s_mov_b32 s53, 3407872\n" \
  "s_add_u32 m0, s52, 24576\n" \
  "s_nop 0\n" \
  "buffer_load_dwordx4 %[wo], s[44:47], s53 offen lds\n" \
  "s_mov_b32 s53, 3538944\n" \
  "s_add_u32 m0, s52, 25600\n" \
  "s_nop 0\n" \
  "buffer_load_dwordx4 %[wo], s[44:47], s53 offen lds\n" \
  "s_mov_b32 s53, 3407872\n" \
  "s_add_u32 m0, s52, 26624\n" \
  "s_nop 0\n" \
  "buffer_load_dwordx4 %[wo], s[48:51], s53 offen lds\n" \
  "s_mov_b32 s53, 3538944\n" \
  "s_add_u32 m0, s52, 27648\n" \
  "s_nop 0\n" \
  "buffer_load_dwordx4 %[wo], s[48:51], s53 offen lds\n" \
  "s_waitcnt vmcnt(20)\n" \
  "ds_read_b64_tr_b16 a[128:129], %[tr] offset:4096\n" \
  "ds_read_b64_tr_b16 a[130:131], %[tr] offset:4352\n" \
  "ds_read_b64_tr_b16 a[132:133], %[tr] offset:4128\n" \
  "ds_read_b64_tr_b16 a[134:135], %[tr] offset:4384\n" \
  "ds_read_b64_tr_b16 a[136:137], %[tr] offset:6144\n" \
  "ds_read_b64_tr_b16 a[138:139], %[tr] offset:6400\n" \
  "ds_read_b64_tr_b16 a[140:141], %[tr] offset:6176\n" \
  "s_waitcnt lgkmcnt(14)\n" \
  "ds_read_b64_tr_b16 a[142:143], %[tr] offset:6432\n" \
  "s_waitcnt lgkmcnt(8)\n" \
  "s_mov_b32 s53, 3670016\n" \
  "s_add_u32 m0, s52, 0\n" \
  "s_nop 0\n" \
  "buffer_load_dwordx4 %[wo], s[44:47], s53 offen lds\n" \
  "s_mov_b32 s53, 3801088\n" \
  "s_add_u32 m0, s52, 1024\n" \
  "s_nop 0\n" \
  "buffer_load_dwordx4 %[wo], s[44:47], s53 offen lds\n" \
  "s_mov_b32 s53, 3670016\n" \
  "s_add_u32 m0, s52, 2048\n" \
  "s_nop 0\n" \
  "buffer_load_dwordx4 %[wo], s[48:51], s53 offen lds\n" \
  "s_mov_b32 s53, 3801088\n" \
  "s_add_u32 m0, s52, 3072\n" \
  "s_nop 0\n" \
  "buffer_load_dwordx4 %[wo], s[48:51], s53 offen lds\n" \
  "s_waitcnt vmcnt(20)\n" \
  "ds_read_b64_tr_b16 a[144:145], %[tr] offset:8192\n" \
  "ds_read_b64_tr_b16 a[146:147], %[tr] offset:8448\n" \
  "ds_read_b64_tr_b16 a[148:149], %[tr] offset:8224\n" \
  "ds_read_b64_tr_b16 a[150:151], %[tr] offset:8480\n" \
  "ds_read_b64_tr_b16 a[152:153], %[tr] offset:10240\n" \
  "ds_read_b64_tr_b16 a[154:155], %[tr] offset:10496\n" \
  "ds_read_b64_tr_b16 a[156:157], %[tr] offset:10272\n" \
  "s_waitcnt lgkmcnt(14)\n" \
  "ds_read_b64_tr_b16 a[158:159], %[tr] offset:10528\n" \
  "s_waitcnt lgkmcnt(8)\n" \
  "s_mov_b32 s53, 3932160\n" \
  "s_add_u32 m0, s52, 4096\n" \
  "s_nop 0\n" \
  "buffer_load_dwordx4 %[wo], s[44:47], s53 offen lds\n" \
  "s_mov_b32 s53, 4063232\n" \
  "s_add_u32 m0, s52, 5120\n" \
  "s_nop 0\n" \
  "buffer_load_dwordx4 %[wo], s[44:47], s53 offen lds\n" \
  "s_mov_b32 s53, 3932160\n" \
  "s_add_u32 m0, s52, 6144\n" \
  "s_nop 0\n" \
  "buffer_load_dwordx4 %[wo], s[48:51], s53 offen lds\n" \
  "s_mov_b32 s53, 4063232\n" \
  "s_add_u32 m0, s52, 7168\n" \
  "s_nop 0\n" \
  "buffer_load_dwordx4 %[wo], s[48:51], s53 offen lds\n" \
  "s_waitcnt vmcnt(20)\n" \
  "ds_read_b64_tr_b16 a[160:161], %[tr] offset:12288\n" \
  "ds_read_b64_tr_b16 a[162:163], %[tr] offset:12544\n" \
  "ds_read_b64_tr_b16 a[164:165], %[tr] offset:12320\n" \
  "ds_read_b64_tr_b16 a[166:167], %[tr] offset:12576\n" \
  "ds_read_b64_tr_b16 a[168:169], %[tr] offset:14336\n" \
  "ds_read_b64_tr_b16 a[170:171], %[tr] offset:14592\n" \
  "ds_read_b64_tr_b16 a[172:173], %[tr] offset:14368\n" \
  "s_waitcnt lgkmcnt(14)\n" \
  "ds_read_b64_tr_b16 a[174:175], %[tr] offset:14624\n" \
  "s_waitcnt lgkmcnt(8)\n" \
  "s_mov_b32 s53, 4194304\n" \
  "s_add_u32 m0, s52, 8192\n" \
  "s_nop 0\n" \
  "buffer_load_dwordx4 %[wo], s[44:47], s53 offen lds\n" \
  "s_mov_b32 s53, 4325376\n" \
  "s_add_u32 m0, s52, 9216\n" \
  "s_nop 0\n" \
  "buffer_load_dwordx4 %[wo], s[44:47], s53 offen lds\n" \
  "s_mov_b32 s53, 4194304\n" \
  "s_add_u32 m0, s52, 10240\n" \
  "s_nop 0\n" \
  "buffer_load_dwordx4 %[wo], s[48:51], s53 offen lds\n" \
  "s_mov_b32 s53, 4325376\n" \
  "s_add_u32 m0, s52, 11264\n" \
  "s_nop 0\n" \
  "buffer_load_dwordx4 %[wo], s[48:51], s53 offen lds\n" \
  "s_waitcnt vmcnt(20)\n" \
  "ds_read_b64_tr_b16 a[176:177], %[tr] offset:16384\n" \
  "ds_read_b64_tr_b16 a[178:179], %[tr] offset:16640\n" \
  "ds_read_b64_tr_b16 a[180:181], %[tr] offset:16416\n" \
  "ds_read_b64_tr_b16 a[182:183], %[tr] offset:16672\n" \
  "ds_read_b64_tr_b16 a[184:185], %[tr] offset:18432\n" \
  "ds_read_b64_tr_b16 a[186:187], %[tr] offset:18688\n" \
  "ds_read_b64_tr_b16 a[188:189], %[tr] offset:18464\n" \
  "s_waitcnt lgkmcnt(14)\n" \
  "ds_read_b64_tr_b16 a[190:191], %[tr] offset:18720\n" \
  "s_waitcnt lgkmcnt(8)\n" \
  "s_mov_b32 s53, 4456448\n" \
  "s_add_u32 m0, s52, 12288\n" \
  "s_nop 0\n" \
  "buffer_load_dwordx4 %[wo], s[44:47], s53 offen lds\n" \
  "s_mov_b32 s53, 4587520\n" \
  "s_add_u32 m0, s52, 13312\n" \
  "s_nop 0\n" \
  "buffer_load_dwordx4 %[wo], s[44:47], s53 offen lds\n" \
  "s_mov_b32 s53, 4456448\n" \
  "s_add_u32 m0, s52, 14336\n" \
  "s_nop 0\n" \
  "buffer_load_dwordx4 %[wo], s[48:51], s53 offen lds\n" \
  "s_mov_b32 s53, 4587520\n" \
  "s_add_u32 m0, s52, 15360\n" \
  "s_nop 0\n" \
  "buffer_load_dwordx4 %[wo], s[48:51], s53 offen lds\n" \
  "s_waitcnt vmcnt(20)\n" \
  "ds_read_b64_tr_b16 a[192:193], %[tr] offset:20480\n" \
  "ds_read_b64_tr_b16 a[194:195], %[tr] offset:20736\n" \
  "ds_read_b64_tr_b16 a[196:197], %[tr] offset:20512\n" \
  "ds_read_b64_tr_b16 a[198:199], %[tr] offset:20768\n" \
  "ds_read_b64_tr_b16 a[200:201], %[tr] offset:22528\n" \
  "ds_read_b64_tr_b16 a[202:203], %[tr] offset:22784\n" \
  "ds_read_b64_tr_b16 a[204:205], %[tr] offset:22560\n" \
  "s_waitcnt lgkmcnt(14)\n" \
  "ds_read_b64_tr_b16 a[206:207], %[tr] offset:22816\n" \
  "s_waitcnt lgkmcnt(8)\n" \
  "s_mov_b32 s53, 4718592\n" \
  "s_add_u32 m0, s52, 16384\n" \
  "s_nop 0\n" \
  "buffer_load_dwordx4 %[wo], s[44:47], s53 offen lds\n" \
  "s_mov_b32 s53, 4849664\n" \
  "s_add_u32 m0, s52, 17408\n" \
  "s_nop 0\n" \
  "buffer_load_dwordx4 %[wo], s[44:47], s53 offen lds\n" \
  "s_mov_b32 s53, 4718592\n" \
  "s_add_u32 m0, s52, 18432\n" \
  "s_nop 0\n" \
  "buffer_load_dwordx4 %[wo], s[48:51], s53 offen lds\n" \
  "s_mov_b32 s53, 4849664\n" \
  "s_add_u32 m0, s52, 19456\n" \
  "s_nop 0\n" \
  "buffer_load_dwordx4 %[wo], s[48:51], s53 offen lds\n" \
  "s_waitcnt vmcnt(20)\n" \
  "ds_read_b64_tr_b16 a[208:209], %[tr] offset:24576\n" \
  "ds_read_b64_tr_b16 a[210:211], %[tr] offset:24832\n" \
  "ds_read_b64_tr_b16 a[212:213], %[tr] offset:24608\n" \
  "ds_read_b64_tr_b16 a[214:215], %[tr] offset:24864\n" \
  "ds_read_b64_tr_b16 a[216:217], %[tr] offset:26624\n" \
  "ds_read_b64_tr_b16 a[218:219], %[tr] offset:26880\n" \
  "ds_read_b64_tr_b16 a[220:221], %[tr] offset:26656\n" \
  "s_waitcnt lgkmcnt(14)\n" \
  "ds_read_b64_tr_b16 a[222:223], %[tr] offset:26912\n" \
  "s_waitcnt lgkmcnt(8)\n" \
  "s_mov_b32 s53, 4980736\n" \
  "s_add_u32 m0, s52, 20480\n" \
  "s_nop 0\n" \
  "buffer_load_dwordx4 %[wo], s[44:47], s53 offen lds\n" \
  "s_mov_b32 s53, 5111808\n" \
  "s_add_u32 m0, s52, 21504\n" \
  "s_nop 0\n" \
  "buffer_load_dwordx4 %[wo], s[44:47], s53 offen lds\n" \
  "s_mov_b32 s53, 4980736\n" \
  "s_add_u32 m0, s52, 22528\n" \
  "s_nop 0\n" \
  "buffer_load_dwordx4 %[wo], s[48:51], s53 offen lds\n" \
  "s_mov_b32 s53, 5111808\n" \
  "s_add_u32 m0, s52, 23552\n" \
  "s_nop 0\n" \
  "buffer_load_dwordx4 %[wo], s[48:51], s53 offen lds\n" \
  "s_waitcnt vmcnt(20)\n" \
  "ds_read_b64_tr_b16 a[224:225], %[tr] offset:0\n" \
  "ds_read_b64_tr_b16 a[226:227], %[tr] offset:256\n" \
  "ds_read_b64_tr_b16 a[228:229], %[tr] offset:32\n" \
  "ds_read_b64_tr_b16 a[230:231], %[tr] offset:288\n" \
  "ds_read_b64_tr_b16 a[232:233], %[tr] offset:2048\n" \
  "ds_read_b64_tr_b16 a[234:235], %[tr] offset:2304\n" \
  "ds_read_b64_tr_b16 a[236:237], %[tr] offset:2080\n" \
  "s_waitcnt lgkmcnt(14)\n" \
  "ds_read_b64_tr_b16 a[238:239], %[tr] offset:2336\n" \
  "s_waitcnt vmcnt(16)\n" \
  "s_waitcnt lgkmcnt(14)\n" \
  "ds_read_b64_tr_b16 a[240:241], %[tr] offset:4096\n" \
  "s_waitcnt lgkmcnt(14)\n" \
  "ds_read_b64_tr_b16 a[242:243], %[tr] offset:4352\n" \
  "s_waitcnt lgkmcnt(14)\n" \
  "ds_read_b64_tr_b16 a[244:245], %[tr] offset:4128\n" \
  "s_waitcnt lgkmcnt(14)\n" \
  "ds_read_b64_tr_b16 a[246:247], %[tr] offset:4384\n" \
  "s_waitcnt lgkmcnt(14)\n" \
  "ds_read_b64_tr_b16 a[248:249], %[tr] offset:6144\n" \
  "s_waitcnt lgkmcnt(14)\n" \
  "ds_read_b64_tr_b16 a[250:251], %[tr] offset:6400\n" \
  "s_waitcnt lgkmcnt(14)\n" \
  "ds_read_b64_tr_b16 a[252:253], %[tr] offset:6176\n" \
  "s_waitcnt lgkmcnt(14)\n" \
  "ds_read_b64_tr_b16 a[254:255], %[tr] offset:6432\n" \
  "s_waitcnt vmcnt(12)\n" \
  "s_waitcnt lgkmcnt(14)\n" \
  "ds_read_b64_tr_b16 v[192:193], %[tr] offset:8192\n" \
  "s_waitcnt lgkmcnt(14)\n" \
  "ds_read_b64_tr_b16 v[194:195], %[tr] offset:8448\n" \
  "s_waitcnt lgkmcnt(14)\n" \
  "ds_read_b64_tr_b16 v[196:197], %[tr] offset:8224\n" \
  "s_waitcnt lgkmcnt(14)\n" \
  "ds_read_b64_tr_b16 v[198:199], %[tr] offset:8480\n" \
  "s_waitcnt lgkmcnt(14)\n" \
  "ds_read_b64_tr_b16 v[200:201], %[tr] offset:10240\n" \
  "s_waitcnt lgkmcnt(14)\n" \
  "ds_read_b64_tr_b16 v[202:203], %[tr] offset:10496\n" \
  "s_waitcnt lgkmcnt(14)\n" \
  "ds_read_b64_tr_b16 v[204:205], %[tr] offset:10272\n" \
  "s_waitcnt lgkmcnt(14)\n" \
  "ds_read_b64_tr_b16 v[206:207], %[tr] offset:10528\n" \
  "s_waitcnt vmcnt(8)\n" \
  "s_waitcnt lgkmcnt(14)\n" \
  "ds_read_b64_tr_b16 v[208:209], %[tr] offset:12288\n" \
  "s_waitcnt lgkmcnt(14)\n" \
  "ds_read_b64_tr_b16 v[210:211], %[tr] offset:12544\n" \
  "s_waitcnt lgkmcnt(14)\n" \
  "ds_read_b64_tr_b16 v[212:213], %[tr] offset:12320\n" \
  "s_waitcnt lgkmcnt(14)\n" \
  "ds_read_b64_tr_b16 v[214:215], %[tr] offset:12576\n" \
  "s_waitcnt lgkmcnt(14)\n" \
  "ds_read_b64_tr_b16 v[216:217], %[tr] offset:14336\n" \
  "s_waitcnt lgkmcnt(14)\n" \
  "ds_read_b64_tr_b16 v[218:219], %[tr] offset:14592\n" \
  "s_waitcnt lgkmcnt(14)\n" \
  "ds_read_b64_tr_b16 v[220:221], %[tr] offset:14368\n" \
  "s_waitcnt lgkmcnt(14)\n" \
  "ds_read_b64_tr_b16 v[222:223], %[tr] offset:14624\n" \
  "s_waitcnt vmcnt(4)\n" \
  "s_waitcnt lgkmcnt(14)\n" \
  "ds_read_b64_tr_b16 v[224:225], %[tr] offset:16384\n" \
  "s_waitcnt lgkmcnt(14)\n" \
  "ds_read_b64_tr_b16 v[226:227], %[tr] offset:16640\n" \
  "s_waitcnt lgkmcnt(14)\n" \
  "ds_read_b64_tr_b16 v[228:229], %[tr] offset:16416\n" \
  "s_waitcnt lgkmcnt(14)\n" \
  "ds_read_b64_tr_b16 v[230:231], %[tr] offset:16672\n" \
  "s_waitcnt lgkmcnt(14)\n" \
  "ds_read_b64_tr_b16 v[232:233], %[tr] offset:18432\n" \
  "s_waitcnt lgkmcnt(14)\n" \
  "ds_read_b64_tr_b16 v[234:235], %[tr] offset:18688\n" \
  "s_waitcnt lgkmcnt(14)\n" \
  "ds_read_b64_tr_b16 v[236:237], %[tr] offset:18464\n" \
  "s_waitcnt lgkmcnt(14)\n" \
  "ds_read_b64_tr_b16 v[238:239], %[tr] offset:18720\n" \
  "s_waitcnt vmcnt(0)\n" \
  "s_waitcnt lgkmcnt(14)\n" \
  "ds_read_b64_tr_b16 v[240:241], %[tr] offset:20480\n" \
  "s_waitcnt lgkmcnt(14)\n" \
  "ds_read_b64_tr_b16 v[242:243], %[tr] offset:20736\n" \
  "s_waitcnt lgkmcnt(14)\n" \
  "ds_read_b64_tr_b16 v[244:245], %[tr] offset:20512\n" \
  "s_waitcnt lgkmcnt(14)\n" \
  "ds_read_b64_tr_b16 v[246:247], %[tr] offset:20768\n" \
  "s_waitcnt lgkmcnt(14)\n" \
  "ds_read_b64_tr_b16 v[248:249], %[tr] offset:22528\n" \
  "s_waitcnt lgkmcnt(14)\n" \
  "ds_read_b64_tr_b16 v[250:251], %[tr] offset:22784\n" \
  "s_waitcnt lgkmcnt(14)\n" \
  "ds_read_b64_tr_b16 v[252:253], %[tr] offset:22560\n" \
  "s_waitcnt lgkmcnt(14)\n" \
  "ds_read_b64_tr_b16 v[254:255], %[tr] offset:22816\n" \
  "s_waitcnt vmcnt(0) lgkmcnt(0)\n" \
  "s_barrier\n" \
  "s_add_u32 s57, s57, 40960\n" \
  "s_add_u32 s58, s56, 40960\n" \
  "s_add_u32 m0, s58, 0\n" \
  "s_add_u32 s53, s57, 0\n" \
  "buffer_load_dwordx4 %[l16], s[40:43], s53 offen offset:0 lds\n" \
  "buffer_load_dwordx4 %[l16], s[40:43], s53 offen offset:1024 lds\n" \
  "buffer_load_dwordx4 %[l16], s[40:43], s53 offen offset:2048 lds\n" \
  "buffer_load_dwordx4 %[l16], s[40:43], s53 offen offset:3072 lds\n" \
  "s_add_u32 m0, s58, 4096\n" \
  "s_add_u32 s53, s57, 4096\n" \
  "buffer_load_dwordx4 %[l16], s[40:43], s53 offen offset:0 lds\n" \
  "buffer_load_dwordx4 %[l16], s[40:43], s53 offen offset:1024 lds\n" \
  "buffer_load_dwordx4 %[l16], s[40:43], s53 offen offset:2048 lds\n" \
  "buffer_load_dwordx4 %[l16], s[40:43], s53 offen offset:3072 lds\n" \
  "s_add_u32 m0, s58, 8192\n" \
  "s_add_u32 s53, s57, 8192\n" \
  "buffer_load_dwordx4 %[l16], s[40:43], s53 offen offset:0 lds\n" \
  "buffer_load_dwordx4 %[l16], s[40:43], s53 offen offset:1024 lds\n" \
  "s_add_u32 s57, s57, 40960\n" \
  "s_mov_b32 s59, 81920\n" \
  "s_mov_b32 s60, 40960\n" \
  "s_lshl_b32 s61, %[h], 19\n" \
  "s_lshl_b32 s55, %[w], 10\n" \
  "s_add_u32 s61, s61, s55\n" \
  "s_add_u32 s61, s61, 14680064\n" \
  "s_mov_b32 s62, 0\n" \
  "s_mov_b32 s63, 6291456\n" \
  "v_mov_b32 v16, %[l16]\n" \
  "ds_read_b128 v[100:103], v16 offset:20480\n" \
  "ds_read_b128 v[96:99], v16 offset:0\n" \
  "ds_read_b128 v[108:111], v16 offset:21504\n" \
  "ds_read_b128 v[104:107], v16 offset:1024\n" \
  "ds_read_b128 v[116:119], v16 offset:22528\n" \
  "ds_read_b128 v[112:115], v16 offset:2048\n" \
  "s_waitcnt lgkmcnt(4)\n" \
  "v_mfma_f32_16x16x32_bf16 v[128:131], a[0:3], v[96:99], 0\n" \
  "ds_read_b128 v[124:127], v16 offset:23552\n" \
  "v_mfma_f32_16x16x32_bf16 v[132:135], a[0:3], v[100:103], 0\n" \
  "ds_read_b128 v[120:123], v16 offset:3072\n" \
  "v_mfma_f32_16x16x32_bf16 v[136:139], a[4:7], v[96:99], 0\n" \
  "v_mfma_f32_16x16x32_bf16 v[140:143], a[4:7], v[100:103], 0\n" \
  "v_mfma_f32_16x16x32_bf16 v[144:147], a[8:11], v[96:99], 0\n" \
  "v_mfma_f32_16x16x32_bf16 v[148:151], a[8:11], v[100:103], 0\n" \
  "v_mfma_f32_16x16x32_bf16 v[152:155], a[12:15], v[96:99], 0\n" \
  "v_mfma_f32_16x16x32_bf16 v[156:159], a[12:15], v[100:103], 0\n" \
  "s_waitcnt lgkmcnt(4)\n" \
  "v_mfma_f32_16x16x32_bf16 v[128:131], a[16:19], v[104:107], v[128:131]\n" \
  "ds_read_b128 v[100:103], v16 offset:24576\n" \
  "v_mfma_f32_16x16x32_bf16 v[132:135], a[16:19], v[108:111], v[132:135]\n" \
  "ds_read_b128 v[96:99], v16 offset:4096\n" \
  "v_mfma_f32_16x16x32_bf16 v[136:139], a[20:23], v[104:107], v[136:139]\n" \
  "v_mfma_f32_16x16x32_bf16 v[140:143], a[20:23], v[108:111], v[140:143]\n" \
  "v_mfma_f32_16x16x32_bf16 v[144:147], a[24:27], v[104:107], v[144:147]\n" \
  "v_mfma_f32_16x16x32_bf16 v[148:151], a[24:27], v[108:111], v[148:151]\n" \
  "v_mfma_f32_16x16x32_bf16 v[152:155], a[28:31], v[104:107], v[152:155]\n" \
  "v_mfma_f32_16x16x32_bf16 v[156:159], a[28:31], v[108:111], v[156:159]\n" \
  "s_waitcnt lgkmcnt(4)\n" \
  "v_mfma_f32_16x16x32_bf16 v[128:131], a[32:35], v[112:115], v[128:131]\n" \
  "ds_read_b128 v[108:111], v16 offset:25600\n" \
  "v_mfma_f32_16x16x32_bf16 v[132:135], a[32:35], v[116:119], v[132:135]\n" \
  "ds_read_b128 v[104:107], v16 offset:5120\n" \
  "v_mfma_f32_16x16x32_bf16 v[136:139], a[36:39], v[112:115], v[136:139]\n" \
  "v_mfma_f32_16x16x32_bf16 v[140:143], a[36:39], v[116:119], v[140:143]\n" \
  "v_mfma_f32_16x16x32_bf16 v[144:147], a[40:43], v[112:115], v[144:147]\n" \
  "v_mfma_f32_16x16x32_bf16 v[148:151], a[40:43], v[116:119], v[148:151]\n" \
  "v_mfma_f32_16x16x32_bf16 v[152:155], a[44:47], v[112:115], v[152:155]\n" \
  "v_mfma_f32_16x16x32_bf16 v[156:159], a[44:47], v[116:119], v[156:159]\n" \
  "s_waitcnt lgkmcnt(4)\n" \
  "v_mfma_f32_16x16x32_bf16 v[128:131], a[48:51], v[120:123], v[128:131]\n" \
  "ds_read_b128 v[116:119], v16 offset:26624\n" \
  "v_mfma_f32_16x16x32_bf16 v[132:135], a[48:51], v[124:127], v[132:135]\n" \
  "ds_read_b128 v[112:115], v16 offset:6144\n" \
  "v_mfma_f32_16x16x32_bf16 v[136:139], a[52:55], v[120:123], v[136:139]\n" \
  "v_mfma_f32_16x16x32_bf16 v[140:143], a[52:55], v[124:127], v[140:143]\n" \
  "v_mfma_f32_16x16x32_bf16 v[144:147], a[56:59], v[120:123], v[144:147]\n" \
  "v_mfma_f32_16x16x32_bf16 v[148:151], a[56:59], v[124:127], v[148:151]\n" \
  "v_mfma_f32_16x16x32_bf16 v[152:155], a[60:63], v[120:123], v[152:155]\n" \
  "v_mfma_f32_16x16x32_bf16 v[156:159], a[60:63], v[124:127], v[156:159]\n" \
  "s_waitcnt lgkmcnt(4)\n" \
  "v_mfma_f32_16x16x32_bf16 v[128:131], a[64:67], v[96:99], v[128:131]\n" \
  "ds_read_b128 v[124:127], v16 offset:27648\n" \
  "v_mfma_f32_16x16x32_bf16 v[132:135], a[64:67], v[100:103], v[132:135]\n" \
  "ds_read_b128 v[120:123], v16 offset:7168\n" \
  "v_mfma_f32_16x16x32_bf16 v[136:139], a[68:71], v[96:99], v[136:139]\n" \
  "v_mfma_f32_16x16x32_bf16 v[140:143], a[68:71], v[100:103], v[140:143]\n" \
  "v_mfma_f32_16x16x32_bf16 v[144:147], a[72:75], v[96:99], v[144:147]\n" \
  "v_mfma_f32_16x16x32_bf16 v[148:151], a[72:75], v[100:103], v[148:151]\n" \
  "v_mfma_f32_16x16x32_bf16 v[152:155], a[76:79], v[96:99], v[152:155]\n" \
  "v_mfma_f32_16x16x32_bf16 v[156:159], a[76:79], v[100:103], v[156:159]\n" \
  "s_waitcnt lgkmcnt(4)\n" \
  "v_mfma_f32_16x16x32_bf16 v[128:131], a[80:83], v[104:107], v[128:131]\n" \
  "ds_read_b128 v[100:103], v16 offset:28672\n" \
  "v_mfma_f32_16x16x32_bf16 v[132:135], a[80:83], v[108:111], v[132:135]\n" \
  "ds_read_b128 v[96:99], v16 offset:8192\n" \
  "v_mfma_f32_16x16x32_bf16 v[136:139], a[84:87], v[104:107], v[136:139]\n" \
  "v_mfma_f32_16x16x32_bf16 v[140:143], a[84:87], v[108:111], v[140:143]\n" \
  "v_mfma_f32_16x16x32_bf16 v[144:147], a[88:91], v[104:107], v[144:147]\n" \
  "v_mfma_f32_16x16x32_bf16 v[148:151], a[88:91], v[108:111], v[148:151]\n" \
  "v_mfma_f32_16x16x32_bf16 v[152:155], a[92:95], v[104:107], v[152:155]\n" \
  "v_mfma_f32_16x16x32_bf16 v[156:159], a[92:95], v[108:111], v[156:159]\n" \
  "s_waitcnt lgkmcnt(4)\n" \
  "v_mfma_f32_16x16x32_bf16 v[128:131], a[96:99], v[112:115], v[128:131]\n" \
  "ds_read_b128 v[108:111], v16 offset:29696\n" \
  "v_mfma_f32_16x16x32_bf16 v[132:135], a[96:99], v[116:119], v[132:135]\n" \
  "ds_read_b128 v[104:107], v16 offset:9216\n" \
  "v_mfma_f32_16x16x32_bf16 v[136:139], a[100:103], v[112:115], v[136:139]\n" \
  "v_mfma_f32_16x16x32_bf16 v[140:143], a[100:103], v[116:119], v[140:143]\n" \
  "v_mfma_f32_16x16x32_bf16 v[144:147], a[104:107], v[112:115], v[144:147]\n" \
  "v_mfma_f32_16x16x32_bf16 v[148:151], a[104:107], v[116:119], v[148:151]\n" \
  "v_mfma_f32_16x16x32_bf16 v[152:155], a[108:111], v[112:115], v[152:155]\n" \
  "v_mfma_f32_16x16x32_bf16 v[156:159], a[108:111], v[116:119], v[156:159]\n" \
  "s_waitcnt lgkmcnt(4)\n" \
  "v_mfma_f32_16x16x32_bf16 v[128:131], a[112:115], v[120:123], v[128:131]\n" \
  "ds_read_b128 v[116:119], v16 offset:30720\n" \
  "v_mfma_f32_16x16x32_bf16 v[132:135], a[112:115], v[124:127], v[132:135]\n" \
  "ds_read_b128 v[112:115], v16 offset:10240\n" \
  "v_mfma_f32_16x16x32_bf16 v[136:139], a[116:119], v[120:123], v[136:139]\n" \
  "v_mfma_f32_16x16x32_bf16 v[140:143], a[116:119], v[124:127], v[140:143]\n" \
  "v_mfma_f32_16x16x32_bf16 v[144:147], a[120:123], v[120:123], v[144:147]\n" \
  "v_mfma_f32_16x16x32_bf16 v[148:151], a[120:123], v[124:127], v[148:151]\n" \
  "v_mfma_f32_16x16x32_bf16 v[152:155], a[124:127], v[120:123], v[152:155]\n" \
  "v_mfma_f32_16x16x32_bf16 v[156:159], a[124:127], v[124:127], v[156:159]\n" \
  "s_waitcnt lgkmcnt(4)\n" \
  "v_mfma_f32_16x16x32_bf16 v[128:131], a[128:131], v[96:99], v[128:131]\n" \
  "ds_read_b128 v[124:127], v16 offset:31744\n" \
  "v_mfma_f32_16x16x32_bf16 v[132:135], a[128:131], v[100:103], v[132:135]\n" \
  "ds_read_b128 v[120:123], v16 offset:11264\n" \
  "v_mfma_f32_16x16x32_bf16 v[136:139], a[132:135], v[96:99], v[136:139]\n" \
  "v_mfma_f32_16x16x32_bf16 v[140:143], a[132:135], v[100:103], v[140:143]\n" \
  "v_mfma_f32_16x16x32_bf16 v[144:147], a[136:139], v[96:99], v[144:147]\n" \
  "v_mfma_f32_16x16x32_bf16 v[148:151], a[136:139], v[100:103], v[148:151]\n" \
  "v_mfma_f32_16x16x32_bf16 v[152:155], a[140:143], v[96:99], v[152:155]\n" \
  "v_mfma_f32_16x16x32_bf16 v[156:159], a[140:143], v[100:103], v[156:159]\n" \
  "s_waitcnt lgkmcnt(4)\n" \
  "v_mfma_f32_16x16x32_bf16 v[128:131], a[144:147], v[104:107], v[128:131]\n" \
  "ds_read_b128 v[100:103], v16 offset:32768\n" \
  "v_mfma_f32_16x16x32_bf16 v[132:135], a[144:147], v[108:111], v[132:135]\n" \
  "ds_read_b128 v[96:99], v16 offset:12288\n" \
  "v_mfma_f32_16x16x32_bf16 v[136:139], a[148:151], v[104:107], v[136:139]\n" \
  "s_waitcnt vmcnt(0)\n" \
  "v_mfma_f32_16x16x32_bf16 v[140:143], a[148:151], v[108:111], v[140:143]\n" \
  "s_barrier\n" \
  "v_mfma_f32_16x16x32_bf16 v[144:147], a[152:155], v[104:107], v[144:147]\n" \
  "s_add_u32 s58, s56, s59\n" \
  "v_mfma_f32_16x16x32_bf16 v[148:151], a[152:155], v[108:111], v[148:151]\n" \
  "v_add_u32 v17, s60, %[l16]\n" \
  "v_mfma_f32_16x16x32_bf16 v[152:155], a[156:159], v[104:107], v[152:155]\n" \
  "v_mfma_f32_16x16x32_bf16 v[156:159], a[156:159], v[108:111], v[156:159]\n" \
  "s_waitcnt lgkmcnt(4)\n" \
  "v_mfma_f32_16x16x32_bf16 v[128:131], a[160:163], v[112:115], v[128:131]\n" \
  "ds_read_b128 v[108:111], v16 offset:33792\n" \
  "v_mfma_f32_16x16x32_bf16 v[132:135], a[160:163], v[116:119], v[132:135]\n" \
  "ds_read_b128 v[104:107], v16 offset:13312\n" \
  "v_mfma_f32_16x16x32_bf16 v[136:139], a[164:167], v[112:115], v[136:139]\n" \
  "s_add_u32 m0, s58, 0\n" \
  "v_mfma_f32_16x16x32_bf16 v[140:143], a[164:167], v[116:119], v[140:143]\n" \
  "s_add_u32 s53, s57, 0\n" \
  "v_mfma_f32_16x16x32_bf16 v[144:147], a[168:171], v[112:115], v[144:147]\n" \
  "buffer_load_dwordx4 %[l16], s[40:43], s53 offen offset:0 lds\n" \
  "v_mfma_f32_16x16x32_bf16 v[148:151], a[168:171], v[116:119], v[148:151]\n" \
  "buffer_load_dwordx4 %[l16], s[40:43], s53 offen offset:1024 lds\n" \
  "v_mfma_f32_16x16x32_bf16 v[152:155], a[172:175], v[112:115], v[152:155]\n" \
  "buffer_load_dwordx4 %[l16], s[40:43], s53 offen offset:2048 lds\n" \
  "v_mfma_f32_16x16x32_bf16 v[156:159], a[172:175], v[116:119], v[156:159]\n" \
  "buffer_load_dwordx4 %[l16], s[40:43], s53 offen offset:3072 lds\n" \
  "s_waitcnt lgkmcnt(4)\n" \
  "v_mfma_f32_16x16x32_bf16 v[128:131], a[176:179], v[120:123], v[128:131]\n" \
  "ds_read_b128 v[116:119], v16 offset:34816\n" \
  "v_mfma_f32_16x16x32_bf16 v[132:135], a[176:179], v[124:127], v[132:135]\n" \
  "ds_read_b128 v[112:115], v16 offset:14336\n" \
  "v_mfma_f32_16x16x32_bf16 v[136:139], a[180:183], v[120:123], v[136:139]\n" \
  "s_add_u32 m0, s58, 4096\n" \
  "v_mfma_f32_16x16x32_bf16 v[140:143], a[180:183], v[124:127], v[140:143]\n" \
  "s_add_u32 s53, s57, 4096\n" \
  "v_mfma_f32_16x16x32_bf16 v[144:147], a[184:187], v[120:123], v[144:147]\n" \
  "buffer_load_dwordx4 %[l16], s[40:43], s53 offen offset:0 lds\n" \
  "v_mfma_f32_16x16x32_bf16 v[148:151], a[184:187], v[124:127], v[148:151]\n" \
  "buffer_load_dwordx4 %[l16], s[40:43], s53 offen offset:1024 lds\n" \
  "v_mfma_f32_16x16x32_bf16 v[152:155], a[188:191], v[120:123], v[152:155]\n" \
  "buffer_load_dwordx4 %[l16], s[40:43], s53 offen offset:2048 lds\n" \
  "v_mfma_f32_16x16x32_bf16 v[156:159], a[188:191], v[124:127], v[156:159]\n" \
  "buffer_load_dwordx4 %[l16], s[40:43], s53 offen offset:3072 lds\n" \
  "s_waitcnt lgkmcnt(4)\n" \
  "v_mfma_f32_16x16x32_bf16 v[128:131], a[192:195], v[96:99], v[128:131]\n" \
  "ds_read_b128 v[124:127], v16 offset:35840\n" \
  "v_mfma_f32_16x16x32_bf16 v[132:135], a[192:195], v[100:103], v[132:135]\n" \
  "ds_read_b128 v[120:123], v16 offset:15360\n" \
  "v_mfma_f32_16x16x32_bf16 v[136:139], a[196:199], v[96:99], v[136:139]\n" \
  "s_add_u32 m0, s58, 8192\n" \
  "v_mfma_f32_16x16x32_bf16 v[140:143], a[196:199], v[100:103], v[140:143]\n" \
  "s_add_u32 s53, s57, 8192\n" \
  "v_mfma_f32_16x16x32_bf16 v[144:147], a[200:203], v[96:99], v[144:147]\n" \
  "buffer_load_dwordx4 %[l16], s[40:43], s53 offen offset:0 lds\n" \
  "v_mfma_f32_16x16x32_bf16 v[148:151], a[200:203], v[100:103], v[148:151]\n" \
  "buffer_load_dwordx4 %[l16], s[40:43], s53 offen offset:1024 lds\n" \
  "v_mfma_f32_16x16x32_bf16 v[152:155], a[204:207], v[96:99], v[152:155]\n" \
  "v_mfma_f32_16x16x32_bf16 v[156:159], a[204:207], v[100:103], v[156:159]\n" \
  "s_waitcnt lgkmcnt(4)\n" \
  "v_mfma_f32_16x16x32_bf16 v[128:131], a[208:211], v[104:107], v[128:131]\n" \
  "ds_read_b128 v[100:103], v16 offset:36864\n" \
  "v_mfma_f32_16x16x32_bf16 v[132:135], a[208:211], v[108:111], v[132:135]\n" \
  "ds_read_b128 v[96:99], v16 offset:16384\n" \
  "v_mfma_f32_16x16x32_bf16 v[136:139], a[212:215], v[104:107], v[136:139]\n" \
  "s_add_u32 s57, s57, 40960\n" \
  "v_mfma_f32_16x16x32_bf16 v[140:143], a[212:215], v[108:111], v[140:143]\n" \
  "s_mov_b32 s60, s59\n" \
  "v_mfma_f32_16x16x32_bf16 v[144:147], a[216:219], v[104:107], v[144:147]\n" \
  "s_add_u32 s59, s59, 40960\n" \
  "v_mfma_f32_16x16x32_bf16 v[148:151], a[216:219], v[108:111], v[148:151]\n" \
  "s_cmp_eq_u32 s59, 122880\n" \
  "v_mfma_f32_16x16x32_bf16 v[152:155], a[220:223], v[104:107], v[152:155]\n" \
  "s_cselect_b32 s59, 0, s59\n" \
  "v_mfma_f32_16x16x32_bf16 v[156:159], a[220:223], v[108:111], v[156:159]\n" \
  "s_waitcnt lgkmcnt(4)\n" \
  "v_mfma_f32_16x16x32_bf16 v[128:131], a[224:227], v[112:115], v[128:131]\n" \
  "ds_read_b128 v[108:111], v16 offset:37888\n" \
  "v_mfma_f32_16x16x32_bf16 v[132:135], a[224:227], v[116:119], v[132:135]\n" \
  "ds_read_b128 v[104:107], v16 offset:17408\n" \
  "v_mfma_f32_16x16x32_bf16 v[136:139], a[228:231], v[112:115], v[136:139]\n" \
  "v_mfma_f32_16x16x32_bf16 v[140:143], a[228:231], v[116:119], v[140:143]\n" \
  "v_mfma_f32_16x16x32_bf16 v[144:147], a[232:235], v[112:115], v[144:147]\n" \
  "v_mfma_f32_16x16x32_bf16 v[148:151], a[232:235], v[116:119], v[148:151]\n" \
  "v_mfma_f32_16x16x32_bf16 v[152:155], a[236:239], v[112:115], v[152:155]\n" \
  "v_mfma_f32_16x16x32_bf16 v[156:159], a[236:239], v[116:119], v[156:159]\n" \
  "s_waitcnt lgkmcnt(4)\n" \
  "v_mfma_f32_16x16x32_bf16 v[128:131], a[240:243], v[120:123], v[128:131]\n" \
  "ds_read_b128 v[116:119], v16 offset:38912\n" \
  "v_mfma_f32_16x16x32_bf16 v[132:135], a[240:243], v[124:127], v[132:135]\n" \
  "ds_read_b128 v[112:115], v16 offset:18432\n" \
  "v_mfma_f32_16x16x32_bf16 v[136:139], a[244:247], v[120:123], v[136:139]\n" \
  "v_mfma_f32_16x16x32_bf16 v[140:143], a[244:247], v[124:127], v[140:143]\n" \
  "v_mfma_f32_16x16x32_bf16 v[144:147], a[248:251], v[120:123], v[144:147]\n" \
  "v_mfma_f32_16x16x32_bf16 v[148:151], a[248:251], v[124:127], v[148:151]\n" \
  "v_mfma_f32_16x16x32_bf16 v[152:155], a[252:255], v[120:123], v[152:155]\n" \
  "v_mfma_f32_16x16x32_bf16 v[156:159], a[252:255], v[124:127], v[156:159]\n" \
  "s_waitcnt lgkmcnt(4)\n" \
  "v_mfma_f32_16x16x32_bf16 v[128:131], v[192:195], v[96:99], v[128:131]\n" \
  "ds_read_b128 v[124:127], v16 offset:39936\n" \
  "v_mfma_f32_16x16x32_bf16 v[132:135], v[192:195], v[100:103], v[132:135]\n" \
  "ds_read_b128 v[120:123], v16 offset:19456\n" \
  "v_mfma_f32_16x16x32_bf16 v[136:139], v[196:199], v[96:99], v[136:139]\n" \
  "v_mfma_f32_16x16x32_bf16 v[140:143], v[196:199], v[100:103], v[140:143]\n" \
  "v_mfma_f32_16x16x32_bf16 v[144:147], v[200:203], v[96:99], v[144:147]\n" \
  "v_mfma_f32_16x16x32_bf16 v[148:151], v[200:203], v[100:103], v[148:151]\n" \
  "v_mfma_f32_16x16x32_bf16 v[152:155], v[204:207], v[96:99], v[152:155]\n" \
  "v_mfma_f32_16x16x32_bf16 v[156:159], v[204:207], v[100:103], v[156:159]\n" \
  "s_waitcnt lgkmcnt(4)\n" \
  "v_mfma_f32_16x16x32_bf16 v[128:131], v[208:211], v[104:107], v[128:131]\n" \
  "ds_read_b128 v[100:103], v17 offset:20480\n" \
  "v_mfma_f32_16x16x32_bf16 v[132:135], v[208:211], v[108:111], v[132:135]\n" \
  "ds_read_b128 v[96:99], v17 offset:0\n" \
  "v_mfma_f32_16x16x32_bf16 v[136:139], v[212:215], v[104:107], v[136:139]\n" \
  "v_mfma_f32_16x16x32_bf16 v[140:143], v[212:215], v[108:111], v[140:143]\n" \
  "v_mfma_f32_16x16x32_bf16 v[144:147], v[216:219], v[104:107], v[144:147]\n" \
  "v_mfma_f32_16x16x32_bf16 v[148:151], v[216:219], v[108:111], v[148:151]\n" \
  "v_mfma_f32_16x16x32_bf16 v[152:155], v[220:223], v[104:107], v[152:155]\n" \
  "v_mfma_f32_16x16x32_bf16 v[156:159], v[220:223], v[108:111], v[156:159]\n" \
  "s_waitcnt lgkmcnt(4)\n" \
  "v_mfma_f32_16x16x32_bf16 v[128:131], v[224:227], v[112:115], v[128:131]\n" \
  "ds_read_b128 v[108:111], v17 offset:21504\n" \
  "v_mfma_f32_16x16x32_bf16 v[132:135], v[224:227], v[116:119], v[132:135]\n" \
  "ds_read_b128 v[104:107], v17 offset:1024\n" \
  "v_mfma_f32_16x16x32_bf16 v[136:139], v[228:231], v[112:115], v[136:139]\n" \
  "v_mfma_f32_16x16x32_bf16 v[140:143], v[228:231], v[116:119], v[140:143]\n" \
  "v_mfma_f32_16x16x32_bf16 v[144:147], v[232:235], v[112:115], v[144:147]\n" \
  "v_mfma_f32_16x16x32_bf16 v[148:151], v[232:235], v[116:119], v[148:151]\n" \
  "v_mfma_f32_16x16x32_bf16 v[152:155], v[236:239], v[112:115], v[152:155]\n" \
  "v_mfma_f32_16x16x32_bf16 v[156:159], v[236:239], v[116:119], v[156:159]\n" \
  "s_waitcnt lgkmcnt(4)\n" \
  "v_mfma_f32_16x16x32_bf16 v[128:131], v[240:243], v[120:123], v[128:131]\n" \
  "ds_read_b128 v[116:119], v17 offset:22528\n" \
  "v_mfma_f32_16x16x32_bf16 v[132:135], v[240:243], v[124:127], v[132:135]\n" \
  "ds_read_b128 v[112:115], v17 offset:2048\n" \
  "v_mfma_f32_16x16x32_bf16 v[136:139], v[244:247], v[120:123], v[136:139]\n" \
  "v_mfma_f32_16x16x32_bf16 v[140:143], v[244:247], v[124:127], v[140:143]\n" \
  "v_mfma_f32_16x16x32_bf16 v[144:147], v[248:251], v[120:123], v[144:147]\n" \
  "v_mfma_f32_16x16x32_bf16 v[148:151], v[248:251], v[124:127], v[148:151]\n" \
  "v_mfma_f32_16x16x32_bf16 v[152:155], v[252:255], v[120:123], v[152:155]\n" \
  "v_mfma_f32_16x16x32_bf16 v[156:159], v[252:255], v[124:127], v[156:159]\n" \
  "s_mov_b32 s65, 31\n" \
  "Lqk%=:\n" \
  "s_waitcnt lgkmcnt(4)\n" \
  "v_mfma_f32_16x16x32_bf16 v[160:163], a[0:3], v[96:99], 0\n" \
  "ds_read_b128 v[124:127], v17 offset:23552\n" \
  "ds_read_b128 v[120:123], v17 offset:3072\n" \
  "v_mfma_f32_16x16x32_bf16 v[164:167], a[0:3], v[100:103], 0\n" \
  "v_cvt_pk_bf16_f32 v80, v128, v129\n" \
  "v_cvt_pk_bf16_f32 v81, v130, v131\n" \
  "v_mfma_f32_16x16x32_bf16 v[168:171], a[4:7], v[96:99], 0\n" \
  "v_cvt_pk_bf16_f32 v82, v136, v137\n" \
  "v_cvt_pk_bf16_f32 v83, v138, v139\n" \
  "v_mfma_f32_16x16x32_bf16 v[172:175], a[4:7], v[100:103], 0\n" \
  "s_add_u32 s64, s61, 0\n" \
  "buffer_store_dwordx4 v[80:83], %[l16], s[40:43], s64 offen sc1\n" \
  "v_mfma_f32_16x16x32_bf16 v[176:179], a[8:11], v[96:99], 0\n" \
  "v_cvt_pk_bf16_f32 v84, v132, v133\n" \
  "v_mfma_f32_16x16x32_bf16 v[180:183], a[8:11], v[100:103], 0\n" \
  "v_mfma_f32_16x16x32_bf16 v[184:187], a[12:15], v[96:99], 0\n" \
  "v_mfma_f32_16x16x32_bf16 v[188:191], a[12:15], v[100:103], 0\n" \
  "s_waitcnt lgkmcnt(4)\n" \
  "v_mfma_f32_16x16x32_bf16 v[160:163], a[16:19], v[104:107], v[160:163]\n" \
  "ds_read_b128 v[100:103], v17 offset:24576\n" \
  "ds_read_b128 v[96:99], v17 offset:4096\n" \
  "v_mfma_f32_16x16x32_bf16 v[164:167], a[16:19], v[108:111], v[164:167]\n" \
  "v_cvt_pk_bf16_f32 v85, v134, v135\n" \
  "v_cvt_pk_bf16_f32 v86, v140, v141\n" \
  "v_mfma_f32_16x16x32_bf16 v[168:171], a[20:23], v[104:107], v[168:171]\n" \
  "v_cvt_pk_bf16_f32 v87, v142, v143\n" \
  "s_add_u32 s64, s61, 4096\n" \
  "v_mfma_f32_16x16x32_bf16 v[172:175], a[20:23], v[108:111], v[172:175]\n" \
  "buffer_store_dwordx4 v[84:87], %[l16], s[40:43], s64 offen sc1\n" \
  "v_cvt_pk_bf16_f32 v88, v144, v145\n" \
  "v_mfma_f32_16x16x32_bf16 v[176:179], a[24:27], v[104:107], v[176:179]\n" \
  "v_cvt_pk_bf16_f32 v89, v146, v147\n" \
  "v_mfma_f32_16x16x32_bf16 v[180:183], a[24:27], v[108:111], v[180:183]\n" \
  "v_mfma_f32_16x16x32_bf16 v[184:187], a[28:31], v[104:107], v[184:187]\n" \
  "v_mfma_f32_16x16x32_bf16 v[188:191], a[28:31], v[108:111], v[188:191]\n" \
  "s_waitcnt lgkmcnt(4)\n" \
  "v_mfma_f32_16x16x32_bf16 v[160:163], a[32:35], v[112:115], v[160:163]\n" \
  "ds_read_b128 v[108:111], v17 offset:25600\n" \
  "ds_read_b128 v[104:107], v17 offset:5120\n" \
  "v_mfma_f32_16x16x32_bf16 v[164:167], a[32:35], v[116:119], v[164:167]\n" \
  "v_cvt_pk_bf16_f32 v90, v152, v153\n" \
  "v_cvt_pk_bf16_f32 v91, v154, v155\n" \
  "v_mfma_f32_16x16x32_bf16 v[168:171], a[36:39], v[112:115], v[168:171]\n" \
  "s_add_u32 s64, s61, 16777216\n" \
  "buffer_store_dwordx4 v[88:91], %[l16], s[40:43], s64 offen sc1\n" \
  "v_mfma_f32_16x16x32_bf16 v[172:175], a[36:39], v[116:119], v[172:175]\n" \
  "v_cvt_pk_bf16_f32 v92, v148, v149\n" \
  "v_cvt_pk_bf16_f32 v93, v150, v151\n" \
  "v_mfma_f32_16x16x32_bf16 v[176:179], a[40:43], v[112:115], v[176:179]\n" \
  "v_cvt_pk_bf16_f32 v94, v156, v157\n" \
  "v_mfma_f32_16x16x32_bf16 v[180:183], a[40:43], v[116:119], v[180:183]\n" \
  "v_mfma_f32_16x16x32_bf16 v[184:187], a[44:47], v[112:115], v[184:187]\n" \
  "v_mfma_f32_16x16x32_bf16 v[188:191], a[44:47], v[116:119], v[188:191]\n" \
  "s_waitcnt lgkmcnt(4)\n" \
  "v_mfma_f32_16x16x32_bf16 v[160:163], a[48:51], v[120:123], v[160:163]\n" \
  "ds_read_b128 v[116:119], v17 offset:26624\n" \
  "v_mfma_f32_16x16x32_bf16 v[164:167], a[48:51], v[124:127], v[164:167]\n" \
  "ds_read_b128 v[112:115], v17 offset:6144\n" \
  "v_mfma_f32_16x16x32_bf16 v[168:171], a[52:55], v[120:123], v[168:171]\n" \
  "v_cvt_pk_bf16_f32 v95, v158, v159\n" \
  "v_mfma_f32_16x16x32_bf16 v[172:175], a[52:55], v[124:127], v[172:175]\n" \
  "s_add_u32 s64, s61, 16781312\n" \
  "v_mfma_f32_16x16x32_bf16 v[176:179], a[56:59], v[120:123], v[176:179]\n" \
  "buffer_store_dwordx4 v[92:95], %[l16], s[40:43], s64 offen sc1\n" \
  "v_mfma_f32_16x16x32_bf16 v[180:183], a[56:59], v[124:127], v[180:183]\n" \
  "s_add_u32 s61, s61, 8192\n" \
  "v_mfma_f32_16x16x32_bf16 v[184:187], a[60:63], v[120:123], v[184:187]\n" \
  "v_mfma_f32_16x16x32_bf16 v[188:191], a[60:63], v[124:127], v[188:191]\n" \
  "s_waitcnt lgkmcnt(4)\n" \
  "v_mfma_f32_16x16x32_bf16 v[160:163], a[64:67], v[96:99], v[160:163]\n" \
  "ds_read_b128 v[124:127], v17 offset:27648\n" \
  "v_mfma_f32_16x16x32_bf16 v[164:167], a[64:67], v[100:103], v[164:167]\n" \
  "ds_read_b128 v[120:123], v17 offset:7168\n" \
  "v_mfma_f32_16x16x32_bf16 v[168:171], a[68:71], v[96:99], v[168:171]\n" \
  "v_mfma_f32_16x16x32_bf16 v[172:175], a[68:71], v[100:103], v[172:175]\n" \
  "v_mfma_f32_16x16x32_bf16 v[176:179], a[72:75], v[96:99], v[176:179]\n" \
  "v_mfma_f32_16x16x32_bf16 v[180:183], a[72:75], v[100:103], v[180:183]\n" \
  "v_mfma_f32_16x16x32_bf16 v[184:187], a[76:79], v[96:99], v[184:187]\n" \
  "v_mfma_f32_16x16x32_bf16 v[188:191], a[76:79], v[100:103], v[188:191]\n" \
  "s_waitcnt lgkmcnt(4)\n" \
  "v_mfma_f32_16x16x32_bf16 v[160:163], a[80:83], v[104:107], v[160:163]\n" \
  "ds_read_b128 v[100:103], v17 offset:28672\n" \
  "v_mfma_f32_16x16x32_bf16 v[164:167], a[80:83], v[108:111], v[164:167]\n" \
  "ds_read_b128 v[96:99], v17 offset:8192\n" \
  "v_mfma_f32_16x16x32_bf16 v[168:171], a[84:87], v[104:107], v[168:171]\n" \
  "v_mfma_f32_16x16x32_bf16 v[172:175], a[84:87], v[108:111], v[172:175]\n" \
  "v_mfma_f32_16x16x32_bf16 v[176:179], a[88:91], v[104:107], v[176:179]\n" \
  "v_mfma_f32_16x16x32_bf16 v[180:183], a[88:91], v[108:111], v[180:183]\n" \
  "v_mfma_f32_16x16x32_bf16 v[184:187], a[92:95], v[104:107], v[184:187]\n" \
  "v_mfma_f32_16x16x32_bf16 v[188:191], a[92:95], v[108:111], v[188:191]\n" \
  "s_waitcnt lgkmcnt(4)\n" \
  "v_mfma_f32_16x16x32_bf16 v[160:163], a[96:99], v[112:115], v[160:163]\n" \
  "ds_read_b128 v[108:111], v17 offset:29696\n" \
  "v_mfma_f32_16x16x32_bf16 v[164:167], a[96:99], v[116:119], v[164:167]\n" \
  "ds_read_b128 v[104:107], v17 offset:9216\n" \
  "v_mfma_f32_16x16x32_bf16 v[168:171], a[100:103], v[112:115], v[168:171]\n" \
  "v_mfma_f32_16x16x32_bf16 v[172:175], a[100:103], v[116:119], v[172:175]\n" \
  "v_mfma_f32_16x16x32_bf16 v[176:179], a[104:107], v[112:115], v[176:179]\n" \
  "v_mfma_f32_16x16x32_bf16 v[180:183], a[104:107], v[116:119], v[180:183]\n" \
  "v_mfma_f32_16x16x32_bf16 v[184:187], a[108:111], v[112:115], v[184:187]\n" \
  "v_mfma_f32_16x16x32_bf16 v[188:191], a[108:111], v[116:119], v[188:191]\n" \
  "s_waitcnt lgkmcnt(4)\n" \
  "v_mfma_f32_16x16x32_bf16 v[160:163], a[112:115], v[120:123], v[160:163]\n" \
  "ds_read_b128 v[116:119], v17 offset:30720\n" \
  "v_mfma_f32_16x16x32_bf16 v[164:167], a[112:115], v[124:127], v[164:167]\n" \
  "ds_read_b128 v[112:115], v17 offset:10240\n" \
  "v_mfma_f32_16x16x32_bf16 v[168:171], a[116:119], v[120:123], v[168:171]\n" \
  "v_mfma_f32_16x16x32_bf16 v[172:175], a[116:119], v[124:127], v[172:175]\n" \
  "v_mfma_f32_16x16x32_bf16 v[176:179], a[120:123], v[120:123], v[176:179]\n" \
  "v_mfma_f32_16x16x32_bf16 v[180:183], a[120:123], v[124:127], v[180:183]\n" \
  "v_mfma_f32_16x16x32_bf16 v[184:187], a[124:127], v[120:123], v[184:187]\n" \
  "v_mfma_f32_16x16x32_bf16 v[188:191], a[124:127], v[124:127], v[188:191]\n" \
  "s_waitcnt lgkmcnt(4)\n" \
  "v_mfma_f32_16x16x32_bf16 v[160:163], a[128:131], v[96:99], v[160:163]\n" \
  "ds_read_b128 v[124:127], v17 offset:31744\n" \
  "v_mfma_f32_16x16x32_bf16 v[164:167], a[128:131], v[100:103], v[164:167]\n" \
  "ds_read_b128 v[120:123], v17 offset:11264\n" \
  "v_mfma_f32_16x16x32_bf16 v[168:171], a[132:135], v[96:99], v[168:171]\n" \
  "v_mfma_f32_16x16x32_bf16 v[172:175], a[132:135], v[100:103], v[172:175]\n" \
  "v_mfma_f32_16x16x32_bf16 v[176:179], a[136:139], v[96:99], v[176:179]\n" \
  "v_mfma_f32_16x16x32_bf16 v[180:183], a[136:139], v[100:103], v[180:183]\n" \
  "v_mfma_f32_16x16x32_bf16 v[184:187], a[140:143], v[96:99], v[184:187]\n" \
  "v_mfma_f32_16x16x32_bf16 v[188:191], a[140:143], v[100:103], v[188:191]\n" \
  "s_waitcnt lgkmcnt(4)\n" \
  "v_mfma_f32_16x16x32_bf16 v[160:163], a[144:147], v[104:107], v[160:163]\n" \
  "ds_read_b128 v[100:103], v17 offset:32768\n" \
  "v_mfma_f32_16x16x32_bf16 v[164:167], a[144:147], v[108:111], v[164:167]\n" \
  "ds_read_b128 v[96:99], v17 offset:12288\n" \
  "v_mfma_f32_16x16x32_bf16 v[168:171], a[148:151], v[104:107], v[168:171]\n" \
  "s_waitcnt vmcnt(4)\n" \
  "v_mfma_f32_16x16x32_bf16 v[172:175], a[148:151], v[108:111], v[172:175]\n" \
  "s_barrier\n" \
  "v_mfma_f32_16x16x32_bf16 v[176:179], a[152:155], v[104:107], v[176:179]\n" \
  "s_add_u32 s58, s56, s59\n" \
  "v_mfma_f32_16x16x32_bf16 v[180:183], a[152:155], v[108:111], v[180:183]\n" \
  "v_add_u32 v16, s60, %[l16]\n" \
  "v_mfma_f32_16x16x32_bf16 v[184:187], a[156:159], v[104:107], v[184:187]\n" \
  "v_mfma_f32_16x16x32_bf16 v[188:191], a[156:159], v[108:111], v[188:191]\n" \
  "s_waitcnt lgkmcnt(4)\n" \
  "v_mfma_f32_16x16x32_bf16 v[160:163], a[160:163], v[112:115], v[160:163]\n" \
  "ds_read_b128 v[108:111], v17 offset:33792\n" \
  "v_mfma_f32_16x16x32_bf16 v[164:167], a[160:163], v[116:119], v[164:167]\n" \
  "ds_read_b128 v[104:107], v17 offset:13312\n" \
  "v_mfma_f32_16x16x32_bf16 v[168:171], a[164:167], v[112:115], v[168:171]\n" \
  "s_add_u32 m0, s58, 0\n" \
  "v_mfma_f32_16x16x32_bf16 v[172:175], a[164:167], v[116:119], v[172:175]\n" \
  "s_add_u32 s53, s57, 0\n" \
  "v_mfma_f32_16x16x32_bf16 v[176:179], a[168:171], v[112:115], v[176:179]\n" \
  "buffer_load_dwordx4 %[l16], s[40:43], s53 offen offset:0 lds\n" \
  "v_mfma_f32_16x16x32_bf16 v[180:183], a[168:171], v[116:119], v[180:183]\n" \
  "buffer_load_dwordx4 %[l16], s[40:43], s53 offen offset:1024 lds\n" \
  "v_mfma_f32_16x16x32_bf16 v[184:187], a[172:175], v[112:115], v[184:187]\n" \
  "buffer_load_dwordx4 %[l16], s[40:43], s53 offen offset:2048 lds\n" \
  "v_mfma_f32_16x16x32_bf16 v[188:191], a[172:175], v[116:119], v[188:191]\n" \
  "buffer_load_dwordx4 %[l16], s[40:43], s53 offen offset:3072 lds\n" \
  "s_waitcnt lgkmcnt(4)\n" \
  "v_mfma_f32_16x16x32_bf16 v[160:163], a[176:179], v[120:123], v[160:163]\n" \
  "ds_read_b128 v[116:119], v17 offset:34816\n" \
  "v_mfma_f32_16x16x32_bf16 v[164:167], a[176:179], v[124:127], v[164:167]\n" \
  "ds_read_b128 v[112:115], v17 offset:14336\n" \
  "v_mfma_f32_16x16x32_bf16 v[168:171], a[180:183], v[120:123], v[168:171]\n" \
  "s_add_u32 m0, s58, 4096\n" \
  "v_mfma_f32_16x16x32_bf16 v[172:175], a[180:183], v[124:127], v[172:175]\n" \
  "s_add_u32 s53, s57, 4096\n" \
  "v_mfma_f32_16x16x32_bf16 v[176:179], a[184:187], v[120:123], v[176:179]\n" \
  "buffer_load_dwordx4 %[l16], s[40:43], s53 offen offset:0 lds\n" \
  "v_mfma_f32_16x16x32_bf16 v[180:183], a[184:187], v[124:127], v[180:183]\n" \
  "buffer_load_dwordx4 %[l16], s[40:43], s53 offen offset:1024 lds\n" \
  "v_mfma_f32_16x16x32_bf16 v[184:187], a[188:191], v[120:123], v[184:187]\n" \
  "buffer_load_dwordx4 %[l16], s[40:43], s53 offen offset:2048 lds\n" \
  "v_mfma_f32_16x16x32_bf16 v[188:191], a[188:191], v[124:127], v[188:191]\n" \
  "buffer_load_dwordx4 %[l16], s[40:43], s53 offen offset:3072 lds\n" \
  "s_waitcnt lgkmcnt(4)\n" \
  "v_mfma_f32_16x16x32_bf16 v[160:163], a[192:195], v[96:99], v[160:163]\n" \
  "ds_read_b128 v[124:127], v17 offset:35840\n" \
  "v_mfma_f32_16x16x32_bf16 v[164:167], a[192:195], v[100:103], v[164:167]\n" \
  "ds_read_b128 v[120:123], v17 offset:15360\n" \
  "v_mfma_f32_16x16x32_bf16 v[168:171], a[196:199], v[96:99], v[168:171]\n" \
  "s_add_u32 m0, s58, 8192\n" \
  "v_mfma_f32_16x16x32_bf16 v[172:175], a[196:199], v[100:103], v[172:175]\n" \
  "s_add_u32 s53, s57, 8192\n" \
  "v_mfma_f32_16x16x32_bf16 v[176:179], a[200:203], v[96:99], v[176:179]\n" \
  "buffer_load_dwordx4 %[l16], s[40:43], s53 offen offset:0 lds\n" \
  "v_mfma_f32_16x16x32_bf16 v[180:183], a[200:203], v[100:103], v[180:183]\n" \
  "buffer_load_dwordx4 %[l16], s[40:43], s53 offen offset:1024 lds\n" \
  "v_mfma_f32_16x16x32_bf16 v[184:187], a[204:207], v[96:99], v[184:187]\n" \
  "v_mfma_f32_16x16x32_bf16 v[188:191], a[204:207], v[100:103], v[188:191]\n" \
  "s_waitcnt lgkmcnt(4)\n" \
  "v_mfma_f32_16x16x32_bf16 v[160:163], a[208:211], v[104:107], v[160:163]\n" \
  "ds_read_b128 v[100:103], v17 offset:36864\n" \
  "v_mfma_f32_16x16x32_bf16 v[164:167], a[208:211], v[108:111], v[164:167]\n" \
  "ds_read_b128 v[96:99], v17 offset:16384\n" \
  "v_mfma_f32_16x16x32_bf16 v[168:171], a[212:215], v[104:107], v[168:171]\n" \
  "s_add_u32 s57, s57, 40960\n" \
  "v_mfma_f32_16x16x32_bf16 v[172:175], a[212:215], v[108:111], v[172:175]\n" \
  "s_mov_b32 s60, s59\n" \
  "v_mfma_f32_16x16x32_bf16 v[176:179], a[216:219], v[104:107], v[176:179]\n" \
  "s_add_u32 s59, s59, 40960\n" \
  "v_mfma_f32_16x16x32_bf16 v[180:183], a[216:219], v[108:111], v[180:183]\n" \
  "s_cmp_eq_u32 s59, 122880\n" \
  "v_mfma_f32_16x16x32_bf16 v[184:187], a[220:223], v[104:107], v[184:187]\n" \
  "s_cselect_b32 s59, 0, s59\n" \
  "v_mfma_f32_16x16x32_bf16 v[188:191], a[220:223], v[108:111], v[188:191]\n" \
  "s_waitcnt lgkmcnt(4)\n" \
  "v_mfma_f32_16x16x32_bf16 v[160:163], a[224:227], v[112:115], v[160:163]\n" \
  "ds_read_b128 v[108:111], v17 offset:37888\n" \
  "v_mfma_f32_16x16x32_bf16 v[164:167], a[224:227], v[116:119], v[164:167]\n" \
  "ds_read_b128 v[104:107], v17 offset:17408\n" \
  "v_mfma_f32_16x16x32_bf16 v[168:171], a[228:231], v[112:115], v[168:171]\n" \
  "s_add_u32 s62, s62, 1\n" \
  "v_mfma_f32_16x16x32_bf16 v[172:175], a[228:231], v[116:119], v[172:175]\n" \
  "s_waitcnt vmcnt(10)\n" \
  "v_mfma_f32_16x16x32_bf16 v[176:179], a[232:235], v[112:115], v[176:179]\n" \
  "v_mov_b32 v18, s62\n" \
  "v_mfma_f32_16x16x32_bf16 v[180:183], a[232:235], v[116:119], v[180:183]\n" \
  "buffer_store_dword v18, %[fo], s[40:43], s63 offen sc1\n" \
  "v_mfma_f32_16x16x32_bf16 v[184:187], a[236:239], v[112:115], v[184:187]\n" \
  "v_mfma_f32_16x16x32_bf16 v[188:191], a[236:239], v[116:119], v[188:191]\n" \
  "s_waitcnt lgkmcnt(4)\n" \
  "v_mfma_f32_16x16x32_bf16 v[160:163], a[240:243], v[120:123], v[160:163]\n" \
  "ds_read_b128 v[116:119], v17 offset:38912\n" \
  "v_mfma_f32_16x16x32_bf16 v[164:167], a[240:243], v[124:127], v[164:167]\n" \
  "ds_read_b128 v[112:115], v17 offset:18432\n" \
  "v_mfma_f32_16x16x32_bf16 v[168:171], a[244:247], v[120:123], v[168:171]\n" \
  "v_mfma_f32_16x16x32_bf16 v[172:175], a[244:247], v[124:127], v[172:175]\n" \
  "v_mfma_f32_16x16x32_bf16 v[176:179], a[248:251], v[120:123], v[176:179]\n" \
  "v_mfma_f32_16x16x32_bf16 v[180:183], a[248:251], v[124:127], v[180:183]\n" \
  "v_mfma_f32_16x16x32_bf16 v[184:187], a[252:255], v[120:123], v[184:187]\n" \
  "v_mfma_f32_16x16x32_bf16 v[188:191], a[252:255], v[124:127], v[188:191]\n" \
  "s_waitcnt lgkmcnt(4)\n" \
  "v_mfma_f32_16x16x32_bf16 v[160:163], v[192:195], v[96:99], v[160:163]\n" \
  "ds_read_b128 v[124:127], v17 offset:39936\n" \
  "v_mfma_f32_16x16x32_bf16 v[164:167], v[192:195], v[100:103], v[164:167]\n" \
  "ds_read_b128 v[120:123], v17 offset:19456\n" \
  "v_mfma_f32_16x16x32_bf16 v[168:171], v[196:199], v[96:99], v[168:171]\n" \
  "v_mfma_f32_16x16x32_bf16 v[172:175], v[196:199], v[100:103], v[172:175]\n" \
  "v_mfma_f32_16x16x32_bf16 v[176:179], v[200:203], v[96:99], v[176:179]\n" \
  "v_mfma_f32_16x16x32_bf16 v[180:183], v[200:203], v[100:103], v[180:183]\n" \
  "v_mfma_f32_16x16x32_bf16 v[184:187], v[204:207], v[96:99], v[184:187]\n" \
  "v_mfma_f32_16x16x32_bf16 v[188:191], v[204:207], v[100:103], v[188:191]\n" \
  "s_waitcnt lgkmcnt(4)\n" \
  "v_mfma_f32_16x16x32_bf16 v[160:163], v[208:211], v[104:107], v[160:163]\n" \
  "ds_read_b128 v[100:103], v16 offset:20480\n" \
  "v_mfma_f32_16x16x32_bf16 v[164:167], v[208:211], v[108:111], v[164:167]\n" \
  "ds_read_b128 v[96:99], v16 offset:0\n" \
  "v_mfma_f32_16x16x32_bf16 v[168:171], v[212:215], v[104:107], v[168:171]\n" \
  "v_mfma_f32_16x16x32_bf16 v[172:175], v[212:215], v[108:111], v[172:175]\n" \
  "v_mfma_f32_16x16x32_bf16 v[176:179], v[216:219], v[104:107], v[176:179]\n" \
  "v_mfma_f32_16x16x32_bf16 v[180:183], v[216:219], v[108:111], v[180:183]\n" \
  "v_mfma_f32_16x16x32_bf16 v[184:187], v[220:223], v[104:107], v[184:187]\n" \
  "v_mfma_f32_16x16x32_bf16 v[188:191], v[220:223], v[108:111], v[188:191]\n" \
  "s_waitcnt lgkmcnt(4)\n" \
  "v_mfma_f32_16x16x32_bf16 v[160:163], v[224:227], v[112:115], v[160:163]\n" \
  "ds_read_b128 v[108:111], v16 offset:21504\n" \
  "v_mfma_f32_16x16x32_bf16 v[164:167], v[224:227], v[116:119], v[164:167]\n" \
  "ds_read_b128 v[104:107], v16 offset:1024\n" \
  "v_mfma_f32_16x16x32_bf16 v[168:171], v[228:231], v[112:115], v[168:171]\n" \
  "v_mfma_f32_16x16x32_bf16 v[172:175], v[228:231], v[116:119], v[172:175]\n" \
  "v_mfma_f32_16x16x32_bf16 v[176:179], v[232:235], v[112:115], v[176:179]\n" \
  "v_mfma_f32_16x16x32_bf16 v[180:183], v[232:235], v[116:119], v[180:183]\n" \
  "v_mfma_f32_16x16x32_bf16 v[184:187], v[236:239], v[112:115], v[184:187]\n" \
  "v_mfma_f32_16x16x32_bf16 v[188:191], v[236:239], v[116:119], v[188:191]\n" \
  "s_waitcnt lgkmcnt(4)\n" \
  "v_mfma_f32_16x16x32_bf16 v[160:163], v[240:243], v[120:123], v[160:163]\n" \
  "ds_read_b128 v[116:119], v16 offset:22528\n" \
  "v_mfma_f32_16x16x32_bf16 v[164:167], v[240:243], v[124:127], v[164:167]\n" \
  "ds_read_b128 v[112:115], v16 offset:2048\n" \
  "v_mfma_f32_16x16x32_bf16 v[168:171], v[244:247], v[120:123], v[168:171]\n" \
  "v_mfma_f32_16x16x32_bf16 v[172:175], v[244:247], v[124:127], v[172:175]\n" \
  "v_mfma_f32_16x16x32_bf16 v[176:179], v[248:251], v[120:123], v[176:179]\n" \
  "v_mfma_f32_16x16x32_bf16 v[180:183], v[248:251], v[124:127], v[180:183]\n" \
  "v_mfma_f32_16x16x32_bf16 v[184:187], v[252:255], v[120:123], v[184:187]\n" \
  "v_mfma_f32_16x16x32_bf16 v[188:191], v[252:255], v[124:127], v[188:191]\n" \
  "s_waitcnt lgkmcnt(4)\n" \
  "v_mfma_f32_16x16x32_bf16 v[128:131], a[0:3], v[96:99], 0\n" \
  "ds_read_b128 v[124:127], v16 offset:23552\n" \
  "ds_read_b128 v[120:123], v16 offset:3072\n" \
  "v_mfma_f32_16x16x32_bf16 v[132:135], a[0:3], v[100:103], 0\n" \
  "v_cvt_pk_bf16_f32 v80, v160, v161\n" \
  "v_cvt_pk_bf16_f32 v81, v162, v163\n" \
  "v_mfma_f32_16x16x32_bf16 v[136:139], a[4:7], v[96:99], 0\n" \
  "v_cvt_pk_bf16_f32 v82, v168, v169\n" \
  "v_cvt_pk_bf16_f32 v83, v170, v171\n" \
  "v_mfma_f32_16x16x32_bf16 v[140:143], a[4:7], v[100:103], 0\n" \
  "s_add_u32 s64, s61, 0\n" \
  "buffer_store_dwordx4 v[80:83], %[l16], s[40:43], s64 offen sc1\n" \
  "v_mfma_f32_16x16x32_bf16 v[144:147], a[8:11], v[96:99], 0\n" \
  "v_cvt_pk_bf16_f32 v84, v164, v165\n" \
  "v_mfma_f32_16x16x32_bf16 v[148:151], a[8:11], v[100:103], 0\n" \
  "v_mfma_f32_16x16x32_bf16 v[152:155], a[12:15], v[96:99], 0\n" \
  "v_mfma_f32_16x16x32_bf16 v[156:159], a[12:15], v[100:103], 0\n" \
  "s_waitcnt lgkmcnt(4)\n" \
  "v_mfma_f32_16x16x32_bf16 v[128:131], a[16:19], v[104:107], v[128:131]\n" \
  "ds_read_b128 v[100:103], v16 offset:24576\n" \
  "ds_read_b128 v[96:99], v16 offset:4096\n" \
  "v_mfma_f32_16x16x32_bf16 v[132:135], a[16:19], v[108:111], v[132:135]\n" \
  "v_cvt_pk_bf16_f32 v85, v166, v167\n" \
  "v_cvt_pk_bf16_f32 v86, v172, v173\n" \
  "v_mfma_f32_16x16x32_bf16 v[136:139], a[20:23], v[104:107], v[136:139]\n" \
  "v_cvt_pk_bf16_f32 v87, v174, v175\n" \
  "s_add_u32 s64, s61, 4096\n" \
  "v_mfma_f32_16x16x32_bf16 v[140:143], a[20:23], v[108:111], v[140:143]\n" \
  "buffer_store_dwordx4 v[84:87], %[l16], s[40:43], s64 offen sc1\n" \
  "v_cvt_pk_bf16_f32 v88, v176, v177\n" \
  "v_mfma_f32_16x16x32_bf16 v[144:147], a[24:27], v[104:107], v[144:147]\n" \
  "v_cvt_pk_bf16_f32 v89, v178, v179\n" \
  "v_mfma_f32_16x16x32_bf16 v[148:151], a[24:27], v[108:111], v[148:151]\n" \
  "v_mfma_f32_16x16x32_bf16 v[152:155], a[28:31], v[104:107], v[152:155]\n" \
  "v_mfma_f32_16x16x32_bf16 v[156:159], a[28:31], v[108:111], v[156:159]\n" \
  "s_waitcnt lgkmcnt(4)\n" \
  "v_mfma_f32_16x16x32_bf16 v[128:131], a[32:35], v[112:115], v[128:131]\n" \
  "ds_read_b128 v[108:111], v16 offset:25600\n" \
  "ds_read_b128 v[104:107], v16 offset:5120\n" \
  "v_mfma_f32_16x16x32_bf16 v[132:135], a[32:35], v[116:119], v[132:135]\n" \
  "v_cvt_pk_bf16_f32 v90, v184, v185\n" \
  "v_cvt_pk_bf16_f32 v91, v186, v187\n" \
  "v_mfma_f32_16x16x32_bf16 v[136:139], a[36:39], v[112:115], v[136:139]\n" \
  "s_add_u32 s64, s61, 16777216\n" \
  "buffer_store_dwordx4 v[88:91], %[l16], s[40:43], s64 offen sc1\n" \
  "v_mfma_f32_16x16x32_bf16 v[140:143], a[36:39], v[116:119], v[140:143]\n" \
  "v_cvt_pk_bf16_f32 v92, v180, v181\n" \
  "v_cvt_pk_bf16_f32 v93, v182, v183\n" \
  "v_mfma_f32_16x16x32_bf16 v[144:147], a[40:43], v[112:115], v[144:147]\n" \
  "v_cvt_pk_bf16_f32 v94, v188, v189\n" \
  "v_mfma_f32_16x16x32_bf16 v[148:151], a[40:43], v[116:119], v[148:151]\n" \
  "v_mfma_f32_16x16x32_bf16 v[152:155], a[44:47], v[112:115], v[152:155]\n" \
  "v_mfma_f32_16x16x32_bf16 v[156:159], a[44:47], v[116:119], v[156:159]\n" \
  "s_waitcnt lgkmcnt(4)\n" \
  "v_mfma_f32_16x16x32_bf16 v[128:131], a[48:51], v[120:123], v[128:131]\n" \
  "ds_read_b128 v[116:119], v16 offset:26624\n" \
  "v_mfma_f32_16x16x32_bf16 v[132:135], a[48:51], v[124:127], v[132:135]\n" \
  "ds_read_b128 v[112:115], v16 offset:6144\n" \
  "v_mfma_f32_16x16x32_bf16 v[136:139], a[52:55], v[120:123], v[136:139]\n" \
  "v_cvt_pk_bf16_f32 v95, v190, v191\n" \
  "v_mfma_f32_16x16x32_bf16 v[140:143], a[52:55], v[124:127], v[140:143]\n" \
  "s_add_u32 s64, s61, 16781312\n" \
  "v_mfma_f32_16x16x32_bf16 v[144:147], a[56:59], v[120:123], v[144:147]\n" \
  "buffer_store_dwordx4 v[92:95], %[l16], s[40:43], s64 offen sc1\n" \
  "v_mfma_f32_16x16x32_bf16 v[148:151], a[56:59], v[124:127], v[148:151]\n" \
  "s_add_u32 s61, s61, 8192\n" \
  "v_mfma_f32_16x16x32_bf16 v[152:155], a[60:63], v[120:123], v[152:155]\n" \
  "v_mfma_f32_16x16x32_bf16 v[156:159], a[60:63], v[124:127], v[156:159]\n" \
  "s_waitcnt lgkmcnt(4)\n" \
  "v_mfma_f32_16x16x32_bf16 v[128:131], a[64:67], v[96:99], v[128:131]\n" \
  "ds_read_b128 v[124:127], v16 offset:27648\n" \
  "v_mfma_f32_16x16x32_bf16 v[132:135], a[64:67], v[100:103], v[132:135]\n" \
  "ds_read_b128 v[120:123], v16 offset:7168\n" \
  "v_mfma_f32_16x16x32_bf16 v[136:139], a[68:71], v[96:99], v[136:139]\n" \
  "v_mfma_f32_16x16x32_bf16 v[140:143], a[68:71], v[100:103], v[140:143]\n" \
  "v_mfma_f32_16x16x32_bf16 v[144:147], a[72:75], v[96:99], v[144:147]\n" \
  "v_mfma_f32_16x16x32_bf16 v[148:151], a[72:75], v[100:103], v[148:151]\n" \
  "v_mfma_f32_16x16x32_bf16 v[152:155], a[76:79], v[96:99], v[152:155]\n" \
  "v_mfma_f32_16x16x32_bf16 v[156:159], a[76:79], v[100:103], v[156:159]\n" \
  "s_waitcnt lgkmcnt(4)\n" \
  "v_mfma_f32_16x16x32_bf16 v[128:131], a[80:83], v[104:107], v[128:131]\n" \
  "ds_read_b128 v[100:103], v16 offset:28672\n" \
  "v_mfma_f32_16x16x32_bf16 v[132:135], a[80:83], v[108:111], v[132:135]\n" \
  "ds_read_b128 v[96:99], v16 offset:8192\n" \
  "v_mfma_f32_16x16x32_bf16 v[136:139], a[84:87], v[104:107], v[136:139]\n" \
  "v_mfma_f32_16x16x32_bf16 v[140:143], a[84:87], v[108:111], v[140:143]\n" \
  "v_mfma_f32_16x16x32_bf16 v[144:147], a[88:91], v[104:107], v[144:147]\n" \
  "v_mfma_f32_16x16x32_bf16 v[148:151], a[88:91], v[108:111], v[148:151]\n" \
  "v_mfma_f32_16x16x32_bf16 v[152:155], a[92:95], v[104:107], v[152:155]\n" \
  "v_mfma_f32_16x16x32_bf16 v[156:159], a[92:95], v[108:111], v[156:159]\n" \
  "s_waitcnt lgkmcnt(4)\n" \
  "v_mfma_f32_16x16x32_bf16 v[128:131], a[96:99], v[112:115], v[128:131]\n" \
  "ds_read_b128 v[108:111], v16 offset:29696\n" \
  "v_mfma_f32_16x16x32_bf16 v[132:135], a[96:99], v[116:119], v[132:135]\n" \
  "ds_read_b128 v[104:107], v16 offset:9216\n" \
  "v_mfma_f32_16x16x32_bf16 v[136:139], a[100:103], v[112:115], v[136:139]\n" \
  "v_mfma_f32_16x16x32_bf16 v[140:143], a[100:103], v[116:119], v[140:143]\n" \
  "v_mfma_f32_16x16x32_bf16 v[144:147], a[104:107], v[112:115], v[144:147]\n" \
  "v_mfma_f32_16x16x32_bf16 v[148:151], a[104:107], v[116:119], v[148:151]\n" \
  "v_mfma_f32_16x16x32_bf16 v[152:155], a[108:111], v[112:115], v[152:155]\n" \
  "v_mfma_f32_16x16x32_bf16 v[156:159], a[108:111], v[116:119], v[156:159]\n" \
  "s_waitcnt lgkmcnt(4)\n" \
  "v_mfma_f32_16x16x32_bf16 v[128:131], a[112:115], v[120:123], v[128:131]\n" \
  "ds_read_b128 v[116:119], v16 offset:30720\n" \
  "v_mfma_f32_16x16x32_bf16 v[132:135], a[112:115], v[124:127], v[132:135]\n" \
  "ds_read_b128 v[112:115], v16 offset:10240\n" \
  "v_mfma_f32_16x16x32_bf16 v[136:139], a[116:119], v[120:123], v[136:139]\n" \
  "v_mfma_f32_16x16x32_bf16 v[140:143], a[116:119], v[124:127], v[140:143]\n" \
  "v_mfma_f32_16x16x32_bf16 v[144:147], a[120:123], v[120:123], v[144:147]\n" \
  "v_mfma_f32_16x16x32_bf16 v[148:151], a[120:123], v[124:127], v[148:151]\n" \
  "v_mfma_f32_16x16x32_bf16 v[152:155], a[124:127], v[120:123], v[152:155]\n" \
  "v_mfma_f32_16x16x32_bf16 v[156:159], a[124:127], v[124:127], v[156:159]\n" \
  "s_waitcnt lgkmcnt(4)\n" \
  "v_mfma_f32_16x16x32_bf16 v[128:131], a[128:131], v[96:99], v[128:131]\n" \
  "ds_read_b128 v[124:127], v16 offset:31744\n" \
  "v_mfma_f32_16x16x32_bf16 v[132:135], a[128:131], v[100:103], v[132:135]\n" \
  "ds_read_b128 v[120:123], v16 offset:11264\n" \
  "v_mfma_f32_16x16x32_bf16 v[136:139], a[132:135], v[96:99], v[136:139]\n" \
  "v_mfma_f32_16x16x32_bf16 v[140:143], a[132:135], v[100:103], v[140:143]\n" \
  "v_mfma_f32_16x16x32_bf16 v[144:147], a[136:139], v[96:99], v[144:147]\n" \
  "v_mfma_f32_16x16x32_bf16 v[148:151], a[136:139], v[100:103], v[148:151]\n" \
  "v_mfma_f32_16x16x32_bf16 v[152:155], a[140:143], v[96:99], v[152:155]\n" \
  "v_mfma_f32_16x16x32_bf16 v[156:159], a[140:143], v[100:103], v[156:159]\n" \
  "s_waitcnt lgkmcnt(4)\n" \
  "v_mfma_f32_16x16x32_bf16 v[128:131], a[144:147], v[104:107], v[128:131]\n" \
  "ds_read_b128 v[100:103], v16 offset:32768\n" \
  "v_mfma_f32_16x16x32_bf16 v[132:135], a[144:147], v[108:111], v[132:135]\n" \
  "ds_read_b128 v[96:99], v16 offset:12288\n" \
  "v_mfma_f32_16x16x32_bf16 v[136:139], a[148:151], v[104:107], v[136:139]\n" \
  "s_waitcnt vmcnt(5)\n" \
  "v_mfma_f32_16x16x32_bf16 v[140:143], a[148:151], v[108:111], v[140:143]\n" \
  "s_barrier\n" \
  "v_mfma_f32_16x16x32_bf16 v[144:147], a[152:155], v[104:107], v[144:147]\n" \
  "s_add_u32 s58, s56, s59\n" \
  "v_mfma_f32_16x16x32_bf16 v[148:151], a[152:155], v[108:111], v[148:151]\n" \
  "v_add_u32 v17, s60, %[l16]\n" \
  "v_mfma_f32_16x16x32_bf16 v[152:155], a[156:159], v[104:107], v[152:155]\n" \
  "v_mfma_f32_16x16x32_bf16 v[156:159], a[156:159], v[108:111], v[156:159]\n" \
  "s_waitcnt lgkmcnt(4)\n" \
  "v_mfma_f32_16x16x32_bf16 v[128:131], a[160:163], v[112:115], v[128:131]\n" \
  "ds_read_b128 v[108:111], v16 offset:33792\n" \
  "v_mfma_f32_16x16x32_bf16 v[132:135], a[160:163], v[116:119], v[132:135]\n" \
  "ds_read_b128 v[104:107], v16 offset:13312\n" \
  "v_mfma_f32_16x16x32_bf16 v[136:139], a[164:167], v[112:115], v[136:139]\n" \
  "s_add_u32 m0, s58, 0\n" \
  "v_mfma_f32_16x16x32_bf16 v[140:143], a[164:167], v[116:119], v[140:143]\n" \
  "s_add_u32 s53, s57, 0\n" \
  "v_mfma_f32_16x16x32_bf16 v[144:147], a[168:171], v[112:115], v[144:147]\n" \
  "buffer_load_dwordx4 %[l16], s[40:43], s53 offen offset:0 lds\n" \
  "v_mfma_f32_16x16x32_bf16 v[148:151], a[168:171], v[116:119], v[148:151]\n" \
  "buffer_load_dwordx4 %[l16], s[40:43], s53 offen offset:1024 lds\n" \
  "v_mfma_f32_16x16x32_bf16 v[152:155], a[172:175], v[112:115], v[152:155]\n" \
  "buffer_load_dwordx4 %[l16], s[40:43], s53 offen offset:2048 lds\n" \
  "v_mfma_f32_16x16x32_bf16 v[156:159], a[172:175], v[116:119], v[156:159]\n" \
  "buffer_load_dwordx4 %[l16], s[40:43], s53 offen offset:3072 lds\n" \
  "s_waitcnt lgkmcnt(4)\n" \
  "v_mfma_f32_16x16x32_bf16 v[128:131], a[176:179], v[120:123], v[128:131]\n" \
  "ds_read_b128 v[116:119], v16 offset:34816\n" \
  "v_mfma_f32_16x16x32_bf16 v[132:135], a[176:179], v[124:127], v[132:135]\n" \
  "ds_read_b128 v[112:115], v16 offset:14336\n" \
  "v_mfma_f32_16x16x32_bf16 v[136:139], a[180:183], v[120:123], v[136:139]\n" \
  "s_add_u32 m0, s58, 4096\n" \
  "v_mfma_f32_16x16x32_bf16 v[140:143], a[180:183], v[124:127], v[140:143]\n" \
  "s_add_u32 s53, s57, 4096\n" \
  "v_mfma_f32_16x16x32_bf16 v[144:147], a[184:187], v[120:123], v[144:147]\n" \
  "buffer_load_dwordx4 %[l16], s[40:43], s53 offen offset:0 lds\n" \
  "v_mfma_f32_16x16x32_bf16 v[148:151], a[184:187], v[124:127], v[148:151]\n" \
  "buffer_load_dwordx4 %[l16], s[40:43], s53 offen offset:1024 lds\n" \
  "v_mfma_f32_16x16x32_bf16 v[152:155], a[188:191], v[120:123], v[152:155]\n" \
  "buffer_load_dwordx4 %[l16], s[40:43], s53 offen offset:2048 lds\n" \
  "v_mfma_f32_16x16x32_bf16 v[156:159], a[188:191], v[124:127], v[156:159]\n" \
  "buffer_load_dwordx4 %[l16], s[40:43], s53 offen offset:3072 lds\n" \
  "s_waitcnt lgkmcnt(4)\n" \
  "v_mfma_f32_16x16x32_bf16 v[128:131], a[192:195], v[96:99], v[128:131]\n" \
  "ds_read_b128 v[124:127], v16 offset:35840\n" \
  "v_mfma_f32_16x16x32_bf16 v[132:135], a[192:195], v[100:103], v[132:135]\n" \
  "ds_read_b128 v[120:123], v16 offset:15360\n" \
  "v_mfma_f32_16x16x32_bf16 v[136:139], a[196:199], v[96:99], v[136:139]\n" \
  "s_add_u32 m0, s58, 8192\n" \
  "v_mfma_f32_16x16x32_bf16 v[140:143], a[196:199], v[100:103], v[140:143]\n" \
  "s_add_u32 s53, s57, 8192\n" \
  "v_mfma_f32_16x16x32_bf16 v[144:147], a[200:203], v[96:99], v[144:147]\n" \
  "buffer_load_dwordx4 %[l16], s[40:43], s53 offen offset:0 lds\n" \
  "v_mfma_f32_16x16x32_bf16 v[148:151], a[200:203], v[100:103], v[148:151]\n" \
  "buffer_load_dwordx4 %[l16], s[40:43], s53 offen offset:1024 lds\n" \
  "v_mfma_f32_16x16x32_bf16 v[152:155], a[204:207], v[96:99], v[152:155]\n" \
  "v_mfma_f32_16x16x32_bf16 v[156:159], a[204:207], v[100:103], v[156:159]\n" \
  "s_waitcnt lgkmcnt(4)\n" \
  "v_mfma_f32_16x16x32_bf16 v[128:131], a[208:211], v[104:107], v[128:131]\n" \
  "ds_read_b128 v[100:103], v16 offset:36864\n" \
  "v_mfma_f32_16x16x32_bf16 v[132:135], a[208:211], v[108:111], v[132:135]\n" \
  "ds_read_b128 v[96:99], v16 offset:16384\n" \
  "v_mfma_f32_16x16x32_bf16 v[136:139], a[212:215], v[104:107], v[136:139]\n" \
  "s_add_u32 s57, s57, 40960\n" \
  "v_mfma_f32_16x16x32_bf16 v[140:143], a[212:215], v[108:111], v[140:143]\n" \
  "s_mov_b32 s60, s59\n" \
  "v_mfma_f32_16x16x32_bf16 v[144:147], a[216:219], v[104:107], v[144:147]\n" \
  "s_add_u32 s59, s59, 40960\n" \
  "v_mfma_f32_16x16x32_bf16 v[148:151], a[216:219], v[108:111], v[148:151]\n" \
  "s_cmp_eq_u32 s59, 122880\n" \
  "v_mfma_f32_16x16x32_bf16 v[152:155], a[220:223], v[104:107], v[152:155]\n" \
  "s_cselect_b32 s59, 0, s59\n" \
  "v_mfma_f32_16x16x32_bf16 v[156:159], a[220:223], v[108:111], v[156:159]\n" \
  "s_waitcnt lgkmcnt(4)\n" \
  "v_mfma_f32_16x16x32_bf16 v[128:131], a[224:227], v[112:115], v[128:131]\n" \
  "ds_read_b128 v[108:111], v16 offset:37888\n" \
  "v_mfma_f32_16x16x32_bf16 v[132:135], a[224:227], v[116:119], v[132:135]\n" \
  "ds_read_b128 v[104:107], v16 offset:17408\n" \
  "v_mfma_f32_16x16x32_bf16 v[136:139], a[228:231], v[112:115], v[136:139]\n" \
  "s_add_u32 s62, s62, 1\n" \
  "v_mfma_f32_16x16x32_bf16 v[140:143], a[228:231], v[116:119], v[140:143]\n" \
  "s_waitcnt vmcnt(10)\n" \
  "v_mfma_f32_16x16x32_bf16 v[144:147], a[232:235], v[112:115], v[144:147]\n" \
  "v_mov_b32 v18, s62\n" \
  "v_mfma_f32_16x16x32_bf16 v[148:151], a[232:235], v[116:119], v[148:151]\n" \
  "buffer_store_dword v18, %[fo], s[40:43], s63 offen sc1\n" \
  "v_mfma_f32_16x16x32_bf16 v[152:155], a[236:239], v[112:115], v[152:155]\n" \
  "v_mfma_f32_16x16x32_bf16 v[156:159], a[236:239], v[116:119], v[156:159]\n" \
  "s_waitcnt lgkmcnt(4)\n" \
  "v_mfma_f32_16x16x32_bf16 v[128:131], a[240:243], v[120:123], v[128:131]\n" \
  "ds_read_b128 v[116:119], v16 offset:38912\n" \
  "v_mfma_f32_16x16x32_bf16 v[132:135], a[240:243], v[124:127], v[132:135]\n" \
  "ds_read_b128 v[112:115], v16 offset:18432\n" \
  "v_mfma_f32_16x16x32_bf16 v[136:139], a[244:247], v[120:123], v[136:139]\n" \
  "v_mfma_f32_16x16x32_bf16 v[140:143], a[244:247], v[124:127], v[140:143]\n" \
  "v_mfma_f32_16x16x32_bf16 v[144:147], a[248:251], v[120:123], v[144:147]\n" \
  "v_mfma_f32_16x16x32_bf16 v[148:151], a[248:251], v[124:127], v[148:151]\n" \
  "v_mfma_f32_16x16x32_bf16 v[152:155], a[252:255], v[120:123], v[152:155]\n" \
  "v_mfma_f32_16x16x32_bf16 v[156:159], a[252:255], v[124:127], v[156:159]\n" \
  "s_waitcnt lgkmcnt(4)\n" \
  "v_mfma_f32_16x16x32_bf16 v[128:131], v[192:195], v[96:99], v[128:131]\n" \
  "ds_read_b128 v[124:127], v16 offset:39936\n" \
  "v_mfma_f32_16x16x32_bf16 v[132:135], v[192:195], v[100:103], v[132:135]\n" \
  "ds_read_b128 v[120:123], v16 offset:19456\n" \
  "v_mfma_f32_16x16x32_bf16 v[136:139], v[196:199], v[96:99], v[136:139]\n" \
  "v_mfma_f32_16x16x32_bf16 v[140:143], v[196:199], v[100:103], v[140:143]\n" \
  "v_mfma_f32_16x16x32_bf16 v[144:147], v[200:203], v[96:99], v[144:147]\n" \
  "v_mfma_f32_16x16x32_bf16 v[148:151], v[200:203], v[100:103], v[148:151]\n" \
  "v_mfma_f32_16x16x32_bf16 v[152:155], v[204:207], v[96:99], v[152:155]\n" \
  "v_mfma_f32_16x16x32_bf16 v[156:159], v[204:207], v[100:103], v[156:159]\n" \
  "s_waitcnt lgkmcnt(4)\n" \
  "v_mfma_f32_16x16x32_bf16 v[128:131], v[208:211], v[104:107], v[128:131]\n" \
  "ds_read_b128 v[100:103], v17 offset:20480\n" \
  "v_mfma_f32_16x16x32_bf16 v[132:135], v[208:211], v[108:111], v[132:135]\n" \
  "ds_read_b128 v[96:99], v17 offset:0\n" \
  "v_mfma_f32_16x16x32_bf16 v[136:139], v[212:215], v[104:107], v[136:139]\n" \
  "v_mfma_f32_16x16x32_bf16 v[140:143], v[212:215], v[108:111], v[140:143]\n" \
  "v_mfma_f32_16x16x32_bf16 v[144:147], v[216:219], v[104:107], v[144:147]\n" \
  "v_mfma_f32_16x16x32_bf16 v[148:151], v[216:219], v[108:111], v[148:151]\n" \
  "v_mfma_f32_16x16x32_bf16 v[152:155], v[220:223], v[104:107], v[152:155]\n" \
  "v_mfma_f32_16x16x32_bf16 v[156:159], v[220:223], v[108:111], v[156:159]\n" \
  "s_waitcnt lgkmcnt(4)\n" \
  "v_mfma_f32_16x16x32_bf16 v[128:131], v[224:227], v[112:115], v[128:131]\n" \
  "ds_read_b128 v[108:111], v17 offset:21504\n" \
  "v_mfma_f32_16x16x32_bf16 v[132:135], v[224:227], v[116:119], v[132:135]\n" \
  "ds_read_b128 v[104:107], v17 offset:1024\n" \
  "v_mfma_f32_16x16x32_bf16 v[136:139], v[228:231], v[112:115], v[136:139]\n" \
  "v_mfma_f32_16x16x32_bf16 v[140:143], v[228:231], v[116:119], v[140:143]\n" \
  "v_mfma_f32_16x16x32_bf16 v[144:147], v[232:235], v[112:115], v[144:147]\n" \
  "v_mfma_f32_16x16x32_bf16 v[148:151], v[232:235], v[116:119], v[148:151]\n" \
  "v_mfma_f32_16x16x32_bf16 v[152:155], v[236:239], v[112:115], v[152:155]\n" \
  "v_mfma_f32_16x16x32_bf16 v[156:159], v[236:239], v[116:119], v[156:159]\n" \
  "s_waitcnt lgkmcnt(4)\n" \
  "v_mfma_f32_16x16x32_bf16 v[128:131], v[240:243], v[120:123], v[128:131]\n" \
  "ds_read_b128 v[116:119], v17 offset:22528\n" \
  "v_mfma_f32_16x16x32_bf16 v[132:135], v[240:243], v[124:127], v[132:135]\n" \
  "ds_read_b128 v[112:115], v17 offset:2048\n" \
  "v_mfma_f32_16x16x32_bf16 v[136:139], v[244:247], v[120:123], v[136:139]\n" \
  "v_mfma_f32_16x16x32_bf16 v[140:143], v[244:247], v[124:127], v[140:143]\n" \
  "v_mfma_f32_16x16x32_bf16 v[144:147], v[248:251], v[120:123], v[144:147]\n" \
  "v_mfma_f32_16x16x32_bf16 v[148:151], v[248:251], v[124:127], v[148:151]\n" \
  "v_mfma_f32_16x16x32_bf16 v[152:155], v[252:255], v[120:123], v[152:155]\n" \
  "v_mfma_f32_16x16x32_bf16 v[156:159], v[252:255], v[124:127], v[156:159]\n" \
  "s_sub_u32 s65, s65, 1\n" \
  "s_cmp_lg_u32 s65, 0\n" \
  "s_cbranch_scc1 Lqk%=\n" \
  "s_waitcnt lgkmcnt(4)\n" \
  "v_mfma_f32_16x16x32_bf16 v[160:163], a[0:3], v[96:99], 0\n" \
  "ds_read_b128 v[124:127], v17 offset:23552\n" \
  "ds_read_b128 v[120:123], v17 offset:3072\n" \
  "v_mfma_f32_16x16x32_bf16 v[164:167], a[0:3], v[100:103], 0\n" \
  "v_cvt_pk_bf16_f32 v80, v128, v129\n" \
  "v_cvt_pk_bf16_f32 v81, v130, v131\n" \
  "v_mfma_f32_16x16x32_bf16 v[168:171], a[4:7], v[96:99], 0\n" \
  "v_cvt_pk_bf16_f32 v82, v136, v137\n" \
  "v_cvt_pk_bf16_f32 v83, v138, v139\n" \
  "v_mfma_f32_16x16x32_bf16 v[172:175], a[4:7], v[100:103], 0\n" \
  "s_add_u32 s64, s61, 0\n" \
  "buffer_store_dwordx4 v[80:83], %[l16], s[40:43], s64 offen sc1\n" \
  "v_mfma_f32_16x16x32_bf16 v[176:179], a[8:11], v[96:99], 0\n" \
  "v_cvt_pk_bf16_f32 v84, v132, v133\n" \
  "v_mfma_f32_16x16x32_bf16 v[180:183], a[8:11], v[100:103], 0\n" \
  "v_mfma_f32_16x16x32_bf16 v[184:187], a[12:15], v[96:99], 0\n" \
  "v_mfma_f32_16x16x32_bf16 v[188:191], a[12:15], v[100:103], 0\n" \
  "s_waitcnt lgkmcnt(4)\n" \
  "v_mfma_f32_16x16x32_bf16 v[160:163], a[16:19], v[104:107], v[160:163]\n" \
  "ds_read_b128 v[100:103], v17 offset:24576\n" \
  "ds_read_b128 v[96:99], v17 offset:4096\n" \
  "v_mfma_f32_16x16x32_bf16 v[164:167], a[16:19], v[108:111], v[164:167]\n" \
  "v_cvt_pk_bf16_f32 v85, v134, v135\n" \
  "v_cvt_pk_bf16_f32 v86, v140, v141\n" \
  "v_mfma_f32_16x16x32_bf16 v[168:171], a[20:23], v[104:107], v[168:171]\n" \
  "v_cvt_pk_bf16_f32 v87, v142, v143\n" \
  "s_add_u32 s64, s61, 4096\n" \
  "v_mfma_f32_16x16x32_bf16 v[172:175], a[20:23], v[108:111], v[172:175]\n" \
  "buffer_store_dwordx4 v[84:87], %[l16], s[40:43], s64 offen sc1\n" \
  "v_cvt_pk_bf16_f32 v88, v144, v145\n" \
  "v_mfma_f32_16x16x32_bf16 v[176:179], a[24:27], v[104:107], v[176:179]\n" \
  "v_cvt_pk_bf16_f32 v89, v146, v147\n" \
  "v_mfma_f32_16x16x32_bf16 v[180:183], a[24:27], v[108:111], v[180:183]\n" \
  "v_mfma_f32_16x16x32_bf16 v[184:187], a[28:31], v[104:107], v[184:187]\n" \
  "v_mfma_f32_16x16x32_bf16 v[188:191], a[28:31], v[108:111], v[188:191]\n" \
  "s_waitcnt lgkmcnt(4)\n" \
  "v_mfma_f32_16x16x32_bf16 v[160:163], a[32:35], v[112:115], v[160:163]\n" \
  "ds_read_b128 v[108:111], v17 offset:25600\n" \
  "ds_read_b128 v[104:107], v17 offset:5120\n" \
  "v_mfma_f32_16x16x32_bf16 v[164:167], a[32:35], v[116:119], v[164:167]\n" \
  "v_cvt_pk_bf16_f32 v90, v152, v153\n" \
  "v_cvt_pk_bf16_f32 v91, v154, v155\n" \
  "v_mfma_f32_16x16x32_bf16 v[168:171], a[36:39], v[112:115], v[168:171]\n" \
  "s_add_u32 s64, s61, 16777216\n" \
  "buffer_store_dwordx4 v[88:91], %[l16], s[40:43], s64 offen sc1\n" \
  "v_mfma_f32_16x16x32_bf16 v[172:175], a[36:39], v[116:119], v[172:175]\n" \
  "v_cvt_pk_bf16_f32 v92, v148, v149\n" \
  "v_cvt_pk_bf16_f32 v93, v150, v151\n" \
  "v_mfma_f32_16x16x32_bf16 v[176:179], a[40:43], v[112:115], v[176:179]\n" \
  "v_cvt_pk_bf16_f32 v94, v156, v157\n" \
  "v_mfma_f32_16x16x32_bf16 v[180:183], a[40:43], v[116:119], v[180:183]\n" \
  "v_mfma_f32_16x16x32_bf16 v[184:187], a[44:47], v[112:115], v[184:187]\n" \
  "v_mfma_f32_16x16x32_bf16 v[188:191], a[44:47], v[116:119], v[188:191]\n" \
  "s_waitcnt lgkmcnt(4)\n" \
  "v_mfma_f32_16x16x32_bf16 v[160:163], a[48:51], v[120:123], v[160:163]\n" \
  "ds_read_b128 v[116:119], v17 offset:26624\n" \
  "v_mfma_f32_16x16x32_bf16 v[164:167], a[48:51], v[124:127], v[164:167]\n" \
  "ds_read_b128 v[112:115], v17 offset:6144\n" \
  "v_mfma_f32_16x16x32_bf16 v[168:171], a[52:55], v[120:123], v[168:171]\n" \
  "v_cvt_pk_bf16_f32 v95, v158, v159\n" \
  "v_mfma_f32_16x16x32_bf16 v[172:175], a[52:55], v[124:127], v[172:175]\n" \
  "s_add_u32 s64, s61, 16781312\n" \
  "v_mfma_f32_16x16x32_bf16 v[176:179], a[56:59], v[120:123], v[176:179]\n" \
  "buffer_store_dwordx4 v[92:95], %[l16], s[40:43], s64 offen sc1\n" \
  "v_mfma_f32_16x16x32_bf16 v[180:183], a[56:59], v[124:127], v[180:183]\n" \
  "s_add_u32 s61, s61, 8192\n" \
  "v_mfma_f32_16x16x32_bf16 v[184:187], a[60:63], v[120:123], v[184:187]\n" \
  "v_mfma_f32_16x16x32_bf16 v[188:191], a[60:63], v[124:127], v[188:191]\n" \
  "s_waitcnt lgkmcnt(4)\n" \
  "v_mfma_f32_16x16x32_bf16 v[160:163], a[64:67], v[96:99], v[160:163]\n" \
  "ds_read_b128 v[124:127], v17 offset:27648\n" \
  "v_mfma_f32_16x16x32_bf16 v[164:167], a[64:67], v[100:103], v[164:167]\n" \
  "ds_read_b128 v[120:123], v17 offset:7168\n" \
  "v_mfma_f32_16x16x32_bf16 v[168:171], a[68:71], v[96:99], v[168:171]\n" \
  "v_mfma_f32_16x16x32_bf16 v[172:175], a[68:71], v[100:103], v[172:175]\n" \
  "v_mfma_f32_16x16x32_bf16 v[176:179], a[72:75], v[96:99], v[176:179]\n" \
  "v_mfma_f32_16x16x32_bf16 v[180:183], a[72:75], v[100:103], v[180:183]\n" \
  "v_mfma_f32_16x16x32_bf16 v[184:187], a[76:79], v[96:99], v[184:187]\n" \
  "v_mfma_f32_16x16x32_bf16 v[188:191], a[76:79], v[100:103], v[188:191]\n" \
  "s_waitcnt lgkmcnt(4)\n" \
  "v_mfma_f32_16x16x32_bf16 v[160:163], a[80:83], v[104:107], v[160:163]\n" \
  "ds_read_b128 v[100:103], v17 offset:28672\n" \
  "v_mfma_f32_16x16x32_bf16 v[164:167], a[80:83], v[108:111], v[164:167]\n" \
  "ds_read_b128 v[96:99], v17 offset:8192\n" \
  "v_mfma_f32_16x16x32_bf16 v[168:171], a[84:87], v[104:107], v[168:171]\n" \
  "v_mfma_f32_16x16x32_bf16 v[172:175], a[84:87], v[108:111], v[172:175]\n" \
  "v_mfma_f32_16x16x32_bf16 v[176:179], a[88:91], v[104:107], v[176:179]\n" \
  "v_mfma_f32_16x16x32_bf16 v[180:183], a[88:91], v[108:111], v[180:183]\n" \
  "v_mfma_f32_16x16x32_bf16 v[184:187], a[92:95], v[104:107], v[184:187]\n" \
  "v_mfma_f32_16x16x32_bf16 v[188:191], a[92:95], v[108:111], v[188:191]\n" \
  "s_waitcnt lgkmcnt(4)\n" \
  "v_mfma_f32_16x16x32_bf16 v[160:163], a[96:99], v[112:115], v[160:163]\n" \
  "ds_read_b128 v[108:111], v17 offset:29696\n" \
  "v_mfma_f32_16x16x32_bf16 v[164:167], a[96:99], v[116:119], v[164:167]\n" \
  "ds_read_b128 v[104:107], v17 offset:9216\n" \
  "v_mfma_f32_16x16x32_bf16 v[168:171], a[100:103], v[112:115], v[168:171]\n" \
  "v_mfma_f32_16x16x32_bf16 v[172:175], a[100:103], v[116:119], v[172:175]\n" \
  "v_mfma_f32_16x16x32_bf16 v[176:179], a[104:107], v[112:115], v[176:179]\n" \
  "v_mfma_f32_16x16x32_bf16 v[180:183], a[104:107], v[116:119], v[180:183]\n" \
  "v_mfma_f32_16x16x32_bf16 v[184:187], a[108:111], v[112:115], v[184:187]\n" \
  "v_mfma_f32_16x16x32_bf16 v[188:191], a[108:111], v[116:119], v[188:191]\n" \
  "s_waitcnt lgkmcnt(4)\n" \
  "v_mfma_f32_16x16x32_bf16 v[160:163], a[112:115], v[120:123], v[160:163]\n" \
  "ds_read_b128 v[116:119], v17 offset:30720\n" \
  "v_mfma_f32_16x16x32_bf16 v[164:167], a[112:115], v[124:127], v[164:167]\n" \
  "ds_read_b128 v[112:115], v17 offset:10240\n" \
  "v_mfma_f32_16x16x32_bf16 v[168:171], a[116:119], v[120:123], v[168:171]\n" \
  "v_mfma_f32_16x16x32_bf16 v[172:175], a[116:119], v[124:127], v[172:175]\n" \
  "v_mfma_f32_16x16x32_bf16 v[176:179], a[120:123], v[120:123], v[176:179]\n" \
  "v_mfma_f32_16x16x32_bf16 v[180:183], a[120:123], v[124:127], v[180:183]\n" \
  "v_mfma_f32_16x16x32_bf16 v[184:187], a[124:127], v[120:123], v[184:187]\n" \
  "v_mfma_f32_16x16x32_bf16 v[188:191], a[124:127], v[124:127], v[188:191]\n" \
  "s_waitcnt lgkmcnt(4)\n" \
  "v_mfma_f32_16x16x32_bf16 v[160:163], a[128:131], v[96:99], v[160:163]\n" \
  "ds_read_b128 v[124:127], v17 offset:31744\n" \
  "v_mfma_f32_16x16x32_bf16 v[164:167], a[128:131], v[100:103], v[164:167]\n" \
  "ds_read_b128 v[120:123], v17 offset:11264\n" \
  "v_mfma_f32_16x16x32_bf16 v[168:171], a[132:135], v[96:99], v[168:171]\n" \
  "v_mfma_f32_16x16x32_bf16 v[172:175], a[132:135], v[100:103], v[172:175]\n" \
  "v_mfma_f32_16x16x32_bf16 v[176:179], a[136:139], v[96:99], v[176:179]\n" \
  "v_mfma_f32_16x16x32_bf16 v[180:183], a[136:139], v[100:103], v[180:183]\n" \
  "v_mfma_f32_16x16x32_bf16 v[184:187], a[140:143], v[96:99], v[184:187]\n" \
  "v_mfma_f32_16x16x32_bf16 v[188:191], a[140:143], v[100:103], v[188:191]\n" \
  "s_waitcnt lgkmcnt(4)\n" \
  "v_mfma_f32_16x16x32_bf16 v[160:163], a[144:147], v[104:107], v[160:163]\n" \
  "ds_read_b128 v[100:103], v17 offset:32768\n" \
  "v_mfma_f32_16x16x32_bf16 v[164:167], a[144:147], v[108:111], v[164:167]\n" \
  "ds_read_b128 v[96:99], v17 offset:12288\n" \
  "v_mfma_f32_16x16x32_bf16 v[168:171], a[148:151], v[104:107], v[168:171]\n" \
  "v_mfma_f32_16x16x32_bf16 v[172:175], a[148:151], v[108:111], v[172:175]\n" \
  "v_mfma_f32_16x16x32_bf16 v[176:179], a[152:155], v[104:107], v[176:179]\n" \
  "v_mfma_f32_16x16x32_bf16 v[180:183], a[152:155], v[108:111], v[180:183]\n" \
  "v_mfma_f32_16x16x32_bf16 v[184:187], a[156:159], v[104:107], v[184:187]\n" \
  "v_mfma_f32_16x16x32_bf16 v[188:191], a[156:159], v[108:111], v[188:191]\n" \
  "s_waitcnt lgkmcnt(4)\n" \
  "v_mfma_f32_16x16x32_bf16 v[160:163], a[160:163], v[112:115], v[160:163]\n" \
  "ds_read_b128 v[108:111], v17 offset:33792\n" \
  "v_mfma_f32_16x16x32_bf16 v[164:167], a[160:163], v[116:119], v[164:167]\n" \
  "ds_read_b128 v[104:107], v17 offset:13312\n" \
  "v_mfma_f32_16x16x32_bf16 v[168:171], a[164:167], v[112:115], v[168:171]\n" \
  "v_mfma_f32_16x16x32_bf16 v[172:175], a[164:167], v[116:119], v[172:175]\n" \
  "v_mfma_f32_16x16x32_bf16 v[176:179], a[168:171], v[112:115], v[176:179]\n" \
  "v_mfma_f32_16x16x32_bf16 v[180:183], a[168:171], v[116:119], v[180:183]\n" \
  "v_mfma_f32_16x16x32_bf16 v[184:187], a[172:175], v[112:115], v[184:187]\n" \
  "v_mfma_f32_16x16x32_bf16 v[188:191], a[172:175], v[116:119], v[188:191]\n" \
  "s_waitcnt lgkmcnt(4)\n" \
  "v_mfma_f32_16x16x32_bf16 v[160:163], a[176:179], v[120:123], v[160:163]\n" \
  "ds_read_b128 v[116:119], v17 offset:34816\n" \
  "v_mfma_f32_16x16x32_bf16 v[164:167], a[176:179], v[124:127], v[164:167]\n" \
  "ds_read_b128 v[112:115], v17 offset:14336\n" \
  "v_mfma_f32_16x16x32_bf16 v[168:171], a[180:183], v[120:123], v[168:171]\n" \
  "v_mfma_f32_16x16x32_bf16 v[172:175], a[180:183], v[124:127], v[172:175]\n" \
  "v_mfma_f32_16x16x32_bf16 v[176:179], a[184:187], v[120:123], v[176:179]\n" \
  "v_mfma_f32_16x16x32_bf16 v[180:183], a[184:187], v[124:127], v[180:183]\n" \
  "v_mfma_f32_16x16x32_bf16 v[184:187], a[188:191], v[120:123], v[184:187]\n" \
  "v_mfma_f32_16x16x32_bf16 v[188:191], a[188:191], v[124:127], v[188:191]\n" \
  "s_waitcnt lgkmcnt(4)\n" \
  "v_mfma_f32_16x16x32_bf16 v[160:163], a[192:195], v[96:99], v[160:163]\n" \
  "ds_read_b128 v[124:127], v17 offset:35840\n" \
  "v_mfma_f32_16x16x32_bf16 v[164:167], a[192:195], v[100:103], v[164:167]\n" \
  "ds_read_b128 v[120:123], v17 offset:15360\n" \
  "v_mfma_f32_16x16x32_bf16 v[168:171], a[196:199], v[96:99], v[168:171]\n" \
  "v_mfma_f32_16x16x32_bf16 v[172:175], a[196:199], v[100:103], v[172:175]\n" \
  "v_mfma_f32_16x16x32_bf16 v[176:179], a[200:203], v[96:99], v[176:179]\n" \
  "v_mfma_f32_16x16x32_bf16 v[180:183], a[200:203], v[100:103], v[180:183]\n" \
  "v_mfma_f32_16x16x32_bf16 v[184:187], a[204:207], v[96:99], v[184:187]\n" \
  "v_mfma_f32_16x16x32_bf16 v[188:191], a[204:207], v[100:103], v[188:191]\n" \
  "s_waitcnt lgkmcnt(4)\n" \
  "v_mfma_f32_16x16x32_bf16 v[160:163], a[208:211], v[104:107], v[160:163]\n" \
  "ds_read_b128 v[100:103], v17 offset:36864\n" \
  "v_mfma_f32_16x16x32_bf16 v[164:167], a[208:211], v[108:111], v[164:167]\n" \
  "ds_read_b128 v[96:99], v17 offset:16384\n" \
  "v_mfma_f32_16x16x32_bf16 v[168:171], a[212:215], v[104:107], v[168:171]\n" \
  "v_mfma_f32_16x16x32_bf16 v[172:175], a[212:215], v[108:111], v[172:175]\n" \
  "v_mfma_f32_16x16x32_bf16 v[176:179], a[216:219], v[104:107], v[176:179]\n" \
  "v_mfma_f32_16x16x32_bf16 v[180:183], a[216:219], v[108:111], v[180:183]\n" \
  "v_mfma_f32_16x16x32_bf16 v[184:187], a[220:223], v[104:107], v[184:187]\n" \
  "v_mfma_f32_16x16x32_bf16 v[188:191], a[220:223], v[108:111], v[188:191]\n" \
  "s_waitcnt lgkmcnt(4)\n" \
  "v_mfma_f32_16x16x32_bf16 v[160:163], a[224:227], v[112:115], v[160:163]\n" \
  "ds_read_b128 v[108:111], v17 offset:37888\n" \
  "v_mfma_f32_16x16x32_bf16 v[164:167], a[224:227], v[116:119], v[164:167]\n" \
  "ds_read_b128 v[104:107], v17 offset:17408\n" \
  "v_mfma_f32_16x16x32_bf16 v[168:171], a[228:231], v[112:115], v[168:171]\n" \
  "s_add_u32 s62, s62, 1\n" \
  "v_mfma_f32_16x16x32_bf16 v[172:175], a[228:231], v[116:119], v[172:175]\n" \
  "s_waitcnt vmcnt(0)\n" \
  "v_mfma_f32_16x16x32_bf16 v[176:179], a[232:235], v[112:115], v[176:179]\n" \
  "v_mov_b32 v18, s62\n" \
  "v_mfma_f32_16x16x32_bf16 v[180:183], a[232:235], v[116:119], v[180:183]\n" \
  "buffer_store_dword v18, %[fo], s[40:43], s63 offen sc1\n" \
  "v_mfma_f32_16x16x32_bf16 v[184:187], a[236:239], v[112:115], v[184:187]\n" \
  "v_mfma_f32_16x16x32_bf16 v[188:191], a[236:239], v[116:119], v[188:191]\n" \
  "s_waitcnt lgkmcnt(4)\n" \
  "v_mfma_f32_16x16x32_bf16 v[160:163], a[240:243], v[120:123], v[160:163]\n" \
  "ds_read_b128 v[116:119], v17 offset:38912\n" \
  "v_mfma_f32_16x16x32_bf16 v[164:167], a[240:243], v[124:127], v[164:167]\n" \
  "ds_read_b128 v[112:115], v17 offset:18432\n" \
  "v_mfma_f32_16x16x32_bf16 v[168:171], a[244:247], v[120:123], v[168:171]\n" \
  "v_mfma_f32_16x16x32_bf16 v[172:175], a[244:247], v[124:127], v[172:175]\n" \
  "v_mfma_f32_16x16x32_bf16 v[176:179], a[248:251], v[120:123], v[176:179]\n" \
  "v_mfma_f32_16x16x32_bf16 v[180:183], a[248:251], v[124:127], v[180:183]\n" \
  "v_mfma_f32_16x16x32_bf16 v[184:187], a[252:255], v[120:123], v[184:187]\n" \
  "v_mfma_f32_16x16x32_bf16 v[188:191], a[252:255], v[124:127], v[188:191]\n" \
  "s_waitcnt lgkmcnt(4)\n" \
  "v_mfma_f32_16x16x32_bf16 v[160:163], v[192:195], v[96:99], v[160:163]\n" \
  "ds_read_b128 v[124:127], v17 offset:39936\n" \
  "v_mfma_f32_16x16x32_bf16 v[164:167], v[192:195], v[100:103], v[164:167]\n" \
  "ds_read_b128 v[120:123], v17 offset:19456\n" \
  "v_mfma_f32_16x16x32_bf16 v[168:171], v[196:199], v[96:99], v[168:171]\n" \
  "v_mfma_f32_16x16x32_bf16 v[172:175], v[196:199], v[100:103], v[172:175]\n" \
  "v_mfma_f32_16x16x32_bf16 v[176:179], v[200:203], v[96:99], v[176:179]\n" \
  "v_mfma_f32_16x16x32_bf16 v[180:183], v[200:203], v[100:103], v[180:183]\n" \
  "v_mfma_f32_16x16x32_bf16 v[184:187], v[204:207], v[96:99], v[184:187]\n" \
  "v_mfma_f32_16x16x32_bf16 v[188:191], v[204:207], v[100:103], v[188:191]\n" \
  "s_waitcnt lgkmcnt(4)\n" \
  "v_mfma_f32_16x16x32_bf16 v[160:163], v[208:211], v[104:107], v[160:163]\n" \
  "v_mfma_f32_16x16x32_bf16 v[164:167], v[208:211], v[108:111], v[164:167]\n" \
  "v_mfma_f32_16x16x32_bf16 v[168:171], v[212:215], v[104:107], v[168:171]\n" \
  "v_mfma_f32_16x16x32_bf16 v[172:175], v[212:215], v[108:111], v[172:175]\n" \
  "v_mfma_f32_16x16x32_bf16 v[176:179], v[216:219], v[104:107], v[176:179]\n" \
  "v_mfma_f32_16x16x32_bf16 v[180:183], v[216:219], v[108:111], v[180:183]\n" \
  "v_mfma_f32_16x16x32_bf16 v[184:187], v[220:223], v[104:107], v[184:187]\n" \
  "v_mfma_f32_16x16x32_bf16 v[188:191], v[220:223], v[108:111], v[188:191]\n" \
  "s_waitcnt lgkmcnt(2)\n" \
  "v_mfma_f32_16x16x32_bf16 v[160:163], v[224:227], v[112:115], v[160:163]\n" \
  "v_mfma_f32_16x16x32_bf16 v[164:167], v[224:227], v[116:119], v[164:167]\n" \
  "v_mfma_f32_16x16x32_bf16 v[168:171], v[228:231], v[112:115], v[168:171]\n" \
  "v_mfma_f32_16x16x32_bf16 v[172:175], v[228:231], v[116:119], v[172:175]\n" \
  "v_mfma_f32_16x16x32_bf16 v[176:179], v[232:235], v[112:115], v[176:179]\n" \
  "v_mfma_f32_16x16x32_bf16 v[180:183], v[232:235], v[116:119], v[180:183]\n" \
  "v_mfma_f32_16x16x32_bf16 v[184:187], v[236:239], v[112:115], v[184:187]\n" \
  "v_mfma_f32_16x16x32_bf16 v[188:191], v[236:239], v[116:119], v[188:191]\n" \
  "s_waitcnt lgkmcnt(0)\n" \
  "v_mfma_f32_16x16x32_bf16 v[160:163], v[240:243], v[120:123], v[160:163]\n" \
  "v_mfma_f32_16x16x32_bf16 v[164:167], v[240:243], v[124:127], v[164:167]\n" \
  "v_mfma_f32_16x16x32_bf16 v[168:171], v[244:247], v[120:123], v[168:171]\n" \
  "v_mfma_f32_16x16x32_bf16 v[172:175], v[244:247], v[124:127], v[172:175]\n" \
  "v_mfma_f32_16x16x32_bf16 v[176:179], v[248:251], v[120:123], v[176:179]\n" \
  "v_mfma_f32_16x16x32_bf16 v[180:183], v[248:251], v[124:127], v[180:183]\n" \
  "v_mfma_f32_16x16x32_bf16 v[184:187], v[252:255], v[120:123], v[184:187]\n" \
  "v_mfma_f32_16x16x32_bf16 v[188:191], v[252:255], v[124:127], v[188:191]\n" \
  "v_cvt_pk_bf16_f32 v80, v160, v161\n" \
  "v_cvt_pk_bf16_f32 v81, v162, v163\n" \
  "v_cvt_pk_bf16_f32 v82, v168, v169\n" \
  "v_cvt_pk_bf16_f32 v83, v170, v171\n" \
  "s_add_u32 s64, s61, 0\n" \
  "buffer_store_dwordx4 v[80:83], %[l16], s[40:43], s64 offen sc1\n" \
  "v_cvt_pk_bf16_f32 v84, v164, v165\n" \
  "v_cvt_pk_bf16_f32 v85, v166, v167\n" \
  "v_cvt_pk_bf16_f32 v86, v172, v173\n" \
  "v_cvt_pk_bf16_f32 v87, v174, v175\n" \
  "s_add_u32 s64, s61, 4096\n" \
  "buffer_store_dwordx4 v[84:87], %[l16], s[40:43], s64 offen sc1\n" \
  "v_cvt_pk_bf16_f32 v88, v176, v177\n" \
  "v_cvt_pk_bf16_f32 v89, v178, v179\n" \
  "v_cvt_pk_bf16_f32 v90, v184, v185\n" \
  "v_cvt_pk_bf16_f32 v91, v186, v187\n" \
  "s_add_u32 s64, s61, 16777216\n" \
  "buffer_store_dwordx4 v[88:91], %[l16], s[40:43], s64 offen sc1\n" \
  "v_cvt_pk_bf16_f32 v92, v180, v181\n" \
  "v_cvt_pk_bf16_f32 v93, v182, v183\n" \
  "v_cvt_pk_bf16_f32 v94, v188, v189\n" \
  "v_cvt_pk_bf16_f32 v95, v190, v191\n" \
  "s_add_u32 s64, s61, 16781312\n" \
  "buffer_store_dwordx4 v[92:95], %[l16], s[40:43], s64 offen sc1\n" \
  "s_add_u32 s61, s61, 8192\n" \
  "s_add_u32 s62, s62, 1\n" \
  "s_waitcnt vmcnt(0)\n" \
  "v_mov_b32 v18, s62\n" \
  "buffer_store_dword v18, %[fo], s[40:43], s63 offen sc1\n" \
  "s_waitcnt vmcnt(0)\n"

#define ASM_VZ \
  "s_nop 4\n" \
  "s_mov_b64 s[40:41], %[ws]\n" \
  "s_mov_b32 s42, 0x0ff00000\n" \
  "s_mov_b32 s43, 0x20000\n" \
  "s_lshl_b32 s54, %[h], 8\n" \
  "s_lshl_b32 s55, %[w], 6\n" \
  "s_add_u32 s54, s54, s55\n" \
  "s_mov_b64 s[44:45], %[wa]\n" \
  "s_add_u32 s44, s44, s54\n" \
  "s_addc_u32 s45, s45, 0\n" \
  "s_mov_b32 s46, 0x00500000\n" \
  "s_mov_b32 s47, 0x20000\n" \
  "s_sub_u32 s46, s46, s54\n" \
  "s_mul_i32 s54, %[h], 163840\n" \
  "s_mul_i32 s55, %[w], 320\n" \
  "s_add_u32 s54, s54, s55\n" \
  "s_mov_b64 s[48:49], %[wb]\n" \
  "s_add_u32 s48, s48, s54\n" \
  "s_addc_u32 s49, s49, 0\n" \
  "s_mov_b32 s50, 0x00500000\n" \
  "s_mov_b32 s51, 0x20000\n" \
  "s_sub_u32 s50, s50, s54\n" \
  "s_mul_i32 s52, %[w], 28672\n" \
  "s_add_u32 s52, s52, 40960\n" \
  "s_mul_i32 s56, %[w], 10240\n" \
  "s_add_u32 s57, s56, 7340032\n" \
  "s_mov_b32 s53, 0\n" \
  "s_add_u32 m0, s52, 0\n" \
  "s_nop 0\n" \
  "buffer_load_dwordx4 %[wo], s[44:47], s53 offen lds\n" \
  "s_mov_b32 s53, 131072\n" \
  "s_add_u32 m0, s52, 1024\n" \
  "s_nop 0\n" \
  "buffer_load_dwordx4 %[wo], s[44:47], s53 offen lds\n" \
  "s_mov_b32 s53, 0\n" \
  "s_add_u32 m0, s52, 2048\n" \
  "s_nop 0\n" \
  "buffer_load_dwordx4 %[wo2], s[48:51], s53 offen lds\n" \
  "s_mov_b32 s53, 20480\n" \
  "s_add_u32 m0, s52, 3072\n" \
  "s_nop 0\n" \
  "buffer_load_dwordx4 %[wo2], s[48:51], s53 offen lds\n" \
  "s_mov_b32 s53, 262144\n" \
  "s_add_u32 m0, s52, 4096\n" \
  "s_nop 0\n" \
  "buffer_load_dwordx4 %[wo], s[44:47], s53 offen lds\n" \
  "s_mov_b32 s53, 393216\n" \
  "s_add_u32 m0, s52, 5120\n" \
  "s_nop 0\n" \
  "buffer_load_dwordx4 %[wo], s[44:47], s53 offen lds\n" \
  "s_mov_b32 s53, 40960\n" \
  "s_add_u32 m0, s52, 6144\n" \
  "s_nop 0\n" \
  "buffer_load_dwordx4 %[wo2], s[48:51], s53 offen lds\n" \
  "s_mov_b32 s53, 61440\n" \
  "s_add_u32 m0, s52, 7168\n" \
  "s_nop 0\n" \
  "buffer_load_dwordx4 %[wo2], s[48:51], s53 offen lds\n" \
  "s_mov_b32 s53, 524288\n" \
  "s_add_u32 m0, s52, 8192\n" \
  "s_nop 0\n" \
  "buffer_load_dwordx4 %[wo], s[44:47], s53 offen lds\n" \
  "s_mov_b32 s53, 655360\n" \
  "s_add_u32 m0, s52, 9216\n" \
  "s_nop 0\n" \
  "buffer_load_dwordx4 %[wo], s[44:47], s53 offen lds\n" \
  "s_mov_b32 s53, 81920\n" \
  "s_add_u32 m0, s52, 10240\n" \
  "s_nop 0\n" \
  "buffer_load_dwordx4 %[wo2], s[48:51], s53 offen lds\n" \
  "s_mov_b32 s53, 102400\n" \
  "s_add_u32 m0, s52, 11264\n" \
  "s_nop 0\n" \
  "buffer_load_dwordx4 %[wo2], s[48:51], s53 offen lds\n" \
  "s_mov_b32 s53, 786432\n" \
  "s_add_u32 m0, s52, 12288\n" \
  "s_nop 0\n" \
  "buffer_load_dwordx4 %[wo], s[44:47], s53 offen lds\n" \
  "s_mov_b32 s53, 917504\n" \
  "s_add_u32 m0, s52, 13312\n" \
  "s_nop 0\n" \
  "buffer_load_dwordx4 %[wo], s[44:47], s53 offen lds\n" \
  "s_mov_b32 s53, 122880\n" \
  "s_add_u32 m0, s52, 14336\n" \
  "s_nop 0\n" \
  "buffer_load_dwordx4 %[wo2], s[48:51], s53 offen lds\n" \
  "s_mov_b32 s53, 143360\n" \
  "s_add_u32 m0, s52, 15360\n" \
  "s_nop 0\n" \
  "buffer_load_dwordx4 %[wo2], s[48:51], s53 offen lds\n" \
  "s_mov_b32 s53, 1048576\n" \
  "s_add_u32 m0, s52, 16384\n" \
  "s_nop 0\n" \
  "buffer_load_dwordx4 %[wo], s[44:47], s53 offen lds\n" \
  "s_mov_b32 s53, 1179648\n" \
  "s_add_u32 m0, s52, 17408\n" \
  "s_nop 0\n" \
  "buffer_load_dwordx4 %[wo], s[44:47], s53 offen lds\n" \
  "s_mov_b32 s53, 64\n" \
  "s_add_u32 m0, s52, 18432\n" \
  "s_nop 0\n" \
  "buffer_load_dwordx4 %[wo2], s[48:51], s53 offen lds\n" \
  "s_mov_b32 s53, 20544\n" \
  "s_add_u32 m0, s52, 19456\n" \
  "s_nop 0\n" \
  "buffer_load_dwordx4 %[wo2], s[48:51], s53 offen lds\n" \
  "s_mov_b32 s53, 1310720\n" \
  "s_add_u32 m0, s52, 20480\n" \
  "s_nop 0\n" \
  "buffer_load_dwordx4 %[wo], s[44:47], s53 offen lds\n" \
  "s_mov_b32 s53, 1441792\n" \
  "s_add_u32 m0, s52, 21504\n" \
  "s_nop 0\n" \
  "buffer_load_dwordx4 %[wo], s[44:47], s53 offen lds\n" \
  "s_mov_b32 s53, 41024\n" \
  "s_add_u32 m0, s52, 22528\n" \
  "s_nop 0\n" \
  "buffer_load_dwordx4 %[wo2], s[48:51], s53 offen lds\n" \
  "s_mov_b32 s53, 61504\n" \
  "s_add_u32 m0, s52, 23552\n" \
  "s_nop 0\n" \
  "buffer_load_dwordx4 %[wo2], s[48:51], s53 offen lds\n" \
  "s_add_u32 m0, s56, 0\n" \
  "s_add_u32 s53, s57, 0\n" \
  "buffer_load_dwordx4 %[l16], s[40:43], s53 offen offset:0 lds\n" \
  "buffer_load_dwordx4 %[l16], s[40:43], s53 offen offset:1024 lds\n" \
  "buffer_load_dwordx4 %[l16], s[40:43], s53 offen offset:2048 lds\n" \
  "buffer_load_dwordx4 %[l16], s[40:43], s53 offen offset:3072 lds\n" \
  "s_add_u32 m0, s56, 4096\n" \
  "s_add_u32 s53, s57, 4096\n" \
  "buffer_load_dwordx4 %[l16], s[40:43], s53 offen offset:0 lds\n" \
  "buffer_load_dwordx4 %[l16], s[40:43], s53 offen offset:1024 lds\n" \
  "buffer_load_dwordx4 %[l16], s[40:43], s53 offen offset:2048 lds\n" \
  "buffer_load_dwordx4 %[l16], s[40:43], s53 offen offset:3072 lds\n" \
  "s_add_u32 m0, s56, 8192\n" \
  "s_add_u32 s53, s57, 8192\n" \
  "buffer_load_dwordx4 %[l16], s[40:43], s53 offen offset:0 lds\n" \
  "buffer_load_dwordx4 %[l16], s[40:43], s53 offen offset:1024 lds\n" \
  "s_waitcnt vmcnt(30)\n" \
  "ds_read_b64_tr_b16 a[0:1], %[tr] offset:0\n" \
  "ds_read_b64_tr_b16 a[2:3], %[tr] offset:256\n" \
  "ds_read_b64_tr_b16 a[4:5], %[tr] offset:32\n" \
  "ds_read_b64_tr_b16 a[6:7], %[tr] offset:288\n" \
  "ds_read_b64_tr_b16 a[160:161], %[tr2] offset:2048\n" \
  "ds_read_b64_tr_b16 a[162:163], %[tr2] offset:3072\n" \
  "ds_read_b64_tr_b16 a[176:177], %[tr2] offset:2080\n" \
  "ds_read_b64_tr_b16 a[178:179], %[tr2] offset:3104\n" \
  "s_mov_b32 s53, 1572864\n" \
  "s_add_u32 m0, s52, 24576\n" \
  "s_nop 0\n" \
  "buffer_load_dwordx4 %[wo], s[44:47], s53 offen lds\n" \
  "s_mov_b32 s53, 1703936\n" \
  "s_add_u32 m0, s52, 25600\n" \
  "s_nop 0\n" \
  "buffer_load_dwordx4 %[wo], s[44:47], s53 offen lds\n" \
  "s_mov_b32 s53, 81984\n" \
  "s_add_u32 m0, s52, 26624\n" \
  "s_nop 0\n" \
  "buffer_load_dwordx4 %[wo2], s[48:51], s53 offen lds\n" \
  "s_mov_b32 s53, 102464\n" \
  "s_add_u32 m0, s52, 27648\n" \
  "s_nop 0\n" \
  "buffer_load_dwordx4 %[wo2], s[48:51], s53 offen lds\n" \
  "s_waitcnt vmcnt(30)\n" \
  "ds_read_b64_tr_b16 a[8:9], %[tr] offset:4096\n" \
  "ds_read_b64_tr_b16 a[10:11], %[tr] offset:4352\n" \
  "ds_read_b64_tr_b16 a[12:13], %[tr] offset:4128\n" \
  "ds_read_b64_tr_b16 a[14:15], %[tr] offset:4384\n" \
  "ds_read_b64_tr_b16 a[164:165], %[tr2] offset:6144\n" \
  "ds_read_b64_tr_b16 a[166:167], %[tr2] offset:7168\n" \
  "ds_read_b64_tr_b16 a[180:181], %[tr2] offset:6176\n" \
  "s_waitcnt lgkmcnt(14)\n" \
  "ds_read_b64_tr_b16 a[182:183], %[tr2] offset:7200\n" \
  "s_waitcnt lgkmcnt(8)\n" \
  "s_mov_b32 s53, 1835008\n" \
  "s_add_u32 m0, s52, 0\n" \
  "s_nop 0\n" \
  "buffer_load_dwordx4 %[wo], s[44:47], s53 offen lds\n" \
  "s_mov_b32 s53, 1966080\n" \
  "s_add_u32 m0, s52, 1024\n" \
  "s_nop 0\n" \
  "buffer_load_dwordx4 %[wo], s[44:47], s53 offen lds\n" \
  "s_mov_b32 s53, 122944\n" \
  "s_add_u32 m0, s52, 2048\n" \
  "s_nop 0\n" \
  "buffer_load_dwordx4 %[wo2], s[48:51], s53 offen lds\n" \
  "s_mov_b32 s53, 143424\n" \
  "s_add_u32 m0, s52, 3072\n" \
  "s_nop 0\n" \
  "buffer_load_dwordx4 %[wo2], s[48:51], s53 offen lds\n" \
  "s_waitcnt vmcnt(30)\n" \
  "ds_read_b64_tr_b16 a[16:17], %[tr] offset:8192\n" \
  "ds_read_b64_tr_b16 a[18:19], %[tr] offset:8448\n" \
  "ds_read_b64_tr_b16 a[20:21], %[tr] offset:8224\n" \
  "ds_read_b64_tr_b16 a[22:23], %[tr] offset:8480\n" \
  "ds_read_b64_tr_b16 a[168:169], %[tr2] offset:10240\n" \
  "ds_read_b64_tr_b16 a[170:171], %[tr2] offset:11264\n" \
  "ds_read_b64_tr_b16 a[184:185], %[tr2] offset:10272\n" \
  "s_waitcnt lgkmcnt(14)\n" \
  "ds_read_b64_tr_b16 a[186:187], %[tr2] offset:11296\n" \
  "s_waitcnt lgkmcnt(8)\n" \
  "s_mov_b32 s53, 2097152\n" \
  "s_add_u32 m0, s52, 4096\n" \
  "s_nop 0\n" \
  "buffer_load_dwordx4 %[wo], s[44:47], s53 offen lds\n" \
  "s_mov_b32 s53, 2228224\n" \
  "s_add_u32 m0, s52, 5120\n" \
  "s_nop 0\n" \
  "buffer_load_dwordx4 %[wo], s[44:47], s53 offen lds\n" \
  "s_mov_b32 s53, 128\n" \
  "s_add_u32 m0, s52, 6144\n" \
  "s_nop 0\n" \
  "buffer_load_dwordx4 %[wo2], s[48:51], s53 offen lds\n" \
  "s_mov_b32 s53, 20608\n" \
  "s_add_u32 m0, s52, 7168\n" \
  "s_nop 0\n" \
  "buffer_load_dwordx4 %[wo2], s[48:51], s53 offen lds\n" \
  "s_waitcnt vmcnt(30)\n" \
  "ds_read_b64_tr_b16 a[24:25], %[tr] offset:12288\n" \
  "ds_read_b64_tr_b16 a[26:27], %[tr] offset:12544\n" \
  "ds_read_b64_tr_b16 a[28:29], %[tr] offset:12320\n" \
  "ds_read_b64_tr_b16 a[30:31], %[tr] offset:12576\n" \
  "ds_read_b64_tr_b16 a[172:173], %[tr2] offset:14336\n" \
  "ds_read_b64_tr_b16 a[174:175], %[tr2] offset:15360\n" \
  "ds_read_b64_tr_b16 a[188:189], %[tr2] offset:14368\n" \
  "s_waitcnt lgkmcnt(14)\n" \
  "ds_read_b64_tr_b16 a[190:191], %[tr2] offset:15392\n" \
  "s_waitcnt lgkmcnt(8)\n" \
  "s_mov_b32 s53, 2359296\n" \
  "s_add_u32 m0, s52, 8192\n" \
  "s_nop 0\n" \
  "buffer_load_dwordx4 %[wo], s[44:47], s53 offen lds\n" \
  "s_mov_b32 s53, 2490368\n" \
  "s_add_u32 m0, s52, 9216\n" \
  "s_nop 0\n" \
  "buffer_load_dwordx4 %[wo], s[44:47], s53 offen lds\n" \
  "s_mov_b32 s53, 41088\n" \
  "s_add_u32 m0, s52, 10240\n" \
  "s_nop 0\n" \
  "buffer_load_dwordx4 %[wo2], s[48:51], s53 offen lds\n" \
  "s_mov_b32 s53, 61568\n" \
  "s_add_u32 m0, s52, 11264\n" \
  "s_nop 0\n" \
  "buffer_load_dwordx4 %[wo2], s[48:51], s53 offen lds\n" \
  "s_waitcnt vmcnt(30)\n" \
  "ds_read_b64_tr_b16 a[32:33], %[tr] offset:16384\n" \
  "ds_read_b64_tr_b16 a[34:35], %[tr] offset:16640\n" \
  "ds_read_b64_tr_b16 a[36:37], %[tr] offset:16416\n" \
  "ds_read_b64_tr_b16 a[38:39], %[tr] offset:16672\n" \
  "ds_read_b64_tr_b16 a[192:193], %[tr2] offset:18432\n" \
  "ds_read_b64_tr_b16 a[194:195], %[tr2] offset:19456\n" \
  "ds_read_b64_tr_b16 a[208:209], %[tr2] offset:18464\n" \
  "s_waitcnt lgkmcnt(14)\n" \
  "ds_read_b64_tr_b16 a[210:211], %[tr2] offset:19488\n" \
  "s_waitcnt lgkmcnt(8)\n" \
  "s_mov_b32 s53, 2621440\n" \
  "s_add_u32 m0, s52, 12288\n" \
  "s_nop 0\n" \
  "buffer_load_dwordx4 %[wo], s[44:47], s53 offen lds\n" \
  "s_mov_b32 s53, 2752512\n" \
  "s_add_u32 m0, s52, 13312\n" \
  "s_nop 0\n" \
  "buffer_load_dwordx4 %[wo], s[44:47], s53 offen lds\n" \
  "s_mov_b32 s53, 82048\n" \
  "s_add_u32 m0, s52, 14336\n" \
  "s_nop 0\n" \
  "buffer_load_dwordx4 %[wo2], s[48:51], s53 offen lds\n" \
  "s_mov_b32 s53, 102528\n" \
  "s_add_u32 m0, s52, 15360\n" \
  "s_nop 0\n" \
  "buffer_load_dwordx4 %[wo2], s[48:51], s53 offen lds\n" \
  "s_waitcnt vmcnt(30)\n" \
  "ds_read_b64_tr_b16 a[40:41], %[tr] offset:20480\n" \
  "ds_read_b64_tr_b16 a[42:43], %[tr] offset:20736\n" \
  "ds_read_b64_tr_b16 a[44:45], %[tr] offset:20512\n" \
  "ds_read_b64_tr_b16 a[46:47], %[tr] offset:20768\n" \
  "ds_read_b64_tr_b16 a[196:197], %[tr2] offset:22528\n" \
  "ds_read_b64_tr_b16 a[198:199], %[tr2] offset:23552\n" \
  "ds_read_b64_tr_b16 a[212:213], %[tr2] offset:22560\n" \
  "s_waitcnt lgkmcnt(14)\n" \
  "ds_read_b64_tr_b16 a[214:215], %[tr2] offset:23584\n" \
  "s_waitcnt lgkmcnt(8)\n" \
  "s_mov_b32 s53, 2883584\n" \
  "s_add_u32 m0, s52, 16384\n" \
  "s_nop 0\n" \
  "buffer_load_dwordx4 %[wo], s[44:47], s53 offen lds\n" \
  "s_mov_b32 s53, 3014656\n" \
  "s_add_u32 m0, s52, 17408\n" \
  "s_nop 0\n" \
  "buffer_load_dwordx4 %[wo], s[44:47], s53 offen lds\n" \
  "s_mov_b32 s53, 123008\n" \
  "s_add_u32 m0, s52, 18432\n" \
  "s_nop 0\n" \
  "buffer_load_dwordx4 %[wo2], s[48:51], s53 offen lds\n" \
  "s_mov_b32 s53, 143488\n" \
  "s_add_u32 m0, s52, 19456\n" \
  "s_nop 0\n" \
  "buffer_load_dwordx4 %[wo2], s[48:51], s53 offen lds\n" \
  "s_waitcnt vmcnt(20)\n" \
  "ds_read_b64_tr_b16 a[48:49], %[tr] offset:24576\n" \
  "ds_read_b64_tr_b16 a[50:51], %[tr] offset:24832\n" \
  "ds_read_b64_tr_b16 a[52:53], %[tr] offset:24608\n" \
  "ds_read_b64_tr_b16 a[54:55], %[tr] offset:24864\n" \
  "ds_read_b64_tr_b16 a[200:201], %[tr2] offset:26624\n" \
  "ds_read_b64_tr_b16 a[202:203], %[tr2] offset:27648\n" \
  "ds_read_b64_tr_b16 a[216:217], %[tr2] offset:26656\n" \
  "s_waitcnt lgkmcnt(14)\n" \
  "ds_read_b64_tr_b16 a[218:219], %[tr2] offset:27680\n" \
  "s_waitcnt lgkmcnt(8)\n" \
  "s_mov_b32 s53, 3145728\n" \
  "s_add_u32 m0, s52, 20480\n" \
  "s_nop 0\n" \
  "buffer_load_dwordx4 %[wo], s[44:47], s53 offen lds\n" \
  "s_mov_b32 s53, 3276800\n" \
  "s_add_u32 m0, s52, 21504\n" \
  "s_nop 0\n" \
  "buffer_load_dwordx4 %[wo], s[44:47], s53 offen lds\n" \
  "s_mov_b32 s53, 192\n" \
  "s_add_u32 m0, s52, 22528\n" \
  "s_nop 0\n" \
  "buffer_load_dwordx4 %[wo2], s[48:51], s53 offen lds\n" \
  "s_mov_b32 s53, 20672\n" \
  "s_add_u32 m0, s52, 23552\n" \
  "s_nop 0\n" \
  "buffer_load_dwordx4 %[wo2], s[48:51], s53 offen lds\n" \
  "s_waitcnt vmcnt(20)\n" \
  "ds_read_b64_tr_b16 a[56:57], %[tr] offset:0\n" \
  "ds_read_b64_tr_b16 a[58:59], %[tr] offset:256\n" \
  "ds_read_b64_tr_b16 a[60:61], %[tr] offset:32\n" \
  "ds_read_b64_tr_b16 a[62:63], %[tr] offset:288\n" \
  "ds_read_b64_tr_b16 a[204:205], %[tr2] offset:2048\n" \
  "ds_read_b64_tr_b16 a[206:207], %[tr2] offset:3072\n" \
  "ds_read_b64_tr_b16 a[220:221], %[tr2] offset:2080\n" \
  "s_waitcnt lgkmcnt(14)\n" \
  "ds_read_b64_tr_b16 a[222:223], %[tr2] offset:3104\n" \
  "s_waitcnt lgkmcnt(8)\n" \
  "s_mov_b32 s53, 3407872\n" \
  "s_add_u32 m0, s52, 24576\n" \
  "s_nop 0\n" \
  "buffer_load_dwordx4 %[wo], s[44:47], s53 offen lds\n" \
  "s_mov_b32 s53, 3538944\n" \
  "s_add_u32 m0, s52, 25600\n" \
  "s_nop 0\n" \
  "buffer_load_dwordx4 %[wo], s[44:47], s53 offen lds\n" \
  "s_mov_b32 s53, 41152\n" \
  "s_add_u32 m0, s52, 26624\n" \
  "s_nop 0\n" \
  "buffer_load_dwordx4 %[wo2], s[48:51], s53 offen lds\n" \
  "s_mov_b32 s53, 61632\n" \
  "s_add_u32 m0, s52, 27648\n" \
  "s_nop 0\n" \
  "buffer_load_dwordx4 %[wo2], s[48:51], s53 offen lds\n" \
  "s_waitcnt vmcnt(20)\n" \
  "ds_read_b64_tr_b16 a[64:65], %[tr] offset:4096\n" \
  "ds_read_b64_tr_b16 a[66:67], %[tr] offset:4352\n" \
  "ds_read_b64_tr_b16 a[68:69], %[tr] offset:4128\n" \
  "ds_read_b64_tr_b16 a[70:71], %[tr] offset:4384\n" \
  "ds_read_b64_tr_b16 a[224:225], %[tr2] offset:6144\n" \
  "ds_read_b64_tr_b16 a[226:227], %[tr2] offset:7168\n" \
  "ds_read_b64_tr_b16 a[240:241], %[tr2] offset:6176\n" \
  "s_waitcnt lgkmcnt(14)\n" \
  "ds_read_b64_tr_b16 a[242:243], %[tr2] offset:7200\n" \
  "s_waitcnt lgkmcnt(8)\n" \
  "s_mov_b32 s53, 3670016\n" \
  "s_add_u32 m0, s52, 0\n" \
  "s_nop 0\n" \
  "buffer_load_dwordx4 %[wo], s[44:47], s53 offen lds\n" \
  "s_mov_b32 s53, 3801088\n" \
  "s_add_u32 m0, s52, 1024\n" \
  "s_nop 0\n" \
  "buffer_load_dwordx4 %[wo], s[44:47], s53 offen lds\n" \
  "s_mov_b32 s53, 82112\n" \
  "s_add_u32 m0, s52, 2048\n" \
  "s_nop 0\n" \
  "buffer_load_dwordx4 %[wo2], s[48:51], s53 offen lds\n" \
  "s_mov_b32 s53, 102592\n" \
  "s_add_u32 m0, s52, 3072\n" \
  "s_nop 0\n" \
  "buffer_load_dwordx4 %[wo2], s[48:51], s53 offen lds\n" \
  "s_waitcnt vmcnt(20)\n" \
  "ds_read_b64_tr_b16 a[72:73], %[tr] offset:8192\n" \
  "ds_read_b64_tr_b16 a[74:75], %[tr] offset:8448\n" \
  "ds_read_b64_tr_b16 a[76:77], %[tr] offset:8224\n" \
  "ds_read_b64_tr_b16 a[78:79], %[tr] offset:8480\n" \
  "ds_read_b64_tr_b16 a[228:229], %[tr2] offset:10240\n" \
  "ds_read_b64_tr_b16 a[230:231], %[tr2] offset:11264\n" \
  "ds_read_b64_tr_b16 a[244:245], %[tr2] offset:10272\n" \
  "s_waitcnt lgkmcnt(14)\n" \
  "ds_read_b64_tr_b16 a[246:247], %[tr2] offset:11296\n" \
  "s_waitcnt lgkmcnt(8)\n" \
  "s_mov_b32 s53, 3932160\n" \
  "s_add_u32 m0, s52, 4096\n" \
  "s_nop 0\n" \
  "buffer_load_dwordx4 %[wo], s[44:47], s53 offen lds\n" \
  "s_mov_b32 s53, 4063232\n" \
  "s_add_u32 m0, s52, 5120\n" \
  "s_nop 0\n" \
  "buffer_load_dwordx4 %[wo], s[44:47], s53 offen lds\n" \
  "s_mov_b32 s53, 123072\n" \
  "s_add_u32 m0, s52, 6144\n" \
  "s_nop 0\n" \
  "buffer_load_dwordx4 %[wo2], s[48:51], s53 offen lds\n" \
  "s_mov_b32 s53, 143552\n" \
  "s_add_u32 m0, s52, 7168\n" \
  "s_nop 0\n" \
  "buffer_load_dwordx4 %[wo2], s[48:51], s53 offen lds\n" \
  "s_waitcnt vmcnt(20)\n" \
  "ds_read_b64_tr_b16 a[80:81], %[tr] offset:12288\n" \
  "ds_read_b64_tr_b16 a[82:83], %[tr] offset:12544\n" \
  "ds_read_b64_tr_b16 a[84:85], %[tr] offset:12320\n" \
  "ds_read_b64_tr_b16 a[86:87], %[tr] offset:12576\n" \
  "ds_read_b64_tr_b16 a[232:233], %[tr2] offset:14336\n" \
  "ds_read_b64_tr_b16 a[234:235], %[tr2] offset:15360\n" \
  "ds_read_b64_tr_b16 a[248:249], %[tr2] offset:14368\n" \
  "s_waitcnt lgkmcnt(14)\n" \
  "ds_read_b64_tr_b16 a[250:251], %[tr2] offset:15392\n" \
  "s_waitcnt lgkmcnt(8)\n" \
  "s_mov_b32 s53, 4194304\n" \
  "s_add_u32 m0, s52, 8192\n" \
  "s_nop 0\n" \
  "buffer_load_dwordx4 %[wo], s[44:47], s53 offen lds\n" \
  "s_mov_b32 s53, 4325376\n" \
  "s_add_u32 m0, s52, 9216\n" \
  "s_nop 0\n" \
  "buffer_load_dwordx4 %[wo], s[44:47], s53 offen lds\n" \
  "s_mov_b32 s53, 256\n" \
  "s_add_u32 m0, s52, 10240\n" \
  "s_nop 0\n" \
  "buffer_load_dwordx4 %[wo2], s[48:51], s53 offen lds\n" \
  "s_mov_b32 s53, 20736\n" \
  "s_add_u32 m0, s52, 11264\n" \
  "s_nop 0\n" \
  "buffer_load_dwordx4 %[wo2], s[48:51], s53 offen lds\n" \
  "s_waitcnt vmcnt(20)\n" \
  "ds_read_b64_tr_b16 a[88:89], %[tr] offset:16384\n" \
  "ds_read_b64_tr_b16 a[90:91], %[tr] offset:16640\n" \
  "ds_read_b64_tr_b16 a[92:93], %[tr] offset:16416\n" \
  "ds_read_b64_tr_b16 a[94:95], %[tr] offset:16672\n" \
  "ds_read_b64_tr_b16 a[236:237], %[tr2] offset:18432\n" \
  "ds_read_b64_tr_b16 a[238:239], %[tr2] offset:19456\n" \
  "ds_read_b64_tr_b16 a[252:253], %[tr2] offset:18464\n" \
  "s_waitcnt lgkmcnt(14)\n" \
  "ds_read_b64_tr_b16 a[254:255], %[tr2] offset:19488\n" \
  "s_waitcnt lgkmcnt(8)\n" \
  "s_mov_b32 s53, 4456448\n" \
  "s_add_u32 m0, s52, 12288\n" \
  "s_nop 0\n" \
  "buffer_load_dwordx4 %[wo], s[44:47], s53 offen lds\n" \
  "s_mov_b32 s53, 4587520\n" \
  "s_add_u32 m0, s52, 13312\n" \
  "s_nop 0\n" \
  "buffer_load_dwordx4 %[wo], s[44:47], s53 offen lds\n" \
  "s_mov_b32 s53, 41216\n" \
  "s_add_u32 m0, s52, 14336\n" \
  "s_nop 0\n" \
  "buffer_load_dwordx4 %[wo2], s[48:51], s53 offen lds\n" \
  "s_mov_b32 s53, 61696\n" \
  "s_add_u32 m0, s52, 15360\n" \
  "s_nop 0\n" \
  "buffer_load_dwordx4 %[wo2], s[48:51], s53 offen lds\n" \
  "s_waitcnt vmcnt(20)\n" \
  "ds_read_b64_tr_b16 a[96:97], %[tr] offset:20480\n" \
  "ds_read_b64_tr_b16 a[98:99], %[tr] offset:20736\n" \
  "ds_read_b64_tr_b16 a[100:101], %[tr] offset:20512\n" \
  "ds_read_b64_tr_b16 a[102:103], %[tr] offset:20768\n" \
  "ds_read_b64_tr_b16 v[192:193], %[tr2] offset:22528\n" \
  "ds_read_b64_tr_b16 v[194:195], %[tr2] offset:23552\n" \
  "ds_read_b64_tr_b16 v[208:209], %[tr2] offset:22560\n" \
  "s_waitcnt lgkmcnt(14)\n" \
  "ds_read_b64_tr_b16 v[210:211], %[tr2] offset:23584\n" \
  "s_waitcnt lgkmcnt(8)\n" \
  "s_mov_b32 s53, 4718592\n" \
  "s_add_u32 m0, s52, 16384\n" \
  "s_nop 0\n" \
  "buffer_load_dwordx4 %[wo], s[44:47], s53 offen lds\n" \
  "s_mov_b32 s53, 4849664\n" \
  "s_add_u32 m0, s52, 17408\n" \
  "s_nop 0\n" \
  "buffer_load_dwordx4 %[wo], s[44:47], s53 offen lds\n" \
  "s_mov_b32 s53, 82176\n" \
  "s_add_u32 m0, s52, 18432\n" \
  "s_nop 0\n" \
  "buffer_load_dwordx4 %[wo2], s[48:51], s53 offen lds\n" \
  "s_mov_b32 s53, 102656\n" \
  "s_add_u32 m0, s52, 19456\n" \
  "s_nop 0\n" \
  "buffer_load_dwordx4 %[wo2], s[48:51], s53 offen lds\n" \
  "s_waitcnt vmcnt(20)\n" \
  "ds_read_b64_tr_b16 a[104:105], %[tr] offset:24576\n" \
  "ds_read_b64_tr_b16 a[106:107], %[tr] offset:24832\n" \
  "ds_read_b64_tr_b16 a[108:109], %[tr] offset:24608\n" \
  "ds_read_b64_tr_b16 a[110:111], %[tr] offset:24864\n" \
  "ds_read_b64_tr_b16 v[196:197], %[tr2] offset:26624\n" \
  "ds_read_b64_tr_b16 v[198:199], %[tr2] offset:27648\n" \
  "ds_read_b64_tr_b16 v[212:213], %[tr2] offset:26656\n" \
  "s_waitcnt lgkmcnt(14)\n" \
  "ds_read_b64_tr_b16 v[214:215], %[tr2] offset:27680\n" \
  "s_waitcnt lgkmcnt(8)\n" \
  "s_mov_b32 s53, 4980736\n" \
  "s_add_u32 m0, s52, 20480\n" \
  "s_nop 0\n" \
  "buffer_load_dwordx4 %[wo], s[44:47], s53 offen lds\n" \
  "s_mov_b32 s53, 5111808\n" \
  "s_add_u32 m0, s52, 21504\n" \
  "s_nop 0\n" \
  "buffer_load_dwordx4 %[wo], s[44:47], s53 offen lds\n" \
  "s_mov_b32 s53, 123136\n" \
  "s_add_u32 m0, s52, 22528\n" \
  "s_nop 0\n" \
  "buffer_load_dwordx4 %[wo2], s[48:51], s53 offen lds\n" \
  "s_mov_b32 s53, 143616\n" \
  "s_add_u32 m0, s52, 23552\n" \
  "s_nop 0\n" \
  "buffer_load_dwordx4 %[wo2], s[48:51], s53 offen lds\n" \
  "s_waitcnt vmcnt(20)\n" \
  "ds_read_b64_tr_b16 a[112:113], %[tr] offset:0\n" \
  "ds_read_b64_tr_b16 a[114:115], %[tr] offset:256\n" \
  "ds_read_b64_tr_b16 a[116:117], %[tr] offset:32\n" \
  "ds_read_b64_tr_b16 a[118:119], %[tr] offset:288\n" \
  "ds_read_b64_tr_b16 v[200:201], %[tr2] offset:2048\n" \
  "ds_read_b64_tr_b16 v[202:203], %[tr2] offset:3072\n" \
  "ds_read_b64_tr_b16 v[216:217], %[tr2] offset:2080\n" \
  "s_waitcnt lgkmcnt(14)\n" \
  "ds_read_b64_tr_b16 v[218:219], %[tr2] offset:3104\n" \
  "s_waitcnt vmcnt(16)\n" \
  "s_waitcnt lgkmcnt(14)\n" \
  "ds_read_b64_tr_b16 a[120:121], %[tr] offset:4096\n" \
  "s_waitcnt lgkmcnt(14)\n" \
  "ds_read_b64_tr_b16 a[122:123], %[tr] offset:4352\n" \
  "s_waitcnt lgkmcnt(14)\n" \
  "ds_read_b64_tr_b16 a[124:125], %[tr] offset:4128\n" \
  "s_waitcnt lgkmcnt(14)\n" \
  "ds_read_b64_tr_b16 a[126:127], %[tr] offset:4384\n" \
  "s_waitcnt lgkmcnt(14)\n" \
  "ds_read_b64_tr_b16 v[204:205], %[tr2] offset:6144\n" \
  "s_waitcnt lgkmcnt(14)\n" \
  "ds_read_b64_tr_b16 v[206:207], %[tr2] offset:7168\n" \
  "s_waitcnt lgkmcnt(14)\n" \
  "ds_read_b64_tr_b16 v[220:221], %[tr2] offset:6176\n" \
  "s_waitcnt lgkmcnt(14)\n" \
  "ds_read_b64_tr_b16 v[222:223], %[tr2] offset:7200\n" \
  "s_waitcnt vmcnt(12)\n" \
  "s_waitcnt lgkmcnt(14)\n" \
  "ds_read_b64_tr_b16 a[128:129], %[tr] offset:8192\n" \
  "s_waitcnt lgkmcnt(14)\n" \
  "ds_read_b64_tr_b16 a[130:131], %[tr] offset:8448\n" \
  "s_waitcnt lgkmcnt(14)\n" \
  "ds_read_b64_tr_b16 a[132:133], %[tr] offset:8224\n" \
  "s_waitcnt lgkmcnt(14)\n" \
  "ds_read_b64_tr_b16 a[134:135], %[tr] offset:8480\n" \
  "s_waitcnt lgkmcnt(14)\n" \
  "ds_read_b64_tr_b16 v[224:225], %[tr2] offset:10240\n" \
  "s_waitcnt lgkmcnt(14)\n" \
  "ds_read_b64_tr_b16 v[226:227], %[tr2] offset:11264\n" \
  "s_waitcnt lgkmcnt(14)\n" \
  "ds_read_b64_tr_b16 v[240:241], %[tr2] offset:10272\n" \
  "s_waitcnt lgkmcnt(14)\n" \
  "ds_read_b64_tr_b16 v[242:243], %[tr2] offset:11296\n" \
  "s_waitcnt vmcnt(8)\n" \
  "s_waitcnt lgkmcnt(14)\n" \
  "ds_read_b64_tr_b16 a[136:137], %[tr] offset:12288\n" \
  "s_waitcnt lgkmcnt(14)\n" \
  "ds_read_b64_tr_b16 a[138:139], %[tr] offset:12544\n" \
  "s_waitcnt lgkmcnt(14)\n" \
  "ds_read_b64_tr_b16 a[140:141], %[tr] offset:12320\n" \
  "s_waitcnt lgkmcnt(14)\n" \
  "ds_read_b64_tr_b16 a[142:143], %[tr] offset:12576\n" \
  "s_waitcnt lgkmcnt(14)\n" \
  "ds_read_b64_tr_b16 v[228:229], %[tr2] offset:14336\n" \
  "s_waitcnt lgkmcnt(14)\n" \
  "ds_read_b64_tr_b16 v[230:231], %[tr2] offset:15360\n" \
  "s_waitcnt lgkmcnt(14)\n" \
  "ds_read_b64_tr_b16 v[244:245], %[tr2] offset:14368\n" \
  "s_waitcnt lgkmcnt(14)\n" \
  "ds_read_b64_tr_b16 v[246:247], %[tr2] offset:15392\n" \
  "s_waitcnt vmcnt(4)\n" \
  "s_waitcnt lgkmcnt(14)\n" \
  "ds_read_b64_tr_b16 a[144:145], %[tr] offset:16384\n" \
  "s_waitcnt lgkmcnt(14)\n" \
  "ds_read_b64_tr_b16 a[146:147], %[tr] offset:16640\n" \
  "s_waitcnt lgkmcnt(14)\n" \
  "ds_read_b64_tr_b16 a[148:149], %[tr] offset:16416\n" \
  "s_waitcnt lgkmcnt(14)\n" \
  "ds_read_b64_tr_b16 a[150:151], %[tr] offset:16672\n" \
  "s_waitcnt lgkmcnt(14)\n" \
  "ds_read_b64_tr_b16 v[232:233], %[tr2] offset:18432\n" \
  "s_waitcnt lgkmcnt(14)\n" \
  "ds_read_b64_tr_b16 v[234:235], %[tr2] offset:19456\n" \
  "s_waitcnt lgkmcnt(14)\n" \
  "ds_read_b64_tr_b16 v[248:249], %[tr2] offset:18464\n" \
  "s_waitcnt lgkmcnt(14)\n" \
  "ds_read_b64_tr_b16 v[250:251], %[tr2] offset:19488\n" \
  "s_waitcnt vmcnt(0)\n" \
  "s_waitcnt lgkmcnt(14)\n" \
  "ds_read_b64_tr_b16 a[152:153], %[tr] offset:20480\n" \
  "s_waitcnt lgkmcnt(14)\n" \
  "ds_read_b64_tr_b16 a[154:155], %[tr] offset:20736\n" \
  "s_waitcnt lgkmcnt(14)\n" \
  "ds_read_b64_tr_b16 a[156:157], %[tr] offset:20512\n" \
  "s_waitcnt lgkmcnt(14)\n" \
  "ds_read_b64_tr_b16 a[158:159], %[tr] offset:20768\n" \
  "s_waitcnt lgkmcnt(14)\n" \
  "ds_read_b64_tr_b16 v[236:237], %[tr2] offset:22528\n" \
  "s_waitcnt lgkmcnt(14)\n" \
  "ds_read_b64_tr_b16 v[238:239], %[tr2] offset:23552\n" \
  "s_waitcnt lgkmcnt(14)\n" \
  "ds_read_b64_tr_b16 v[252:253], %[tr2] offset:22560\n" \
  "s_waitcnt lgkmcnt(14)\n" \
  "ds_read_b64_tr_b16 v[254:255], %[tr2] offset:23584\n" \
  "s_waitcnt vmcnt(0) lgkmcnt(0)\n" \
  "s_barrier\n" \
  "s_add_u32 s57, s57, 40960\n" \
  "s_xor_b32 s56, s56, 0x10000\n" \
  "s_add_u32 m0, s56, 0\n" \
  "s_add_u32 s53, s57, 0\n" \
  "buffer_load_dwordx4 %[l16], s[40:43], s53 offen offset:0 lds\n" \
  "buffer_load_dwordx4 %[l16], s[40:43], s53 offen offset:1024 lds\n" \
  "buffer_load_dwordx4 %[l16], s[40:43], s53 offen offset:2048 lds\n" \
  "buffer_load_dwordx4 %[l16], s[40:43], s53 offen offset:3072 lds\n" \
  "s_add_u32 m0, s56, 4096\n" \
  "s_add_u32 s53, s57, 4096\n" \
  "buffer_load_dwordx4 %[l16], s[40:43], s53 offen offset:0 lds\n" \
  "buffer_load_dwordx4 %[l16], s[40:43], s53 offen offset:1024 lds\n" \
  "buffer_load_dwordx4 %[l16], s[40:43], s53 offen offset:2048 lds\n" \
  "buffer_load_dwordx4 %[l16], s[40:43], s53 offen offset:3072 lds\n" \
  "s_add_u32 m0, s56, 8192\n" \
  "s_add_u32 s53, s57, 8192\n" \
  "buffer_load_dwordx4 %[l16], s[40:43], s53 offen offset:0 lds\n" \
  "buffer_load_dwordx4 %[l16], s[40:43], s53 offen offset:1024 lds\n" \
  "s_add_u32 s57, s57, 40960\n" \
  "s_xor_b32 s56, s56, 0x10000\n" \
  "s_mul_i32 s61, %[w], 20971520\n" \
  "s_lshl_b32 s55, %[h], 9\n" \
  "s_add_u32 s61, s61, s55\n" \
  "s_add_u32 s61, s61, 113246208\n" \
  "s_mov_b32 s62, 0\n" \
  "s_mov_b32 s63, 6291456\n" \
  "v_mov_b32 v16, %[l16]\n" \
  "s_lshl_b32 s55, %[w], 10\n" \
  "s_add_u32 s55, s55, 49152\n" \
  "v_add_u32 v17, s55, %[l16]\n" \
  "v_add_u32 v19, 49152, %[l16]\n" \
  "ds_read_b128 v[116:119], v16 offset:20480\n" \
  "ds_read_b128 v[112:115], v16 offset:0\n" \
  "ds_read_b128 v[124:127], v16 offset:21504\n" \
  "ds_read_b128 v[120:123], v16 offset:1024\n" \
  "ds_read_b128 v[132:135], v16 offset:22528\n" \
  "ds_read_b128 v[128:131], v16 offset:2048\n" \
  "s_waitcnt lgkmcnt(4)\n" \
  "v_mfma_f32_16x16x32_bf16 v[176:179], a[0:3], v[112:115], 0\n" \
  "ds_read_b128 v[140:143], v16 offset:23552\n" \
  "v_mfma_f32_16x16x32_bf16 v[180:183], a[0:3], v[116:119], 0\n" \
  "ds_read_b128 v[136:139], v16 offset:3072\n" \
  "v_mfma_f32_16x16x32_bf16 v[184:187], a[4:7], v[112:115], 0\n" \
  "v_mfma_f32_16x16x32_bf16 v[188:191], a[4:7], v[116:119], 0\n" \
  "s_waitcnt lgkmcnt(4)\n" \
  "v_mfma_f32_16x16x32_bf16 v[176:179], a[8:11], v[120:123], v[176:179]\n" \
  "ds_read_b128 v[116:119], v16 offset:24576\n" \
  "v_mfma_f32_16x16x32_bf16 v[180:183], a[8:11], v[124:127], v[180:183]\n" \
  "ds_read_b128 v[112:115], v16 offset:4096\n" \
  "v_mfma_f32_16x16x32_bf16 v[184:187], a[12:15], v[120:123], v[184:187]\n" \
  "v_mfma_f32_16x16x32_bf16 v[188:191], a[12:15], v[124:127], v[188:191]\n" \
  "s_waitcnt lgkmcnt(4)\n" \
  "v_mfma_f32_16x16x32_bf16 v[176:179], a[16:19], v[128:131], v[176:179]\n" \
  "ds_read_b128 v[124:127], v16 offset:25600\n" \
  "v_mfma_f32_16x16x32_bf16 v[180:183], a[16:19], v[132:135], v[180:183]\n" \
  "ds_read_b128 v[120:123], v16 offset:5120\n" \
  "v_mfma_f32_16x16x32_bf16 v[184:187], a[20:23], v[128:131], v[184:187]\n" \
  "v_mfma_f32_16x16x32_bf16 v[188:191], a[20:23], v[132:135], v[188:191]\n" \
  "s_waitcnt lgkmcnt(4)\n" \
  "v_mfma_f32_16x16x32_bf16 v[176:179], a[24:27], v[136:139], v[176:179]\n" \
  "ds_read_b128 v[132:135], v16 offset:26624\n" \
  "v_mfma_f32_16x16x32_bf16 v[180:183], a[24:27], v[140:143], v[180:183]\n" \
  "ds_read_b128 v[128:131], v16 offset:6144\n" \
  "v_mfma_f32_16x16x32_bf16 v[184:187], a[28:31], v[136:139], v[184:187]\n" \
  "v_mfma_f32_16x16x32_bf16 v[188:191], a[28:31], v[140:143], v[188:191]\n" \
  "s_waitcnt lgkmcnt(4)\n" \
  "v_mfma_f32_16x16x32_bf16 v[176:179], a[32:35], v[112:115], v[176:179]\n" \
  "ds_read_b128 v[140:143], v16 offset:27648\n" \
  "v_mfma_f32_16x16x32_bf16 v[180:183], a[32:35], v[116:119], v[180:183]\n" \
  "ds_read_b128 v[136:139], v16 offset:7168\n" \
  "v_mfma_f32_16x16x32_bf16 v[184:187], a[36:39], v[112:115], v[184:187]\n" \
  "v_mfma_f32_16x16x32_bf16 v[188:191], a[36:39], v[116:119], v[188:191]\n" \
  "s_waitcnt lgkmcnt(4)\n" \
  "v_mfma_f32_16x16x32_bf16 v[176:179], a[40:43], v[120:123], v[176:179]\n" \
  "ds_read_b128 v[116:119], v16 offset:28672\n" \
  "v_mfma_f32_16x16x32_bf16 v[180:183], a[40:43], v[124:127], v[180:183]\n" \
  "ds_read_b128 v[112:115], v16 offset:8192\n" \
  "v_mfma_f32_16x16x32_bf16 v[184:187], a[44:47], v[120:123], v[184:187]\n" \
  "v_mfma_f32_16x16x32_bf16 v[188:191], a[44:47], v[124:127], v[188:191]\n" \
  "s_waitcnt lgkmcnt(4)\n" \
  "v_mfma_f32_16x16x32_bf16 v[176:179], a[48:51], v[128:131], v[176:179]\n" \
  "ds_read_b128 v[124:127], v16 offset:29696\n" \
  "v_mfma_f32_16x16x32_bf16 v[180:183], a[48:51], v[132:135], v[180:183]\n" \
  "ds_read_b128 v[120:123], v16 offset:9216\n" \
  "v_mfma_f32_16x16x32_bf16 v[184:187], a[52:55], v[128:131], v[184:187]\n" \
  "v_mfma_f32_16x16x32_bf16 v[188:191], a[52:55], v[132:135], v[188:191]\n" \
  "s_waitcnt lgkmcnt(4)\n" \
  "v_mfma_f32_16x16x32_bf16 v[176:179], a[56:59], v[136:139], v[176:179]\n" \
  "ds_read_b128 v[132:135], v16 offset:30720\n" \
  "v_mfma_f32_16x16x32_bf16 v[180:183], a[56:59], v[140:143], v[180:183]\n" \
  "ds_read_b128 v[128:131], v16 offset:10240\n" \
  "v_mfma_f32_16x16x32_bf16 v[184:187], a[60:63], v[136:139], v[184:187]\n" \
  "v_mfma_f32_16x16x32_bf16 v[188:191], a[60:63], v[140:143], v[188:191]\n" \
  "s_waitcnt lgkmcnt(4)\n" \
  "v_mfma_f32_16x16x32_bf16 v[176:179], a[64:67], v[112:115], v[176:179]\n" \
  "ds_read_b128 v[140:143], v16 offset:31744\n" \
  "v_mfma_f32_16x16x32_bf16 v[180:183], a[64:67], v[116:119], v[180:183]\n" \
  "ds_read_b128 v[136:139], v16 offset:11264\n" \
  "v_mfma_f32_16x16x32_bf16 v[184:187], a[68:71], v[112:115], v[184:187]\n" \
  "v_mfma_f32_16x16x32_bf16 v[188:191], a[68:71], v[116:119], v[188:191]\n" \
  "s_waitcnt lgkmcnt(4)\n" \
  "v_mfma_f32_16x16x32_bf16 v[176:179], a[72:75], v[120:123], v[176:179]\n" \
  "ds_read_b128 v[116:119], v16 offset:32768\n" \
  "v_mfma_f32_16x16x32_bf16 v[180:183], a[72:75], v[124:127], v[180:183]\n" \
  "ds_read_b128 v[112:115], v16 offset:12288\n" \
  "v_mfma_f32_16x16x32_bf16 v[184:187], a[76:79], v[120:123], v[184:187]\n" \
  "v_mfma_f32_16x16x32_bf16 v[188:191], a[76:79], v[124:127], v[188:191]\n" \
  "s_waitcnt lgkmcnt(4)\n" \
  "v_mfma_f32_16x16x32_bf16 v[176:179], a[80:83], v[128:131], v[176:179]\n" \
  "ds_read_b128 v[124:127], v16 offset:33792\n" \
  "v_mfma_f32_16x16x32_bf16 v[180:183], a[80:83], v[132:135], v[180:183]\n" \
  "ds_read_b128 v[120:123], v16 offset:13312\n" \
  "v_mfma_f32_16x16x32_bf16 v[184:187], a[84:87], v[128:131], v[184:187]\n" \
  "v_mfma_f32_16x16x32_bf16 v[188:191], a[84:87], v[132:135], v[188:191]\n" \
  "s_waitcnt lgkmcnt(4)\n" \
  "v_mfma_f32_16x16x32_bf16 v[176:179], a[88:91], v[136:139], v[176:179]\n" \
  "ds_read_b128 v[132:135], v16 offset:34816\n" \
  "v_mfma_f32_16x16x32_bf16 v[180:183], a[88:91], v[140:143], v[180:183]\n" \
  "ds_read_b128 v[128:131], v16 offset:14336\n" \
  "v_mfma_f32_16x16x32_bf16 v[184:187], a[92:95], v[136:139], v[184:187]\n" \
  "v_mfma_f32_16x16x32_bf16 v[188:191], a[92:95], v[140:143], v[188:191]\n" \
  "s_waitcnt lgkmcnt(4)\n" \
  "v_mfma_f32_16x16x32_bf16 v[176:179], a[96:99], v[112:115], v[176:179]\n" \
  "ds_read_b128 v[140:143], v16 offset:35840\n" \
  "v_mfma_f32_16x16x32_bf16 v[180:183], a[96:99], v[116:119], v[180:183]\n" \
  "ds_read_b128 v[136:139], v16 offset:15360\n" \
  "v_mfma_f32_16x16x32_bf16 v[184:187], a[100:103], v[112:115], v[184:187]\n" \
  "v_mfma_f32_16x16x32_bf16 v[188:191], a[100:103], v[116:119], v[188:191]\n" \
  "s_waitcnt lgkmcnt(4)\n" \
  "v_mfma_f32_16x16x32_bf16 v[176:179], a[104:107], v[120:123], v[176:179]\n" \
  "ds_read_b128 v[116:119], v16 offset:36864\n" \
  "v_mfma_f32_16x16x32_bf16 v[180:183], a[104:107], v[124:127], v[180:183]\n" \
  "ds_read_b128 v[112:115], v16 offset:16384\n" \
  "v_mfma_f32_16x16x32_bf16 v[184:187], a[108:111], v[120:123], v[184:187]\n" \
  "v_mfma_f32_16x16x32_bf16 v[188:191], a[108:111], v[124:127], v[188:191]\n" \
  "s_waitcnt lgkmcnt(4)\n" \
  "v_mfma_f32_16x16x32_bf16 v[176:179], a[112:115], v[128:131], v[176:179]\n" \
  "ds_read_b128 v[124:127], v16 offset:37888\n" \
  "v_mfma_f32_16x16x32_bf16 v[180:183], a[112:115], v[132:135], v[180:183]\n" \
  "ds_read_b128 v[120:123], v16 offset:17408\n" \
  "v_mfma_f32_16x16x32_bf16 v[184:187], a[116:119], v[128:131], v[184:187]\n" \
  "v_mfma_f32_16x16x32_bf16 v[188:191], a[116:119], v[132:135], v[188:191]\n" \
  "s_waitcnt lgkmcnt(4)\n" \
  "v_mfma_f32_16x16x32_bf16 v[176:179], a[120:123], v[136:139], v[176:179]\n" \
  "ds_read_b128 v[132:135], v16 offset:38912\n" \
  "v_mfma_f32_16x16x32_bf16 v[180:183], a[120:123], v[140:143], v[180:183]\n" \
  "ds_read_b128 v[128:131], v16 offset:18432\n" \
  "v_mfma_f32_16x16x32_bf16 v[184:187], a[124:127], v[136:139], v[184:187]\n" \
  "v_mfma_f32_16x16x32_bf16 v[188:191], a[124:127], v[140:143], v[188:191]\n" \
  "s_waitcnt lgkmcnt(4)\n" \
  "v_mfma_f32_16x16x32_bf16 v[176:179], a[128:131], v[112:115], v[176:179]\n" \
  "ds_read_b128 v[140:143], v16 offset:39936\n" \
  "v_mfma_f32_16x16x32_bf16 v[180:183], a[128:131], v[116:119], v[180:183]\n" \
  "ds_read_b128 v[136:139], v16 offset:19456\n" \
  "v_mfma_f32_16x16x32_bf16 v[184:187], a[132:135], v[112:115], v[184:187]\n" \
  "v_mfma_f32_16x16x32_bf16 v[188:191], a[132:135], v[116:119], v[188:191]\n" \
  "s_waitcnt lgkmcnt(4)\n" \
  "v_mfma_f32_16x16x32_bf16 v[176:179], a[136:139], v[120:123], v[176:179]\n" \
  "v_mfma_f32_16x16x32_bf16 v[180:183], a[136:139], v[124:127], v[180:183]\n" \
  "v_mfma_f32_16x16x32_bf16 v[184:187], a[140:143], v[120:123], v[184:187]\n" \
  "v_mfma_f32_16x16x32_bf16 v[188:191], a[140:143], v[124:127], v[188:191]\n" \
  "s_waitcnt lgkmcnt(2)\n" \
  "v_mfma_f32_16x16x32_bf16 v[176:179], a[144:147], v[128:131], v[176:179]\n" \
  "v_mfma_f32_16x16x32_bf16 v[180:183], a[144:147], v[132:135], v[180:183]\n" \
  "v_mfma_f32_16x16x32_bf16 v[184:187], a[148:151], v[128:131], v[184:187]\n" \
  "v_mfma_f32_16x16x32_bf16 v[188:191], a[148:151], v[132:135], v[188:191]\n" \
  "s_waitcnt lgkmcnt(0)\n" \
  "v_mfma_f32_16x16x32_bf16 v[176:179], a[152:155], v[136:139], v[176:179]\n" \
  "v_mfma_f32_16x16x32_bf16 v[180:183], a[152:155], v[140:143], v[180:183]\n" \
  "v_mfma_f32_16x16x32_bf16 v[184:187], a[156:159], v[136:139], v[184:187]\n" \
  "v_mfma_f32_16x16x32_bf16 v[188:191], a[156:159], v[140:143], v[188:191]\n" \
  "v_cvt_pk_bf16_f32 v80, v176, v177\n" \
  "v_cvt_pk_bf16_f32 v81, v178, v179\n" \
  "s_nop 1\n" \
  "v_cvt_pk_bf16_f32 v82, v184, v185\n" \
  "v_cvt_pk_bf16_f32 v83, v186, v187\n" \
  "ds_write_b128 v17, v[80:83] offset:0\n" \
  "v_cvt_pk_bf16_f32 v84, v180, v181\n" \
  "v_cvt_pk_bf16_f32 v85, v182, v183\n" \
  "v_cvt_pk_bf16_f32 v86, v188, v189\n" \
  "v_cvt_pk_bf16_f32 v87, v190, v191\n" \
  "ds_write_b128 v17, v[84:87] offset:4096\n" \
  "v_xor_b32 v17, 0x2000, v17\n" \
  "s_waitcnt vmcnt(0) lgkmcnt(0)\n" \
  "s_barrier\n" \
  "s_add_u32 m0, s56, 0\n" \
  "s_add_u32 s53, s57, 0\n" \
  "buffer_load_dwordx4 %[l16], s[40:43], s53 offen offset:0 lds\n" \
  "buffer_load_dwordx4 %[l16], s[40:43], s53 offen offset:1024 lds\n" \
  "buffer_load_dwordx4 %[l16], s[40:43], s53 offen offset:2048 lds\n" \
  "buffer_load_dwordx4 %[l16], s[40:43], s53 offen offset:3072 lds\n" \
  "s_add_u32 m0, s56, 4096\n" \
  "s_add_u32 s53, s57, 4096\n" \
  "buffer_load_dwordx4 %[l16], s[40:43], s53 offen offset:0 lds\n" \
  "buffer_load_dwordx4 %[l16], s[40:43], s53 offen offset:1024 lds\n" \
  "buffer_load_dwordx4 %[l16], s[40:43], s53 offen offset:2048 lds\n" \
  "buffer_load_dwordx4 %[l16], s[40:43], s53 offen offset:3072 lds\n" \
  "s_add_u32 m0, s56, 8192\n" \
  "s_add_u32 s53, s57, 8192\n" \
  "buffer_load_dwordx4 %[l16], s[40:43], s53 offen offset:0 lds\n" \
  "buffer_load_dwordx4 %[l16], s[40:43], s53 offen offset:1024 lds\n" \
  "s_add_u32 s57, s57, 40960\n" \
  "s_xor_b32 s56, s56, 0x10000\n" \
  "v_xor_b32 v16, 0x10000, v16\n" \
  "ds_read_b128 v[116:119], v16 offset:20480\n" \
  "ds_read_b128 v[112:115], v16 offset:0\n" \
  "ds_read_b128 v[124:127], v16 offset:21504\n" \
  "ds_read_b128 v[120:123], v16 offset:1024\n" \
  "ds_read_b128 v[132:135], v16 offset:22528\n" \
  "ds_read_b128 v[128:131], v16 offset:2048\n" \
  "s_waitcnt lgkmcnt(4)\n" \
  "v_mfma_f32_16x16x32_bf16 v[176:179], a[0:3], v[112:115], 0\n" \
  "ds_read_b128 v[140:143], v16 offset:23552\n" \
  "v_mfma_f32_16x16x32_bf16 v[180:183], a[0:3], v[116:119], 0\n" \
  "ds_read_b128 v[136:139], v16 offset:3072\n" \
  "v_mfma_f32_16x16x32_bf16 v[184:187], a[4:7], v[112:115], 0\n" \
  "v_mfma_f32_16x16x32_bf16 v[188:191], a[4:7], v[116:119], 0\n" \
  "s_waitcnt lgkmcnt(4)\n" \
  "v_mfma_f32_16x16x32_bf16 v[176:179], a[8:11], v[120:123], v[176:179]\n" \
  "ds_read_b128 v[116:119], v16 offset:24576\n" \
  "v_mfma_f32_16x16x32_bf16 v[180:183], a[8:11], v[124:127], v[180:183]\n" \
  "ds_read_b128 v[112:115], v16 offset:4096\n" \
  "v_mfma_f32_16x16x32_bf16 v[184:187], a[12:15], v[120:123], v[184:187]\n" \
  "v_mfma_f32_16x16x32_bf16 v[188:191], a[12:15], v[124:127], v[188:191]\n" \
  "s_waitcnt lgkmcnt(4)\n" \
  "v_mfma_f32_16x16x32_bf16 v[176:179], a[16:19], v[128:131], v[176:179]\n" \
  "ds_read_b128 v[124:127], v16 offset:25600\n" \
  "v_mfma_f32_16x16x32_bf16 v[180:183], a[16:19], v[132:135], v[180:183]\n" \
  "ds_read_b128 v[120:123], v16 offset:5120\n" \
  "v_mfma_f32_16x16x32_bf16 v[184:187], a[20:23], v[128:131], v[184:187]\n" \
  "v_mfma_f32_16x16x32_bf16 v[188:191], a[20:23], v[132:135], v[188:191]\n" \
  "s_waitcnt lgkmcnt(4)\n" \
  "v_mfma_f32_16x16x32_bf16 v[176:179], a[24:27], v[136:139], v[176:179]\n" \
  "ds_read_b128 v[132:135], v16 offset:26624\n" \
  "v_mfma_f32_16x16x32_bf16 v[180:183], a[24:27], v[140:143], v[180:183]\n" \
  "ds_read_b128 v[128:131], v16 offset:6144\n" \
  "v_mfma_f32_16x16x32_bf16 v[184:187], a[28:31], v[136:139], v[184:187]\n" \
  "v_mfma_f32_16x16x32_bf16 v[188:191], a[28:31], v[140:143], v[188:191]\n" \
  "s_waitcnt lgkmcnt(4)\n" \
  "v_mfma_f32_16x16x32_bf16 v[176:179], a[32:35], v[112:115], v[176:179]\n" \
  "ds_read_b128 v[140:143], v16 offset:27648\n" \
  "v_mfma_f32_16x16x32_bf16 v[180:183], a[32:35], v[116:119], v[180:183]\n" \
  "ds_read_b128 v[136:139], v16 offset:7168\n" \
  "v_mfma_f32_16x16x32_bf16 v[184:187], a[36:39], v[112:115], v[184:187]\n" \
  "v_mfma_f32_16x16x32_bf16 v[188:191], a[36:39], v[116:119], v[188:191]\n" \
  "s_waitcnt lgkmcnt(4)\n" \
  "v_mfma_f32_16x16x32_bf16 v[176:179], a[40:43], v[120:123], v[176:179]\n" \
  "ds_read_b128 v[116:119], v16 offset:28672\n" \
  "v_mfma_f32_16x16x32_bf16 v[180:183], a[40:43], v[124:127], v[180:183]\n" \
  "ds_read_b128 v[112:115], v16 offset:8192\n" \
  "v_mfma_f32_16x16x32_bf16 v[184:187], a[44:47], v[120:123], v[184:187]\n" \
  "v_mfma_f32_16x16x32_bf16 v[188:191], a[44:47], v[124:127], v[188:191]\n" \
  "s_waitcnt lgkmcnt(4)\n" \
  "v_mfma_f32_16x16x32_bf16 v[176:179], a[48:51], v[128:131], v[176:179]\n" \
  "ds_read_b128 v[124:127], v16 offset:29696\n" \
  "v_mfma_f32_16x16x32_bf16 v[180:183], a[48:51], v[132:135], v[180:183]\n" \
  "ds_read_b128 v[120:123], v16 offset:9216\n" \
  "v_mfma_f32_16x16x32_bf16 v[184:187], a[52:55], v[128:131], v[184:187]\n" \
  "v_mfma_f32_16x16x32_bf16 v[188:191], a[52:55], v[132:135], v[188:191]\n" \
  "s_waitcnt lgkmcnt(4)\n" \
  "v_mfma_f32_16x16x32_bf16 v[176:179], a[56:59], v[136:139], v[176:179]\n" \
  "ds_read_b128 v[132:135], v16 offset:30720\n" \
  "v_mfma_f32_16x16x32_bf16 v[180:183], a[56:59], v[140:143], v[180:183]\n" \
  "ds_read_b128 v[128:131], v16 offset:10240\n" \
  "v_mfma_f32_16x16x32_bf16 v[184:187], a[60:63], v[136:139], v[184:187]\n" \
  "v_mfma_f32_16x16x32_bf16 v[188:191], a[60:63], v[140:143], v[188:191]\n" \
  "s_waitcnt lgkmcnt(4)\n" \
  "v_mfma_f32_16x16x32_bf16 v[176:179], a[64:67], v[112:115], v[176:179]\n" \
  "ds_read_b128 v[140:143], v16 offset:31744\n" \
  "v_mfma_f32_16x16x32_bf16 v[180:183], a[64:67], v[116:119], v[180:183]\n" \
  "ds_read_b128 v[136:139], v16 offset:11264\n" \
  "v_mfma_f32_16x16x32_bf16 v[184:187], a[68:71], v[112:115], v[184:187]\n" \
  "v_mfma_f32_16x16x32_bf16 v[188:191], a[68:71], v[116:119], v[188:191]\n" \
  "s_waitcnt lgkmcnt(4)\n" \
  "v_mfma_f32_16x16x32_bf16 v[176:179], a[72:75], v[120:123], v[176:179]\n" \
  "ds_read_b128 v[116:119], v16 offset:32768\n" \
  "v_mfma_f32_16x16x32_bf16 v[180:183], a[72:75], v[124:127], v[180:183]\n" \
  "ds_read_b128 v[112:115], v16 offset:12288\n" \
  "v_mfma_f32_16x16x32_bf16 v[184:187], a[76:79], v[120:123], v[184:187]\n" \
  "v_mfma_f32_16x16x32_bf16 v[188:191], a[76:79], v[124:127], v[188:191]\n" \
  "s_waitcnt lgkmcnt(4)\n" \
  "v_mfma_f32_16x16x32_bf16 v[176:179], a[80:83], v[128:131], v[176:179]\n" \
  "ds_read_b128 v[124:127], v16 offset:33792\n" \
  "v_mfma_f32_16x16x32_bf16 v[180:183], a[80:83], v[132:135], v[180:183]\n" \
  "ds_read_b128 v[120:123], v16 offset:13312\n" \
  "v_mfma_f32_16x16x32_bf16 v[184:187], a[84:87], v[128:131], v[184:187]\n" \
  "v_mfma_f32_16x16x32_bf16 v[188:191], a[84:87], v[132:135], v[188:191]\n" \
  "s_waitcnt lgkmcnt(4)\n" \
  "v_mfma_f32_16x16x32_bf16 v[176:179], a[88:91], v[136:139], v[176:179]\n" \
  "ds_read_b128 v[132:135], v16 offset:34816\n" \
  "v_mfma_f32_16x16x32_bf16 v[180:183], a[88:91], v[140:143], v[180:183]\n" \
  "ds_read_b128 v[128:131], v16 offset:14336\n" \
  "v_mfma_f32_16x16x32_bf16 v[184:187], a[92:95], v[136:139], v[184:187]\n" \
  "v_mfma_f32_16x16x32_bf16 v[188:191], a[92:95], v[140:143], v[188:191]\n" \
  "s_waitcnt lgkmcnt(4)\n" \
  "v_mfma_f32_16x16x32_bf16 v[176:179], a[96:99], v[112:115], v[176:179]\n" \
  "ds_read_b128 v[140:143], v16 offset:35840\n" \
  "v_mfma_f32_16x16x32_bf16 v[180:183], a[96:99], v[116:119], v[180:183]\n" \
  "ds_read_b128 v[136:139], v16 offset:15360\n" \
  "v_mfma_f32_16x16x32_bf16 v[184:187], a[100:103], v[112:115], v[184:187]\n" \
  "ds_read_b128 v[160:163], v19 offset:4096\n" \
  "v_mfma_f32_16x16x32_bf16 v[188:191], a[100:103], v[116:119], v[188:191]\n" \
  "ds_read_b128 v[144:147], v19 offset:0\n" \
  "s_waitcnt lgkmcnt(6)\n" \
  "v_mfma_f32_16x16x32_bf16 v[176:179], a[104:107], v[120:123], v[176:179]\n" \
  "ds_read_b128 v[116:119], v16 offset:36864\n" \
  "v_mfma_f32_16x16x32_bf16 v[180:183], a[104:107], v[124:127], v[180:183]\n" \
  "ds_read_b128 v[112:115], v16 offset:16384\n" \
  "v_mfma_f32_16x16x32_bf16 v[184:187], a[108:111], v[120:123], v[184:187]\n" \
  "ds_read_b128 v[164:167], v19 offset:5120\n" \
  "v_mfma_f32_16x16x32_bf16 v[188:191], a[108:111], v[124:127], v[188:191]\n" \
  "ds_read_b128 v[148:151], v19 offset:1024\n" \
  "s_waitcnt lgkmcnt(8)\n" \
  "v_mfma_f32_16x16x32_bf16 v[176:179], a[112:115], v[128:131], v[176:179]\n" \
  "ds_read_b128 v[124:127], v16 offset:37888\n" \
  "v_mfma_f32_16x16x32_bf16 v[180:183], a[112:115], v[132:135], v[180:183]\n" \
  "ds_read_b128 v[120:123], v16 offset:17408\n" \
  "v_mfma_f32_16x16x32_bf16 v[184:187], a[116:119], v[128:131], v[184:187]\n" \
  "ds_read_b128 v[168:171], v19 offset:6144\n" \
  "v_mfma_f32_16x16x32_bf16 v[188:191], a[116:119], v[132:135], v[188:191]\n" \
  "ds_read_b128 v[152:155], v19 offset:2048\n" \
  "s_waitcnt lgkmcnt(10)\n" \
  "v_mfma_f32_16x16x32_bf16 v[176:179], a[120:123], v[136:139], v[176:179]\n" \
  "ds_read_b128 v[132:135], v16 offset:38912\n" \
  "v_mfma_f32_16x16x32_bf16 v[180:183], a[120:123], v[140:143], v[180:183]\n" \
  "ds_read_b128 v[128:131], v16 offset:18432\n" \
  "v_mfma_f32_16x16x32_bf16 v[184:187], a[124:127], v[136:139], v[184:187]\n" \
  "ds_read_b128 v[172:175], v19 offset:7168\n" \
  "v_mfma_f32_16x16x32_bf16 v[188:191], a[124:127], v[140:143], v[188:191]\n" \
  "ds_read_b128 v[156:159], v19 offset:3072\n" \
  "s_waitcnt lgkmcnt(10)\n" \
  "v_mfma_f32_16x16x32_bf16 v[176:179], a[128:131], v[112:115], v[176:179]\n" \
  "ds_read_b128 v[140:143], v16 offset:39936\n" \
  "v_mfma_f32_16x16x32_bf16 v[180:183], a[128:131], v[116:119], v[180:183]\n" \
  "ds_read_b128 v[136:139], v16 offset:19456\n" \
  "v_mfma_f32_16x16x32_bf16 v[184:187], a[132:135], v[112:115], v[184:187]\n" \
  "v_xor_b32 v19, 0x2000, v19\n" \
  "v_mfma_f32_16x16x32_bf16 v[188:191], a[132:135], v[116:119], v[188:191]\n" \
  "s_waitcnt lgkmcnt(8)\n" \
  "v_mfma_f32_16x16x32_bf16 v[176:179], a[136:139], v[120:123], v[176:179]\n" \
  "v_mfma_f32_16x16x32_bf16 v[180:183], a[136:139], v[124:127], v[180:183]\n" \
  "v_mfma_f32_16x16x32_bf16 v[184:187], a[140:143], v[120:123], v[184:187]\n" \
  "v_mfma_f32_16x16x32_bf16 v[188:191], a[140:143], v[124:127], v[188:191]\n" \
  "s_waitcnt lgkmcnt(4)\n" \
  "v_mfma_f32_16x16x32_bf16 v[176:179], a[144:147], v[128:131], v[176:179]\n" \
  "v_mfma_f32_16x16x32_bf16 v[180:183], a[144:147], v[132:135], v[180:183]\n" \
  "v_mfma_f32_16x16x32_bf16 v[184:187], a[148:151], v[128:131], v[184:187]\n" \
  "v_mfma_f32_16x16x32_bf16 v[188:191], a[148:151], v[132:135], v[188:191]\n" \
  "s_waitcnt lgkmcnt(0)\n" \
  "v_mfma_f32_16x16x32_bf16 v[176:179], a[152:155], v[136:139], v[176:179]\n" \
  "v_mfma_f32_16x16x32_bf16 v[180:183], a[152:155], v[140:143], v[180:183]\n" \
  "v_mfma_f32_16x16x32_bf16 v[184:187], a[156:159], v[136:139], v[184:187]\n" \
  "v_mfma_f32_16x16x32_bf16 v[188:191], a[156:159], v[140:143], v[188:191]\n" \
  "v_mfma_f32_16x16x32_bf16 v[96:99], v[144:147], a[160:163], 0\n" \
  "v_mfma_f32_16x16x32_bf16 v[100:103], v[160:163], a[160:163], 0\n" \
  "v_cvt_pk_bf16_f32 v80, v176, v177\n" \
  "v_cvt_pk_bf16_f32 v81, v178, v179\n" \
  "v_mfma_f32_16x16x32_bf16 v[96:99], v[148:151], a[164:167], v[96:99]\n" \
  "v_cvt_pk_bf16_f32 v82, v184, v185\n" \
  "v_cvt_pk_bf16_f32 v83, v186, v187\n" \
  "v_mfma_f32_16x16x32_bf16 v[100:103], v[164:167], a[164:167], v[100:103]\n" \
  "ds_write_b128 v17, v[80:83] offset:0\n" \
  "v_mfma_f32_16x16x32_bf16 v[96:99], v[152:155], a[168:171], v[96:99]\n" \
  "v_mfma_f32_16x16x32_bf16 v[100:103], v[168:171], a[168:171], v[100:103]\n" \
  "v_mfma_f32_16x16x32_bf16 v[96:99], v[156:159], a[172:175], v[96:99]\n" \
  "v_mfma_f32_16x16x32_bf16 v[100:103], v[172:175], a[172:175], v[100:103]\n" \
  "v_mfma_f32_16x16x32_bf16 v[104:107], v[144:147], a[176:179], 0\n" \
  "v_mfma_f32_16x16x32_bf16 v[108:111], v[160:163], a[176:179], 0\n" \
  "v_cvt_pk_bf16_f32 v84, v180, v181\n" \
  "v_cvt_pk_bf16_f32 v85, v182, v183\n" \
  "v_mfma_f32_16x16x32_bf16 v[104:107], v[148:151], a[180:183], v[104:107]\n" \
  "v_cvt_pk_bf16_f32 v86, v188, v189\n" \
  "v_cvt_pk_bf16_f32 v87, v190, v191\n" \
  "v_mfma_f32_16x16x32_bf16 v[108:111], v[164:167], a[180:183], v[108:111]\n" \
  "ds_write_b128 v17, v[84:87] offset:4096\n" \
  "v_xor_b32 v17, 0x2000, v17\n" \
  "v_mfma_f32_16x16x32_bf16 v[104:107], v[152:155], a[184:187], v[104:107]\n" \
  "v_cvt_pk_bf16_f32 v88, v96, v97\n" \
  "v_cvt_pk_bf16_f32 v89, v98, v99\n" \
  "v_mfma_f32_16x16x32_bf16 v[108:111], v[168:171], a[184:187], v[108:111]\n" \
  "v_cvt_pk_bf16_f32 v90, v100, v101\n" \
  "v_cvt_pk_bf16_f32 v91, v102, v103\n" \
  "v_mfma_f32_16x16x32_bf16 v[104:107], v[156:159], a[188:191], v[104:107]\n" \
  "buffer_store_dwordx4 v[88:91], %[zo], s[40:43], s61 offen sc1\n" \
  "s_add_u32 s61, s61, 2097152\n" \
  "v_mfma_f32_16x16x32_bf16 v[108:111], v[172:175], a[188:191], v[108:111]\n" \
  "v_mfma_f32_16x16x32_bf16 v[96:99], v[144:147], a[192:195], 0\n" \
  "v_mfma_f32_16x16x32_bf16 v[100:103], v[160:163], a[192:195], 0\n" \
  "s_waitcnt vmcnt(1) lgkmcnt(0)\n" \
  "s_barrier\n" \
  "v_mfma_f32_16x16x32_bf16 v[96:99], v[148:151], a[196:199], v[96:99]\n" \
  "s_add_u32 m0, s56, 0\n" \
  "s_add_u32 s53, s57, 0\n" \
  "v_mfma_f32_16x16x32_bf16 v[100:103], v[164:167], a[196:199], v[100:103]\n" \
  "buffer_load_dwordx4 %[l16], s[40:43], s53 offen offset:0 lds\n" \
  "v_cvt_pk_bf16_f32 v92, v104, v105\n" \
  "v_mfma_f32_16x16x32_bf16 v[96:99], v[152:155], a[200:203], v[96:99]\n" \
  "v_cvt_pk_bf16_f32 v93, v106, v107\n" \
  "v_cvt_pk_bf16_f32 v94, v108, v109\n" \
  "v_mfma_f32_16x16x32_bf16 v[100:103], v[168:171], a[200:203], v[100:103]\n" \
  "v_cvt_pk_bf16_f32 v95, v110, v111\n" \
  "buffer_store_dwordx4 v[92:95], %[zo], s[40:43], s61 offen sc1\n" \
  "v_mfma_f32_16x16x32_bf16 v[96:99], v[156:159], a[204:207], v[96:99]\n" \
  "s_add_u32 s61, s61, 2097152\n" \
  "v_mfma_f32_16x16x32_bf16 v[100:103], v[172:175], a[204:207], v[100:103]\n" \
  "v_mfma_f32_16x16x32_bf16 v[104:107], v[144:147], a[208:211], 0\n" \
  "v_mfma_f32_16x16x32_bf16 v[108:111], v[160:163], a[208:211], 0\n" \
  "buffer_load_dwordx4 %[l16], s[40:43], s53 offen offset:1024 lds\n" \
  "buffer_load_dwordx4 %[l16], s[40:43], s53 offen offset:2048 lds\n" \
  "v_mfma_f32_16x16x32_bf16 v[104:107], v[148:151], a[212:215], v[104:107]\n" \
  "buffer_load_dwordx4 %[l16], s[40:43], s53 offen offset:3072 lds\n" \
  "s_add_u32 m0, s56, 4096\n" \
  "v_mfma_f32_16x16x32_bf16 v[108:111], v[164:167], a[212:215], v[108:111]\n" \
  "v_cvt_pk_bf16_f32 v88, v96, v97\n" \
  "v_cvt_pk_bf16_f32 v89, v98, v99\n" \
  "v_mfma_f32_16x16x32_bf16 v[104:107], v[152:155], a[216:219], v[104:107]\n" \
  "v_cvt_pk_bf16_f32 v90, v100, v101\n" \
  "v_cvt_pk_bf16_f32 v91, v102, v103\n" \
  "v_mfma_f32_16x16x32_bf16 v[108:111], v[168:171], a[216:219], v[108:111]\n" \
  "buffer_store_dwordx4 v[88:91], %[zo], s[40:43], s61 offen sc1\n" \
  "s_add_u32 s61, s61, 2097152\n" \
  "v_mfma_f32_16x16x32_bf16 v[104:107], v[156:159], a[220:223], v[104:107]\n" \
  "v_mfma_f32_16x16x32_bf16 v[108:111], v[172:175], a[220:223], v[108:111]\n" \
  "v_mfma_f32_16x16x32_bf16 v[96:99], v[144:147], a[224:227], 0\n" \
  "v_mfma_f32_16x16x32_bf16 v[100:103], v[160:163], a[224:227], 0\n" \
  "s_add_u32 s53, s57, 4096\n" \
  "buffer_load_dwordx4 %[l16], s[40:43], s53 offen offset:0 lds\n" \
  "v_mfma_f32_16x16x32_bf16 v[96:99], v[148:151], a[228:231], v[96:99]\n" \
  "buffer_load_dwordx4 %[l16], s[40:43], s53 offen offset:1024 lds\n" \
  "buffer_load_dwordx4 %[l16], s[40:43], s53 offen offset:2048 lds\n" \
  "v_mfma_f32_16x16x32_bf16 v[100:103], v[164:167], a[228:231], v[100:103]\n" \
  "v_cvt_pk_bf16_f32 v92, v104, v105\n" \
  "v_cvt_pk_bf16_f32 v93, v106, v107\n" \
  "v_mfma_f32_16x16x32_bf16 v[96:99], v[152:155], a[232:235], v[96:99]\n" \
  "v_cvt_pk_bf16_f32 v94, v108, v109\n" \
  "v_cvt_pk_bf16_f32 v95, v110, v111\n" \
  "v_mfma_f32_16x16x32_bf16 v[100:103], v[168:171], a[232:235], v[100:103]\n" \
  "buffer_store_dwordx4 v[92:95], %[zo], s[40:43], s61 offen sc1\n" \
  "s_add_u32 s61, s61, 2097152\n" \
  "v_mfma_f32_16x16x32_bf16 v[96:99], v[156:159], a[236:239], v[96:99]\n" \
  "v_mfma_f32_16x16x32_bf16 v[100:103], v[172:175], a[236:239], v[100:103]\n" \
  "v_mfma_f32_16x16x32_bf16 v[104:107], v[144:147], a[240:243], 0\n" \
  "v_mfma_f32_16x16x32_bf16 v[108:111], v[160:163], a[240:243], 0\n" \
  "buffer_load_dwordx4 %[l16], s[40:43], s53 offen offset:3072 lds\n" \
  "s_add_u32 m0, s56, 8192\n" \
  "v_mfma_f32_16x16x32_bf16 v[104:107], v[148:151], a[244:247], v[104:107]\n" \
  "s_add_u32 s53, s57, 8192\n" \
  "buffer_load_dwordx4 %[l16], s[40:43], s53 offen offset:0 lds\n" \
  "v_mfma_f32_16x16x32_bf16 v[108:111], v[164:167], a[244:247], v[108:111]\n" \
  "v_cvt_pk_bf16_f32 v88, v96, v97\n" \
  "v_cvt_pk_bf16_f32 v89, v98, v99\n" \
  "v_mfma_f32_16x16x32_bf16 v[104:107], v[152:155], a[248:251], v[104:107]\n" \
  "v_cvt_pk_bf16_f32 v90, v100, v101\n" \
  "v_cvt_pk_bf16_f32 v91, v102, v103\n" \
  "v_mfma_f32_16x16x32_bf16 v[108:111], v[168:171], a[248:251], v[108:111]\n" \
  "buffer_store_dwordx4 v[88:91], %[zo], s[40:43], s61 offen sc1\n" \
  "s_add_u32 s61, s61, 2097152\n" \
  "v_mfma_f32_16x16x32_bf16 v[104:107], v[156:159], a[252:255], v[104:107]\n" \
  "v_mfma_f32_16x16x32_bf16 v[108:111], v[172:175], a[252:255], v[108:111]\n" \
  "v_mfma_f32_16x16x32_bf16 v[96:99], v[144:147], v[192:195], 0\n" \
  "v_mfma_f32_16x16x32_bf16 v[100:103], v[160:163], v[192:195], 0\n" \
  "buffer_load_dwordx4 %[l16], s[40:43], s53 offen offset:1024 lds\n" \
  "s_add_u32 s57, s57, 40960\n" \
  "v_mfma_f32_16x16x32_bf16 v[96:99], v[148:151], v[196:199], v[96:99]\n" \
  "s_xor_b32 s56, s56, 0x10000\n" \
  "v_cvt_pk_bf16_f32 v92, v104, v105\n" \
  "v_mfma_f32_16x16x32_bf16 v[100:103], v[164:167], v[196:199], v[100:103]\n" \
  "v_cvt_pk_bf16_f32 v93, v106, v107\n" \
  "v_cvt_pk_bf16_f32 v94, v108, v109\n" \
  "v_mfma_f32_16x16x32_bf16 v[96:99], v[152:155], v[200:203], v[96:99]\n" \
  "v_cvt_pk_bf16_f32 v95, v110, v111\n" \
  "buffer_store_dwordx4 v[92:95], %[zo], s[40:43], s61 offen sc1\n" \
  "v_mfma_f32_16x16x32_bf16 v[100:103], v[168:171], v[200:203], v[100:103]\n" \
  "s_add_u32 s61, s61, 2097152\n" \
  "v_mfma_f32_16x16x32_bf16 v[96:99], v[156:159], v[204:207], v[96:99]\n" \
  "v_mfma_f32_16x16x32_bf16 v[100:103], v[172:175], v[204:207], v[100:103]\n" \
  "v_mfma_f32_16x16x32_bf16 v[104:107], v[144:147], v[208:211], 0\n" \
  "v_mfma_f32_16x16x32_bf16 v[108:111], v[160:163], v[208:211], 0\n" \
  "v_xor_b32 v16, 0x10000, v16\n" \
  "ds_read_b128 v[116:119], v16 offset:20480\n" \
  "v_mfma_f32_16x16x32_bf16 v[104:107], v[148:151], v[212:215], v[104:107]\n" \
  "ds_read_b128 v[112:115], v16 offset:0\n" \
  "v_cvt_pk_bf16_f32 v88, v96, v97\n" \
  "v_mfma_f32_16x16x32_bf16 v[108:111], v[164:167], v[212:215], v[108:111]\n" \
  "v_cvt_pk_bf16_f32 v89, v98, v99\n" \
  "v_cvt_pk_bf16_f32 v90, v100, v101\n" \
  "v_mfma_f32_16x16x32_bf16 v[104:107], v[152:155], v[216:219], v[104:107]\n" \
  "v_cvt_pk_bf16_f32 v91, v102, v103\n" \
  "buffer_store_dwordx4 v[88:91], %[zo], s[40:43], s61 offen sc1\n" \
  "v_mfma_f32_16x16x32_bf16 v[108:111], v[168:171], v[216:219], v[108:111]\n" \
  "s_add_u32 s61, s61, 2097152\n" \
  "v_mfma_f32_16x16x32_bf16 v[104:107], v[156:159], v[220:223], v[104:107]\n" \
  "v_mfma_f32_16x16x32_bf16 v[108:111], v[172:175], v[220:223], v[108:111]\n" \
  "v_mfma_f32_16x16x32_bf16 v[96:99], v[144:147], v[224:227], 0\n" \
  "v_mfma_f32_16x16x32_bf16 v[100:103], v[160:163], v[224:227], 0\n" \
  "ds_read_b128 v[124:127], v16 offset:21504\n" \
  "ds_read_b128 v[120:123], v16 offset:1024\n" \
  "v_mfma_f32_16x16x32_bf16 v[96:99], v[148:151], v[228:231], v[96:99]\n" \
  "v_cvt_pk_bf16_f32 v92, v104, v105\n" \
  "v_cvt_pk_bf16_f32 v93, v106, v107\n" \
  "v_mfma_f32_16x16x32_bf16 v[100:103], v[164:167], v[228:231], v[100:103]\n" \
  "v_cvt_pk_bf16_f32 v94, v108, v109\n" \
  "v_cvt_pk_bf16_f32 v95, v110, v111\n" \
  "v_mfma_f32_16x16x32_bf16 v[96:99], v[152:155], v[232:235], v[96:99]\n" \
  "buffer_store_dwordx4 v[92:95], %[zo], s[40:43], s61 offen sc1\n" \
  "s_add_u32 s61, s61, 2097152\n" \
  "v_mfma_f32_16x16x32_bf16 v[100:103], v[168:171], v[232:235], v[100:103]\n" \
  "v_mfma_f32_16x16x32_bf16 v[96:99], v[156:159], v[236:239], v[96:99]\n" \
  "v_mfma_f32_16x16x32_bf16 v[100:103], v[172:175], v[236:239], v[100:103]\n" \
  "v_mfma_f32_16x16x32_bf16 v[104:107], v[144:147], v[240:243], 0\n" \
  "v_mfma_f32_16x16x32_bf16 v[108:111], v[160:163], v[240:243], 0\n" \
  "ds_read_b128 v[132:135], v16 offset:22528\n" \
  "ds_read_b128 v[128:131], v16 offset:2048\n" \
  "v_mfma_f32_16x16x32_bf16 v[104:107], v[148:151], v[244:247], v[104:107]\n" \
  "v_cvt_pk_bf16_f32 v88, v96, v97\n" \
  "v_cvt_pk_bf16_f32 v89, v98, v99\n" \
  "v_mfma_f32_16x16x32_bf16 v[108:111], v[164:167], v[244:247], v[108:111]\n" \
  "v_cvt_pk_bf16_f32 v90, v100, v101\n" \
  "v_cvt_pk_bf16_f32 v91, v102, v103\n" \
  "v_mfma_f32_16x16x32_bf16 v[104:107], v[152:155], v[248:251], v[104:107]\n" \
  "buffer_store_dwordx4 v[88:91], %[zo], s[40:43], s61 offen sc1\n" \
  "s_add_u32 s61, s61, 2097152\n" \
  "v_mfma_f32_16x16x32_bf16 v[108:111], v[168:171], v[248:251], v[108:111]\n" \
  "v_mfma_f32_16x16x32_bf16 v[104:107], v[156:159], v[252:255], v[104:107]\n" \
  "v_mfma_f32_16x16x32_bf16 v[108:111], v[172:175], v[252:255], v[108:111]\n" \
  "s_mov_b32 s65, 62\n" \
  "Lvz%=:\n" \
  "s_waitcnt lgkmcnt(4)\n" \
  "v_mfma_f32_16x16x32_bf16 v[176:179], a[0:3], v[112:115], 0\n" \
  "ds_read_b128 v[140:143], v16 offset:23552\n" \
  "v_mfma_f32_16x16x32_bf16 v[180:183], a[0:3], v[116:119], 0\n" \
  "ds_read_b128 v[136:139], v16 offset:3072\n" \
  "v_mfma_f32_16x16x32_bf16 v[184:187], a[4:7], v[112:115], 0\n" \
  "v_mfma_f32_16x16x32_bf16 v[188:191], a[4:7], v[116:119], 0\n" \
  "s_waitcnt lgkmcnt(4)\n" \
  "v_mfma_f32_16x16x32_bf16 v[176:179], a[8:11], v[120:123], v[176:179]\n" \
  "ds_read_b128 v[116:119], v16 offset:24576\n" \
  "ds_read_b128 v[112:115], v16 offset:4096\n" \
  "v_mfma_f32_16x16x32_bf16 v[180:183], a[8:11], v[124:127], v[180:183]\n" \
  "v_cvt_pk_bf16_f32 v92, v104, v105\n" \
  "v_cvt_pk_bf16_f32 v93, v106, v107\n" \
  "v_mfma_f32_16x16x32_bf16 v[184:187], a[12:15], v[120:123], v[184:187]\n" \
  "v_cvt_pk_bf16_f32 v94, v108, v109\n" \
  "v_mfma_f32_16x16x32_bf16 v[188:191], a[12:15], v[124:127], v[188:191]\n" \
  "s_waitcnt lgkmcnt(4)\n" \
  "v_mfma_f32_16x16x32_bf16 v[176:179], a[16:19], v[128:131], v[176:179]\n" \
  "ds_read_b128 v[124:127], v16 offset:25600\n" \
  "ds_read_b128 v[120:123], v16 offset:5120\n" \
  "v_mfma_f32_16x16x32_bf16 v[180:183], a[16:19], v[132:135], v[180:183]\n" \
  "v_cvt_pk_bf16_f32 v95, v110, v111\n" \
  "buffer_store_dwordx4 v[92:95], %[zo], s[40:43], s61 offen sc1\n" \
  "v_mfma_f32_16x16x32_bf16 v[184:187], a[20:23], v[128:131], v[184:187]\n" \
  "s_sub_u32 s61, s61, 18857984\n" \
  "v_mfma_f32_16x16x32_bf16 v[188:191], a[20:23], v[132:135], v[188:191]\n" \
  "s_waitcnt lgkmcnt(4)\n" \
  "v_mfma_f32_16x16x32_bf16 v[176:179], a[24:27], v[136:139], v[176:179]\n" \
  "ds_read_b128 v[132:135], v16 offset:26624\n" \
  "v_mfma_f32_16x16x32_bf16 v[180:183], a[24:27], v[140:143], v[180:183]\n" \
  "ds_read_b128 v[128:131], v16 offset:6144\n" \
  "v_mfma_f32_16x16x32_bf16 v[184:187], a[28:31], v[136:139], v[184:187]\n" \
  "v_mfma_f32_16x16x32_bf16 v[188:191], a[28:31], v[140:143], v[188:191]\n" \
  "s_waitcnt lgkmcnt(4)\n" \
  "v_mfma_f32_16x16x32_bf16 v[176:179], a[32:35], v[112:115], v[176:179]\n" \
  "ds_read_b128 v[140:143], v16 offset:27648\n" \
  "v_mfma_f32_16x16x32_bf16 v[180:183], a[32:35], v[116:119], v[180:183]\n" \
  "ds_read_b128 v[136:139], v16 offset:7168\n" \
  "v_mfma_f32_16x16x32_bf16 v[184:187], a[36:39], v[112:115], v[184:187]\n" \
  "v_mfma_f32_16x16x32_bf16 v[188:191], a[36:39], v[116:119], v[188:191]\n" \
  "s_waitcnt lgkmcnt(4)\n" \
  "v_mfma_f32_16x16x32_bf16 v[176:179], a[40:43], v[120:123], v[176:179]\n" \
  "ds_read_b128 v[116:119], v16 offset:28672\n" \
  "v_mfma_f32_16x16x32_bf16 v[180:183], a[40:43], v[124:127], v[180:183]\n" \
  "ds_read_b128 v[112:115], v16 offset:8192\n" \
  "v_mfma_f32_16x16x32_bf16 v[184:187], a[44:47], v[120:123], v[184:187]\n" \
  "v_mfma_f32_16x16x32_bf16 v[188:191], a[44:47], v[124:127], v[188:191]\n" \
  "s_waitcnt lgkmcnt(4)\n" \
  "v_mfma_f32_16x16x32_bf16 v[176:179], a[48:51], v[128:131], v[176:179]\n" \
  "ds_read_b128 v[124:127], v16 offset:29696\n" \
  "v_mfma_f32_16x16x32_bf16 v[180:183], a[48:51], v[132:135], v[180:183]\n" \
  "ds_read_b128 v[120:123], v16 offset:9216\n" \
  "v_mfma_f32_16x16x32_bf16 v[184:187], a[52:55], v[128:131], v[184:187]\n" \
  "v_mfma_f32_16x16x32_bf16 v[188:191], a[52:55], v[132:135], v[188:191]\n" \
  "s_waitcnt lgkmcnt(4)\n" \
  "v_mfma_f32_16x16x32_bf16 v[176:179], a[56:59], v[136:139], v[176:179]\n" \
  "ds_read_b128 v[132:135], v16 offset:30720\n" \
  "v_mfma_f32_16x16x32_bf16 v[180:183], a[56:59], v[140:143], v[180:183]\n" \
  "ds_read_b128 v[128:131], v16 offset:10240\n" \
  "v_mfma_f32_16x16x32_bf16 v[184:187], a[60:63], v[136:139], v[184:187]\n" \
  "v_mfma_f32_16x16x32_bf16 v[188:191], a[60:63], v[140:143], v[188:191]\n" \
  "s_waitcnt lgkmcnt(4)\n" \
  "v_mfma_f32_16x16x32_bf16 v[176:179], a[64:67], v[112:115], v[176:179]\n" \
  "ds_read_b128 v[140:143], v16 offset:31744\n" \
  "v_mfma_f32_16x16x32_bf16 v[180:183], a[64:67], v[116:119], v[180:183]\n" \
  "ds_read_b128 v[136:139], v16 offset:11264\n" \
  "v_mfma_f32_16x16x32_bf16 v[184:187], a[68:71], v[112:115], v[184:187]\n" \
  "v_mfma_f32_16x16x32_bf16 v[188:191], a[68:71], v[116:119], v[188:191]\n" \
  "s_waitcnt lgkmcnt(4)\n" \
  "v_mfma_f32_16x16x32_bf16 v[176:179], a[72:75], v[120:123], v[176:179]\n" \
  "ds_read_b128 v[116:119], v16 offset:32768\n" \
  "v_mfma_f32_16x16x32_bf16 v[180:183], a[72:75], v[124:127], v[180:183]\n" \
  "ds_read_b128 v[112:115], v16 offset:12288\n" \
  "v_mfma_f32_16x16x32_bf16 v[184:187], a[76:79], v[120:123], v[184:187]\n" \
  "v_mfma_f32_16x16x32_bf16 v[188:191], a[76:79], v[124:127], v[188:191]\n" \
  "s_waitcnt lgkmcnt(4)\n" \
  "v_mfma_f32_16x16x32_bf16 v[176:179], a[80:83], v[128:131], v[176:179]\n" \
  "ds_read_b128 v[124:127], v16 offset:33792\n" \
  "v_mfma_f32_16x16x32_bf16 v[180:183], a[80:83], v[132:135], v[180:183]\n" \
  "ds_read_b128 v[120:123], v16 offset:13312\n" \
  "v_mfma_f32_16x16x32_bf16 v[184:187], a[84:87], v[128:131], v[184:187]\n" \
  "v_mfma_f32_16x16x32_bf16 v[188:191], a[84:87], v[132:135], v[188:191]\n" \
  "s_waitcnt lgkmcnt(4)\n" \
  "v_mfma_f32_16x16x32_bf16 v[176:179], a[88:91], v[136:139], v[176:179]\n" \
  "ds_read_b128 v[132:135], v16 offset:34816\n" \
  "v_mfma_f32_16x16x32_bf16 v[180:183], a[88:91], v[140:143], v[180:183]\n" \
  "ds_read_b128 v[128:131], v16 offset:14336\n" \
  "v_mfma_f32_16x16x32_bf16 v[184:187], a[92:95], v[136:139], v[184:187]\n" \
  "v_mfma_f32_16x16x32_bf16 v[188:191], a[92:95], v[140:143], v[188:191]\n" \
  "s_waitcnt lgkmcnt(4)\n" \
  "v_mfma_f32_16x16x32_bf16 v[176:179], a[96:99], v[112:115], v[176:179]\n" \
  "ds_read_b128 v[140:143], v16 offset:35840\n" \
  "v_mfma_f32_16x16x32_bf16 v[180:183], a[96:99], v[116:119], v[180:183]\n" \
  "ds_read_b128 v[136:139], v16 offset:15360\n" \
  "v_mfma_f32_16x16x32_bf16 v[184:187], a[100:103], v[112:115], v[184:187]\n" \
  "ds_read_b128 v[160:163], v19 offset:4096\n" \
  "v_mfma_f32_16x16x32_bf16 v[188:191], a[100:103], v[116:119], v[188:191]\n" \
  "ds_read_b128 v[144:147], v19 offset:0\n" \
  "s_waitcnt lgkmcnt(6)\n" \
  "v_mfma_f32_16x16x32_bf16 v[176:179], a[104:107], v[120:123], v[176:179]\n" \
  "ds_read_b128 v[116:119], v16 offset:36864\n" \
  "v_mfma_f32_16x16x32_bf16 v[180:183], a[104:107], v[124:127], v[180:183]\n" \
  "ds_read_b128 v[112:115], v16 offset:16384\n" \
  "v_mfma_f32_16x16x32_bf16 v[184:187], a[108:111], v[120:123], v[184:187]\n" \
  "ds_read_b128 v[164:167], v19 offset:5120\n" \
  "v_mfma_f32_16x16x32_bf16 v[188:191], a[108:111], v[124:127], v[188:191]\n" \
  "ds_read_b128 v[148:151], v19 offset:1024\n" \
  "s_waitcnt lgkmcnt(8)\n" \
  "v_mfma_f32_16x16x32_bf16 v[176:179], a[112:115], v[128:131], v[176:179]\n" \
  "ds_read_b128 v[124:127], v16 offset:37888\n" \
  "v_mfma_f32_16x16x32_bf16 v[180:183], a[112:115], v[132:135], v[180:183]\n" \
  "ds_read_b128 v[120:123], v16 offset:17408\n" \
  "v_mfma_f32_16x16x32_bf16 v[184:187], a[116:119], v[128:131], v[184:187]\n" \
  "ds_read_b128 v[168:171], v19 offset:6144\n" \
  "v_mfma_f32_16x16x32_bf16 v[188:191], a[116:119], v[132:135], v[188:191]\n" \
  "ds_read_b128 v[152:155], v19 offset:2048\n" \
  "s_waitcnt lgkmcnt(10)\n" \
  "v_mfma_f32_16x16x32_bf16 v[176:179], a[120:123], v[136:139], v[176:179]\n" \
  "ds_read_b128 v[132:135], v16 offset:38912\n" \
  "v_mfma_f32_16x16x32_bf16 v[180:183], a[120:123], v[140:143], v[180:183]\n" \
  "ds_read_b128 v[128:131], v16 offset:18432\n" \
  "v_mfma_f32_16x16x32_bf16 v[184:187], a[124:127], v[136:139], v[184:187]\n" \
  "ds_read_b128 v[172:175], v19 offset:7168\n" \
  "v_mfma_f32_16x16x32_bf16 v[188:191], a[124:127], v[140:143], v[188:191]\n" \
  "ds_read_b128 v[156:159], v19 offset:3072\n" \
  "s_waitcnt lgkmcnt(10)\n" \
  "v_mfma_f32_16x16x32_bf16 v[176:179], a[128:131], v[112:115], v[176:179]\n" \
  "ds_read_b128 v[140:143], v16 offset:39936\n" \
  "v_mfma_f32_16x16x32_bf16 v[180:183], a[128:131], v[116:119], v[180:183]\n" \
  "ds_read_b128 v[136:139], v16 offset:19456\n" \
  "v_mfma_f32_16x16x32_bf16 v[184:187], a[132:135], v[112:115], v[184:187]\n" \
  "v_xor_b32 v19, 0x2000, v19\n" \
  "v_mfma_f32_16x16x32_bf16 v[188:191], a[132:135], v[116:119], v[188:191]\n" \
  "s_waitcnt lgkmcnt(8)\n" \
  "v_mfma_f32_16x16x32_bf16 v[176:179], a[136:139], v[120:123], v[176:179]\n" \
  "v_mfma_f32_16x16x32_bf16 v[180:183], a[136:139], v[124:127], v[180:183]\n" \
  "v_mfma_f32_16x16x32_bf16 v[184:187], a[140:143], v[120:123], v[184:187]\n" \
  "v_mfma_f32_16x16x32_bf16 v[188:191], a[140:143], v[124:127], v[188:191]\n" \
  "s_waitcnt lgkmcnt(4)\n" \
  "v_mfma_f32_16x16x32_bf16 v[176:179], a[144:147], v[128:131], v[176:179]\n" \
  "s_add_u32 s62, s62, 1\n" \
  "v_mfma_f32_16x16x32_bf16 v[180:183], a[144:147], v[132:135], v[180:183]\n" \
  "s_waitcnt vmcnt(0)\n" \
  "v_mfma_f32_16x16x32_bf16 v[184:187], a[148:151], v[128:131], v[184:187]\n" \
  "v_mov_b32 v18, s62\n" \
  "v_mfma_f32_16x16x32_bf16 v[188:191], a[148:151], v[132:135], v[188:191]\n" \
  "buffer_store_dword v18, %[fo], s[40:43], s63 offen sc1\n" \
  "s_waitcnt lgkmcnt(0)\n" \
  "v_mfma_f32_16x16x32_bf16 v[176:179], a[152:155], v[136:139], v[176:179]\n" \
  "v_mfma_f32_16x16x32_bf16 v[180:183], a[152:155], v[140:143], v[180:183]\n" \
  "v_mfma_f32_16x16x32_bf16 v[184:187], a[156:159], v[136:139], v[184:187]\n" \
  "v_mfma_f32_16x16x32_bf16 v[188:191], a[156:159], v[140:143], v[188:191]\n" \
  "v_mfma_f32_16x16x32_bf16 v[96:99], v[144:147], a[160:163], 0\n" \
  "v_mfma_f32_16x16x32_bf16 v[100:103], v[160:163], a[160:163], 0\n" \
  "v_cvt_pk_bf16_f32 v80, v176, v177\n" \
  "v_cvt_pk_bf16_f32 v81, v178, v179\n" \
  "v_mfma_f32_16x16x32_bf16 v[96:99], v[148:151], a[164:167], v[96:99]\n" \
  "v_cvt_pk_bf16_f32 v82, v184, v185\n" \
  "v_cvt_pk_bf16_f32 v83, v186, v187\n" \
  "v_mfma_f32_16x16x32_bf16 v[100:103], v[164:167], a[164:167], v[100:103]\n" \
  "ds_write_b128 v17, v[80:83] offset:0\n" \
  "v_mfma_f32_16x16x32_bf16 v[96:99], v[152:155], a[168:171], v[96:99]\n" \
  "v_mfma_f32_16x16x32_bf16 v[100:103], v[168:171], a[168:171], v[100:103]\n" \
  "v_mfma_f32_16x16x32_bf16 v[96:99], v[156:159], a[172:175], v[96:99]\n" \
  "v_mfma_f32_16x16x32_bf16 v[100:103], v[172:175], a[172:175], v[100:103]\n" \
  "v_mfma_f32_16x16x32_bf16 v[104:107], v[144:147], a[176:179], 0\n" \
  "v_mfma_f32_16x16x32_bf16 v[108:111], v[160:163], a[176:179], 0\n" \
  "v_cvt_pk_bf16_f32 v84, v180, v181\n" \
  "v_cvt_pk_bf16_f32 v85, v182, v183\n" \
  "v_mfma_f32_16x16x32_bf16 v[104:107], v[148:151], a[180:183], v[104:107]\n" \
  "v_cvt_pk_bf16_f32 v86, v188, v189\n" \
  "v_cvt_pk_bf16_f32 v87, v190, v191\n" \
  "v_mfma_f32_16x16x32_bf16 v[108:111], v[164:167], a[180:183], v[108:111]\n" \
  "ds_write_b128 v17, v[84:87] offset:4096\n" \
  "v_xor_b32 v17, 0x2000, v17\n" \
  "v_mfma_f32_16x16x32_bf16 v[104:107], v[152:155], a[184:187], v[104:107]\n" \
  "v_cvt_pk_bf16_f32 v88, v96, v97\n" \
  "v_cvt_pk_bf16_f32 v89, v98, v99\n" \
  "v_mfma_f32_16x16x32_bf16 v[108:111], v[168:171], a[184:187], v[108:111]\n" \
  "v_cvt_pk_bf16_f32 v90, v100, v101\n" \
  "v_cvt_pk_bf16_f32 v91, v102, v103\n" \
  "v_mfma_f32_16x16x32_bf16 v[104:107], v[156:159], a[188:191], v[104:107]\n" \
  "buffer_store_dwordx4 v[88:91], %[zo], s[40:43], s61 offen sc1\n" \
  "s_add_u32 s61, s61, 2097152\n" \
  "v_mfma_f32_16x16x32_bf16 v[108:111], v[172:175], a[188:191], v[108:111]\n" \
  "v_mfma_f32_16x16x32_bf16 v[96:99], v[144:147], a[192:195], 0\n" \
  "v_mfma_f32_16x16x32_bf16 v[100:103], v[160:163], a[192:195], 0\n" \
  "s_waitcnt lgkmcnt(0)\n" \
  "s_barrier\n" \
  "v_mfma_f32_16x16x32_bf16 v[96:99], v[148:151], a[196:199], v[96:99]\n" \
  "s_add_u32 m0, s56, 0\n" \
  "s_add_u32 s53, s57, 0\n" \
  "v_mfma_f32_16x16x32_bf16 v[100:103], v[164:167], a[196:199], v[100:103]\n" \
  "buffer_load_dwordx4 %[l16], s[40:43], s53 offen offset:0 lds\n" \
  "v_cvt_pk_bf16_f32 v92, v104, v105\n" \
  "v_mfma_f32_16x16x32_bf16 v[96:99], v[152:155], a[200:203], v[96:99]\n" \
  "v_cvt_pk_bf16_f32 v93, v106, v107\n" \
  "v_cvt_pk_bf16_f32 v94, v108, v109\n" \
  "v_mfma_f32_16x16x32_bf16 v[100:103], v[168:171], a[200:203], v[100:103]\n" \
  "v_cvt_pk_bf16_f32 v95, v110, v111\n" \
  "buffer_store_dwordx4 v[92:95], %[zo], s[40:43], s61 offen sc1\n" \
  "v_mfma_f32_16x16x32_bf16 v[96:99], v[156:159], a[204:207], v[96:99]\n" \
  "s_add_u32 s61, s61, 2097152\n" \
  "v_mfma_f32_16x16x32_bf16 v[100:103], v[172:175], a[204:207], v[100:103]\n" \
  "v_mfma_f32_16x16x32_bf16 v[104:107], v[144:147], a[208:211], 0\n" \
  "v_mfma_f32_16x16x32_bf16 v[108:111], v[160:163], a[208:211], 0\n" \
  "buffer_load_dwordx4 %[l16], s[40:43], s53 offen offset:1024 lds\n" \
  "buffer_load_dwordx4 %[l16], s[40:43], s53 offen offset:2048 lds\n" \
  "v_mfma_f32_16x16x32_bf16 v[104:107], v[148:151], a[212:215], v[104:107]\n" \
  "buffer_load_dwordx4 %[l16], s[40:43], s53 offen offset:3072 lds\n" \
  "s_add_u32 m0, s56, 4096\n" \
  "v_mfma_f32_16x16x32_bf16 v[108:111], v[164:167], a[212:215], v[108:111]\n" \
  "v_cvt_pk_bf16_f32 v88, v96, v97\n" \
  "v_cvt_pk_bf16_f32 v89, v98, v99\n" \
  "v_mfma_f32_16x16x32_bf16 v[104:107], v[152:155], a[216:219], v[104:107]\n" \
  "v_cvt_pk_bf16_f32 v90, v100, v101\n" \
  "v_cvt_pk_bf16_f32 v91, v102, v103\n" \
  "v_mfma_f32_16x16x32_bf16 v[108:111], v[168:171], a[216:219], v[108:111]\n" \
  "buffer_store_dwordx4 v[88:91], %[zo], s[40:43], s61 offen sc1\n" \
  "s_add_u32 s61, s61, 2097152\n" \
  "v_mfma_f32_16x16x32_bf16 v[104:107], v[156:159], a[220:223], v[104:107]\n" \
  "v_mfma_f32_16x16x32_bf16 v[108:111], v[172:175], a[220:223], v[108:111]\n" \
  "v_mfma_f32_16x16x32_bf16 v[96:99], v[144:147], a[224:227], 0\n" \
  "v_mfma_f32_16x16x32_bf16 v[100:103], v[160:163], a[224:227], 0\n" \
  "s_add_u32 s53, s57, 4096\n" \
  "buffer_load_dwordx4 %[l16], s[40:43], s53 offen offset:0 lds\n" \
  "v_mfma_f32_16x16x32_bf16 v[96:99], v[148:151], a[228:231], v[96:99]\n" \
  "buffer_load_dwordx4 %[l16], s[40:43], s53 offen offset:1024 lds\n" \
  "buffer_load_dwordx4 %[l16], s[40:43], s53 offen offset:2048 lds\n" \
  "v_mfma_f32_16x16x32_bf16 v[100:103], v[164:167], a[228:231], v[100:103]\n" \
  "v_cvt_pk_bf16_f32 v92, v104, v105\n" \
  "v_cvt_pk_bf16_f32 v93, v106, v107\n" \
  "v_mfma_f32_16x16x32_bf16 v[96:99], v[152:155], a[232:235], v[96:99]\n" \
  "v_cvt_pk_bf16_f32 v94, v108, v109\n" \
  "v_cvt_pk_bf16_f32 v95, v110, v111\n" \
  "v_mfma_f32_16x16x32_bf16 v[100:103], v[168:171], a[232:235], v[100:103]\n" \
  "buffer_store_dwordx4 v[92:95], %[zo], s[40:43], s61 offen sc1\n" \
  "s_add_u32 s61, s61, 2097152\n" \
  "v_mfma_f32_16x16x32_bf16 v[96:99], v[156:159], a[236:239], v[96:99]\n" \
  "v_mfma_f32_16x16x32_bf16 v[100:103], v[172:175], a[236:239], v[100:103]\n" \
  "v_mfma_f32_16x16x32_bf16 v[104:107], v[144:147], a[240:243], 0\n" \
  "v_mfma_f32_16x16x32_bf16 v[108:111], v[160:163], a[240:243], 0\n" \
  "buffer_load_dwordx4 %[l16], s[40:43], s53 offen offset:3072 lds\n" \
  "s_add_u32 m0, s56, 8192\n" \
  "v_mfma_f32_16x16x32_bf16 v[104:107], v[148:151], a[244:247], v[104:107]\n" \
  "s_add_u32 s53, s57, 8192\n" \
  "buffer_load_dwordx4 %[l16], s[40:43], s53 offen offset:0 lds\n" \
  "v_mfma_f32_16x16x32_bf16 v[108:111], v[164:167], a[244:247], v[108:111]\n" \
  "v_cvt_pk_bf16_f32 v88, v96, v97\n" \
  "v_cvt_pk_bf16_f32 v89, v98, v99\n" \
  "v_mfma_f32_16x16x32_bf16 v[104:107], v[152:155], a[248:251], v[104:107]\n" \
  "v_cvt_pk_bf16_f32 v90, v100, v101\n" \
  "v_cvt_pk_bf16_f32 v91, v102, v103\n" \
  "v_mfma_f32_16x16x32_bf16 v[108:111], v[168:171], a[248:251], v[108:111]\n" \
  "buffer_store_dwordx4 v[88:91], %[zo], s[40:43], s61 offen sc1\n" \
  "s_add_u32 s61, s61, 2097152\n" \
  "v_mfma_f32_16x16x32_bf16 v[104:107], v[156:159], a[252:255], v[104:107]\n" \
  "v_mfma_f32_16x16x32_bf16 v[108:111], v[172:175], a[252:255], v[108:111]\n" \
  "v_mfma_f32_16x16x32_bf16 v[96:99], v[144:147], v[192:195], 0\n" \
  "v_mfma_f32_16x16x32_bf16 v[100:103], v[160:163], v[192:195], 0\n" \
  "buffer_load_dwordx4 %[l16], s[40:43], s53 offen offset:1024 lds\n" \
  "s_add_u32 s57, s57, 40960\n" \
  "v_mfma_f32_16x16x32_bf16 v[96:99], v[148:151], v[196:199], v[96:99]\n" \
  "s_xor_b32 s56, s56, 0x10000\n" \
  "v_cvt_pk_bf16_f32 v92, v104, v105\n" \
  "v_mfma_f32_16x16x32_bf16 v[100:103], v[164:167], v[196:199], v[100:103]\n" \
  "v_cvt_pk_bf16_f32 v93, v106, v107\n" \
  "v_cvt_pk_bf16_f32 v94, v108, v109\n" \
  "v_mfma_f32_16x16x32_bf16 v[96:99], v[152:155], v[200:203], v[96:99]\n" \
  "v_cvt_pk_bf16_f32 v95, v110, v111\n" \
  "buffer_store_dwordx4 v[92:95], %[zo], s[40:43], s61 offen sc1\n" \
  "v_mfma_f32_16x16x32_bf16 v[100:103], v[168:171], v[200:203], v[100:103]\n" \
  "s_add_u32 s61, s61, 2097152\n" \
  "v_mfma_f32_16x16x32_bf16 v[96:99], v[156:159], v[204:207], v[96:99]\n" \
  "v_mfma_f32_16x16x32_bf16 v[100:103], v[172:175], v[204:207], v[100:103]\n" \
  "v_mfma_f32_16x16x32_bf16 v[104:107], v[144:147], v[208:211], 0\n" \
  "v_mfma_f32_16x16x32_bf16 v[108:111], v[160:163], v[208:211], 0\n" \
  "v_xor_b32 v16, 0x10000, v16\n" \
  "ds_read_b128 v[116:119], v16 offset:20480\n" \
  "v_mfma_f32_16x16x32_bf16 v[104:107], v[148:151], v[212:215], v[104:107]\n" \
  "ds_read_b128 v[112:115], v16 offset:0\n" \
  "v_cvt_pk_bf16_f32 v88, v96, v97\n" \
  "v_mfma_f32_16x16x32_bf16 v[108:111], v[164:167], v[212:215], v[108:111]\n" \
  "v_cvt_pk_bf16_f32 v89, v98, v99\n" \
  "v_cvt_pk_bf16_f32 v90, v100, v101\n" \
  "v_mfma_f32_16x16x32_bf16 v[104:107], v[152:155], v[216:219], v[104:107]\n" \
  "v_cvt_pk_bf16_f32 v91, v102, v103\n" \
  "buffer_store_dwordx4 v[88:91], %[zo], s[40:43], s61 offen sc1\n" \
  "v_mfma_f32_16x16x32_bf16 v[108:111], v[168:171], v[216:219], v[108:111]\n" \
  "s_add_u32 s61, s61, 2097152\n" \
  "v_mfma_f32_16x16x32_bf16 v[104:107], v[156:159], v[220:223], v[104:107]\n" \
  "v_mfma_f32_16x16x32_bf16 v[108:111], v[172:175], v[220:223], v[108:111]\n" \
  "v_mfma_f32_16x16x32_bf16 v[96:99], v[144:147], v[224:227], 0\n" \
  "v_mfma_f32_16x16x32_bf16 v[100:103], v[160:163], v[224:227], 0\n" \
  "ds_read_b128 v[124:127], v16 offset:21504\n" \
  "ds_read_b128 v[120:123], v16 offset:1024\n" \
  "v_mfma_f32_16x16x32_bf16 v[96:99], v[148:151], v[228:231], v[96:99]\n" \
  "v_cvt_pk_bf16_f32 v92, v104, v105\n" \
  "v_cvt_pk_bf16_f32 v93, v106, v107\n" \
  "v_mfma_f32_16x16x32_bf16 v[100:103], v[164:167], v[228:231], v[100:103]\n" \
  "v_cvt_pk_bf16_f32 v94, v108, v109\n" \
  "v_cvt_pk_bf16_f32 v95, v110, v111\n" \
  "v_mfma_f32_16x16x32_bf16 v[96:99], v[152:155], v[232:235], v[96:99]\n" \
  "buffer_store_dwordx4 v[92:95], %[zo], s[40:43], s61 offen sc1\n" \
  "s_add_u32 s61, s61, 2097152\n" \
  "v_mfma_f32_16x16x32_bf16 v[100:103], v[168:171], v[232:235], v[100:103]\n" \
  "v_mfma_f32_16x16x32_bf16 v[96:99], v[156:159], v[236:239], v[96:99]\n" \
  "v_mfma_f32_16x16x32_bf16 v[100:103], v[172:175], v[236:239], v[100:103]\n" \
  "v_mfma_f32_16x16x32_bf16 v[104:107], v[144:147], v[240:243], 0\n" \
  "v_mfma_f32_16x16x32_bf16 v[108:111], v[160:163], v[240:243], 0\n" \
  "ds_read_b128 v[132:135], v16 offset:22528\n" \
  "ds_read_b128 v[128:131], v16 offset:2048\n" \
  "v_mfma_f32_16x16x32_bf16 v[104:107], v[148:151], v[244:247], v[104:107]\n" \
  "v_cvt_pk_bf16_f32 v88, v96, v97\n" \
  "v_cvt_pk_bf16_f32 v89, v98, v99\n" \
  "v_mfma_f32_16x16x32_bf16 v[108:111], v[164:167], v[244:247], v[108:111]\n" \
  "v_cvt_pk_bf16_f32 v90, v100, v101\n" \
  "v_cvt_pk_bf16_f32 v91, v102, v103\n" \
  "v_mfma_f32_16x16x32_bf16 v[104:107], v[152:155], v[248:251], v[104:107]\n" \
  "buffer_store_dwordx4 v[88:91], %[zo], s[40:43], s61 offen sc1\n" \
  "s_add_u32 s61, s61, 2097152\n" \
  "v_mfma_f32_16x16x32_bf16 v[108:111], v[168:171], v[248:251], v[108:111]\n" \
  "v_mfma_f32_16x16x32_bf16 v[104:107], v[156:159], v[252:255], v[104:107]\n" \
  "v_mfma_f32_16x16x32_bf16 v[108:111], v[172:175], v[252:255], v[108:111]\n" \
  "s_sub_u32 s65, s65, 1\n" \
  "s_cmp_lg_u32 s65, 0\n" \
  "s_cbranch_scc1 Lvz%=\n" \
  "ds_read_b128 v[160:163], v19 offset:4096\n" \
  "ds_read_b128 v[144:147], v19 offset:0\n" \
  "ds_read_b128 v[164:167], v19 offset:5120\n" \
  "ds_read_b128 v[148:151], v19 offset:1024\n" \
  "ds_read_b128 v[168:171], v19 offset:6144\n" \
  "ds_read_b128 v[152:155], v19 offset:2048\n" \
  "ds_read_b128 v[172:175], v19 offset:7168\n" \
  "ds_read_b128 v[156:159], v19 offset:3072\n" \
  "v_xor_b32 v19, 0x2000, v19\n" \
  "v_cvt_pk_bf16_f32 v92, v104, v105\n" \
  "v_cvt_pk_bf16_f32 v93, v106, v107\n" \
  "v_cvt_pk_bf16_f32 v94, v108, v109\n" \
  "v_cvt_pk_bf16_f32 v95, v110, v111\n" \
  "buffer_store_dwordx4 v[92:95], %[zo], s[40:43], s61 offen sc1\n" \
  "s_sub_u32 s61, s61, 18857984\n" \
  "s_waitcnt lgkmcnt(6)\n" \
  "v_mfma_f32_16x16x32_bf16 v[96:99], v[144:147], a[160:163], 0\n" \
  "v_mfma_f32_16x16x32_bf16 v[100:103], v[160:163], a[160:163], 0\n" \
  "s_waitcnt lgkmcnt(4)\n" \
  "v_mfma_f32_16x16x32_bf16 v[96:99], v[148:151], a[164:167], v[96:99]\n" \
  "v_mfma_f32_16x16x32_bf16 v[100:103], v[164:167], a[164:167], v[100:103]\n" \
  "s_waitcnt lgkmcnt(2)\n" \
  "v_mfma_f32_16x16x32_bf16 v[96:99], v[152:155], a[168:171], v[96:99]\n" \
  "v_mfma_f32_16x16x32_bf16 v[100:103], v[168:171], a[168:171], v[100:103]\n" \
  "s_waitcnt lgkmcnt(0)\n" \
  "v_mfma_f32_16x16x32_bf16 v[96:99], v[156:159], a[172:175], v[96:99]\n" \
  "v_mfma_f32_16x16x32_bf16 v[100:103], v[172:175], a[172:175], v[100:103]\n" \
  "v_mfma_f32_16x16x32_bf16 v[104:107], v[144:147], a[176:179], 0\n" \
  "v_mfma_f32_16x16x32_bf16 v[108:111], v[160:163], a[176:179], 0\n" \
  "v_cvt_pk_bf16_f32 v88, v96, v97\n" \
  "v_cvt_pk_bf16_f32 v89, v98, v99\n" \
  "v_mfma_f32_16x16x32_bf16 v[104:107], v[148:151], a[180:183], v[104:107]\n" \
  "v_cvt_pk_bf16_f32 v90, v100, v101\n" \
  "v_cvt_pk_bf16_f32 v91, v102, v103\n" \
  "v_mfma_f32_16x16x32_bf16 v[108:111], v[164:167], a[180:183], v[108:111]\n" \
  "buffer_store_dwordx4 v[88:91], %[zo], s[40:43], s61 offen sc1\n" \
  "s_add_u32 s61, s61, 2097152\n" \
  "v_mfma_f32_16x16x32_bf16 v[104:107], v[152:155], a[184:187], v[104:107]\n" \
  "v_mfma_f32_16x16x32_bf16 v[108:111], v[168:171], a[184:187], v[108:111]\n" \
  "v_mfma_f32_16x16x32_bf16 v[104:107], v[156:159], a[188:191], v[104:107]\n" \
  "v_mfma_f32_16x16x32_bf16 v[108:111], v[172:175], a[188:191], v[108:111]\n" \
  "v_mfma_f32_16x16x32_bf16 v[96:99], v[144:147], a[192:195], 0\n" \
  "v_mfma_f32_16x16x32_bf16 v[100:103], v[160:163], a[192:195], 0\n" \
  "v_cvt_pk_bf16_f32 v92, v104, v105\n" \
  "v_cvt_pk_bf16_f32 v93, v106, v107\n" \
  "v_mfma_f32_16x16x32_bf16 v[96:99], v[148:151], a[196:199], v[96:99]\n" \
  "v_cvt_pk_bf16_f32 v94, v108, v109\n" \
  "v_cvt_pk_bf16_f32 v95, v110, v111\n" \
  "v_mfma_f32_16x16x32_bf16 v[100:103], v[164:167], a[196:199], v[100:103]\n" \
  "buffer_store_dwordx4 v[92:95], %[zo], s[40:43], s61 offen sc1\n" \
  "s_add_u32 s61, s61, 2097152\n" \
  "v_mfma_f32_16x16x32_bf16 v[96:99], v[152:155], a[200:203], v[96:99]\n" \
  "v_mfma_f32_16x16x32_bf16 v[100:103], v[168:171], a[200:203], v[100:103]\n" \
  "v_mfma_f32_16x16x32_bf16 v[96:99], v[156:159], a[204:207], v[96:99]\n" \
  "v_mfma_f32_16x16x32_bf16 v[100:103], v[172:175], a[204:207], v[100:103]\n" \
  "v_mfma_f32_16x16x32_bf16 v[104:107], v[144:147], a[208:211], 0\n" \
  "v_mfma_f32_16x16x32_bf16 v[108:111], v[160:163], a[208:211], 0\n" \
  "v_cvt_pk_bf16_f32 v88, v96, v97\n" \
  "v_cvt_pk_bf16_f32 v89, v98, v99\n" \
  "v_mfma_f32_16x16x32_bf16 v[104:107], v[148:151], a[212:215], v[104:107]\n" \
  "v_cvt_pk_bf16_f32 v90, v100, v101\n" \
  "v_cvt_pk_bf16_f32 v91, v102, v103\n" \
  "v_mfma_f32_16x16x32_bf16 v[108:111], v[164:167], a[212:215], v[108:111]\n" \
  "buffer_store_dwordx4 v[88:91], %[zo], s[40:43], s61 offen sc1\n" \
  "s_add_u32 s61, s61, 2097152\n" \
  "v_mfma_f32_16x16x32_bf16 v[104:107], v[152:155], a[216:219], v[104:107]\n" \
  "v_mfma_f32_16x16x32_bf16 v[108:111], v[168:171], a[216:219], v[108:111]\n" \
  "v_mfma_f32_16x16x32_bf16 v[104:107], v[156:159], a[220:223], v[104:107]\n" \
  "v_mfma_f32_16x16x32_bf16 v[108:111], v[172:175], a[220:223], v[108:111]\n" \
  "v_mfma_f32_16x16x32_bf16 v[96:99], v[144:147], a[224:227], 0\n" \
  "v_mfma_f32_16x16x32_bf16 v[100:103], v[160:163], a[224:227], 0\n" \
  "v_cvt_pk_bf16_f32 v92, v104, v105\n" \
  "v_cvt_pk_bf16_f32 v93, v106, v107\n" \
  "v_mfma_f32_16x16x32_bf16 v[96:99], v[148:151], a[228:231], v[96:99]\n" \
  "v_cvt_pk_bf16_f32 v94, v108, v109\n" \
  "v_cvt_pk_bf16_f32 v95, v110, v111\n" \
  "v_mfma_f32_16x16x32_bf16 v[100:103], v[164:167], a[228:231], v[100:103]\n" \
  "buffer_store_dwordx4 v[92:95], %[zo], s[40:43], s61 offen sc1\n" \
  "s_add_u32 s61, s61, 2097152\n" \
  "v_mfma_f32_16x16x32_bf16 v[96:99], v[152:155], a[232:235], v[96:99]\n" \
  "v_mfma_f32_16x16x32_bf16 v[100:103], v[168:171], a[232:235], v[100:103]\n" \
  "v_mfma_f32_16x16x32_bf16 v[96:99], v[156:159], a[236:239], v[96:99]\n" \
  "v_mfma_f32_16x16x32_bf16 v[100:103], v[172:175], a[236:239], v[100:103]\n" \
  "v_mfma_f32_16x16x32_bf16 v[104:107], v[144:147], a[240:243], 0\n" \
  "v_mfma_f32_16x16x32_bf16 v[108:111], v[160:163], a[240:243], 0\n" \
  "s_add_u32 s62, s62, 1\n" \
  "s_waitcnt vmcnt(0)\n" \
  "v_mfma_f32_16x16x32_bf16 v[104:107], v[148:151], a[244:247], v[104:107]\n" \
  "v_mov_b32 v18, s62\n" \
  "buffer_store_dword v18, %[fo], s[40:43], s63 offen sc1\n" \
  "v_mfma_f32_16x16x32_bf16 v[108:111], v[164:167], a[244:247], v[108:111]\n" \
  "v_cvt_pk_bf16_f32 v88, v96, v97\n" \
  "v_cvt_pk_bf16_f32 v89, v98, v99\n" \
  "v_mfma_f32_16x16x32_bf16 v[104:107], v[152:155], a[248:251], v[104:107]\n" \
  "v_cvt_pk_bf16_f32 v90, v100, v101\n" \
  "v_cvt_pk_bf16_f32 v91, v102, v103\n" \
  "v_mfma_f32_16x16x32_bf16 v[108:111], v[168:171], a[248:251], v[108:111]\n" \
  "buffer_store_dwordx4 v[88:91], %[zo], s[40:43], s61 offen sc1\n" \
  "s_add_u32 s61, s61, 2097152\n" \
  "v_mfma_f32_16x16x32_bf16 v[104:107], v[156:159], a[252:255], v[104:107]\n" \
  "v_mfma_f32_16x16x32_bf16 v[108:111], v[172:175], a[252:255], v[108:111]\n" \
  "v_mfma_f32_16x16x32_bf16 v[96:99], v[144:147], v[192:195], 0\n" \
  "v_mfma_f32_16x16x32_bf16 v[100:103], v[160:163], v[192:195], 0\n" \
  "v_cvt_pk_bf16_f32 v92, v104, v105\n" \
  "v_cvt_pk_bf16_f32 v93, v106, v107\n" \
  "v_mfma_f32_16x16x32_bf16 v[96:99], v[148:151], v[196:199], v[96:99]\n" \
  "v_cvt_pk_bf16_f32 v94, v108, v109\n" \
  "v_cvt_pk_bf16_f32 v95, v110, v111\n" \
  "v_mfma_f32_16x16x32_bf16 v[100:103], v[164:167], v[196:199], v[100:103]\n" \
  "buffer_store_dwordx4 v[92:95], %[zo], s[40:43], s61 offen sc1\n" \
  "s_add_u32 s61, s61, 2097152\n" \
  "v_mfma_f32_16x16x32_bf16 v[96:99], v[152:155], v[200:203], v[96:99]\n" \
  "v_mfma_f32_16x16x32_bf16 v[100:103], v[168:171], v[200:203], v[100:103]\n" \
  "v_mfma_f32_16x16x32_bf16 v[96:99], v[156:159], v[204:207], v[96:99]\n" \
  "v_mfma_f32_16x16x32_bf16 v[100:103], v[172:175], v[204:207], v[100:103]\n" \
  "v_mfma_f32_16x16x32_bf16 v[104:107], v[144:147], v[208:211], 0\n" \
  "v_mfma_f32_16x16x32_bf16 v[108:111], v[160:163], v[208:211], 0\n" \
  "v_cvt_pk_bf16_f32 v88, v96, v97\n" \
  "v_cvt_pk_bf16_f32 v89, v98, v99\n" \
  "v_mfma_f32_16x16x32_bf16 v[104:107], v[148:151], v[212:215], v[104:107]\n" \
  "v_cvt_pk_bf16_f32 v90, v100, v101\n" \
  "v_cvt_pk_bf16_f32 v91, v102, v103\n" \
  "v_mfma_f32_16x16x32_bf16 v[108:111], v[164:167], v[212:215], v[108:111]\n" \
  "buffer_store_dwordx4 v[88:91], %[zo], s[40:43], s61 offen sc1\n" \
  "s_add_u32 s61, s61, 2097152\n" \
  "v_mfma_f32_16x16x32_bf16 v[104:107], v[152:155], v[216:219], v[104:107]\n" \
  "v_mfma_f32_16x16x32_bf16 v[108:111], v[168:171], v[216:219], v[108:111]\n" \
  "v_mfma_f32_16x16x32_bf16 v[104:107], v[156:159], v[220:223], v[104:107]\n" \
  "v_mfma_f32_16x16x32_bf16 v[108:111], v[172:175], v[220:223], v[108:111]\n" \
  "v_mfma_f32_16x16x32_bf16 v[96:99], v[144:147], v[224:227], 0\n" \
  "v_mfma_f32_16x16x32_bf16 v[100:103], v[160:163], v[224:227], 0\n" \
  "v_cvt_pk_bf16_f32 v92, v104, v105\n" \
  "v_cvt_pk_bf16_f32 v93, v106, v107\n" \
  "v_mfma_f32_16x16x32_bf16 v[96:99], v[148:151], v[228:231], v[96:99]\n" \
  "v_cvt_pk_bf16_f32 v94, v108, v109\n" \
  "v_cvt_pk_bf16_f32 v95, v110, v111\n" \
  "v_mfma_f32_16x16x32_bf16 v[100:103], v[164:167], v[228:231], v[100:103]\n" \
  "buffer_store_dwordx4 v[92:95], %[zo], s[40:43], s61 offen sc1\n" \
  "s_add_u32 s61, s61, 2097152\n" \
  "v_mfma_f32_16x16x32_bf16 v[96:99], v[152:155], v[232:235], v[96:99]\n" \
  "v_mfma_f32_16x16x32_bf16 v[100:103], v[168:171], v[232:235], v[100:103]\n" \
  "v_mfma_f32_16x16x32_bf16 v[96:99], v[156:159], v[236:239], v[96:99]\n" \
  "v_mfma_f32_16x16x32_bf16 v[100:103], v[172:175], v[236:239], v[100:103]\n" \
  "v_mfma_f32_16x16x32_bf16 v[104:107], v[144:147], v[240:243], 0\n" \
  "v_mfma_f32_16x16x32_bf16 v[108:111], v[160:163], v[240:243], 0\n" \
  "v_cvt_pk_bf16_f32 v88, v96, v97\n" \
  "v_cvt_pk_bf16_f32 v89, v98, v99\n" \
  "v_mfma_f32_16x16x32_bf16 v[104:107], v[148:151], v[244:247], v[104:107]\n" \
  "v_cvt_pk_bf16_f32 v90, v100, v101\n" \
  "v_cvt_pk_bf16_f32 v91, v102, v103\n" \
  "v_mfma_f32_16x16x32_bf16 v[108:111], v[164:167], v[244:247], v[108:111]\n" \
  "buffer_store_dwordx4 v[88:91], %[zo], s[40:43], s61 offen sc1\n" \
  "s_add_u32 s61, s61, 2097152\n" \
  "v_mfma_f32_16x16x32_bf16 v[104:107], v[152:155], v[248:251], v[104:107]\n" \
  "v_mfma_f32_16x16x32_bf16 v[108:111], v[168:171], v[248:251], v[108:111]\n" \
  "v_mfma_f32_16x16x32_bf16 v[104:107], v[156:159], v[252:255], v[104:107]\n" \
  "v_mfma_f32_16x16x32_bf16 v[108:111], v[172:175], v[252:255], v[108:111]\n" \
  "s_nop 3\n" \
  "v_cvt_pk_bf16_f32 v92, v104, v105\n" \
  "v_cvt_pk_bf16_f32 v93, v106, v107\n" \
  "s_nop 1\n" \
  "v_cvt_pk_bf16_f32 v94, v108, v109\n" \
  "v_cvt_pk_bf16_f32 v95, v110, v111\n" \
  "buffer_store_dwordx4 v[92:95], %[zo], s[40:43], s61 offen sc1\n" \
  "s_sub_u32 s61, s61, 18857984\n" \
  "s_add_u32 s62, s62, 1\n" \
  "s_waitcnt vmcnt(0)\n" \
  "v_mov_b32 v18, s62\n" \
  "buffer_store_dword v18, %[fo], s[40:43], s63 offen sc1\n" \
  "s_waitcnt vmcnt(0) lgkmcnt(0)\n"

#define ASM_RA \
  "s_nop 4\n" \
  "s_mov_b64 s[40:41], %[ws]\n" \
  "s_mov_b32 s42, 0x0ff00000\n" \
  "s_mov_b32 s43, 0x20000\n" \
  "s_lshr_b32 s55, %[w], 1\n" \
  "s_lshl_b32 s57, s55, 14\n" \
  "s_and_b32 s55, %[w], 1\n" \
  "s_mul_i32 s55, s55, 2560\n" \
  "s_add_u32 s57, s57, s55\n" \
  "s_lshl_b32 s55, %[cp], 15\n" \
  "s_add_u32 s57, s57, s55\n" \
  "s_mov_b32 s62, 0\n" \
  "s_mov_b32 s66, 131072\n" \
  "s_mov_b32 s67, 0\n" \
  "s_mov_b32 s68, 0\n" \
  "s_memtime s[50:51]\n" \
  "s_memrealtime s[88:89]\n" \
  "s_waitcnt lgkmcnt(0)\n" \
  "s_mov_b32 s70, s50\n" \
  "s_mov_b32 s71, s88\n" \
  "s_lshl_b32 s54, %[id], 8\n" \
  "s_mov_b64 s[44:45], %[wa]\n" \
  "s_add_u32 s44, s44, s54\n" \
  "s_addc_u32 s45, s45, 0\n" \
  "s_mov_b32 s46, 0x00500000\n" \
  "s_mov_b32 s47, 0x20000\n" \
  "s_mov_b64 s[48:49], %[wb]\n" \
  "s_add_u32 s48, s48, s54\n" \
  "s_addc_u32 s49, s49, 0\n" \
  "s_mov_b32 s50, 0x00500000\n" \
  "s_mov_b32 s51, 0x20000\n" \
  "s_sub_u32 s46, s46, s54\n" \
  "s_sub_u32 s50, s50, s54\n" \
  "s_mul_i32 s52, %[w], 28672\n" \
  "s_add_u32 s52, s52, 40960\n" \
  "s_mul_i32 s54, %[w], 1310720\n" \
  "s_add_u32 s55, %[w], 0\n" \
  "s_and_b32 s55, s55, 3\n" \
  "s_lshl_b32 s58, s55, 6\n" \
  "s_add_u32 s58, s58, s54\n" \
  "s_lshl_b32 s53, s55, 9\n" \
  "s_add_u32 s53, s53, 20480\n" \
  "v_add_u32 v184, s53, %[trb]\n" \
  "s_lshl_b32 s53, s55, 11\n" \
  "v_add_u32 v188, s53, %[l16]\n" \
  "s_mul_i32 s53, s55, 3072\n" \
  "s_add_u32 s53, s53, 8192\n" \
  "v_add_u32 v176, s53, %[l16]\n" \
  "s_add_u32 s55, %[w], 1\n" \
  "s_and_b32 s55, s55, 3\n" \
  "s_lshl_b32 s59, s55, 6\n" \
  "s_add_u32 s59, s59, s54\n" \
  "s_lshl_b32 s53, s55, 9\n" \
  "s_add_u32 s53, s53, 20480\n" \
  "v_add_u32 v185, s53, %[trb]\n" \
  "s_lshl_b32 s53, s55, 11\n" \
  "v_add_u32 v189, s53, %[l16]\n" \
  "s_mul_i32 s53, s55, 3072\n" \
  "s_add_u32 s53, s53, 8192\n" \
  "v_add_u32 v177, s53, %[l16]\n" \
  "s_add_u32 s55, %[w], 2\n" \
  "s_and_b32 s55, s55, 3\n" \
  "s_lshl_b32 s60, s55, 6\n" \
  "s_add_u32 s60, s60, s54\n" \
  "s_lshl_b32 s53, s55, 9\n" \
  "s_add_u32 s53, s53, 20480\n" \
  "v_add_u32 v186, s53, %[trb]\n" \
  "s_lshl_b32 s53, s55, 11\n" \
  "v_add_u32 v190, s53, %[l16]\n" \
  "s_mul_i32 s53, s55, 3072\n" \
  "s_add_u32 s53, s53, 9216\n" \
  "v_add_u32 v178, s53, %[l16]\n" \
  "s_add_u32 s55, %[w], 3\n" \
  "s_and_b32 s55, s55, 3\n" \
  "s_lshl_b32 s61, s55, 6\n" \
  "s_add_u32 s61, s61, s54\n" \
  "s_lshl_b32 s53, s55, 9\n" \
  "s_add_u32 s53, s53, 20480\n" \
  "v_add_u32 v187, s53, %[trb]\n" \
  "s_lshl_b32 s53, s55, 11\n" \
  "v_add_u32 v191, s53, %[l16]\n" \
  "s_mul_i32 s53, s55, 3072\n" \
  "s_add_u32 s53, s53, 10240\n" \
  "v_add_u32 v179, s53, %[l16]\n" \
  "s_lshl_b32 s53, %[w], 9\n" \
  "s_add_u32 s53, s53, 20480\n" \
  "v_add_u32 v182, s53, %[dw]\n" \
  "s_lshl_b32 s53, %[w], 11\n" \
  "s_add_u32 s53, s53, 22528\n" \
  "v_add_u32 v183, s53, %[dw]\n" \
  "s_lshl_b32 s53, %[w], 10\n" \
  "s_add_u32 s53, s53, 32768\n" \
  "v_add_u32 v174, s53, %[l16]\n" \
  "v_lshrrev_b32 v175, 1, %[l16]\n" \
  "v_add_u32 v175, 36864, v175\n" \
  "s_lshl_b32 s53, %[w], 9\n" \
  "v_add_u32 v173, s53, v175\n" \
  "v_mov_b32 v16, 0\n" \
  "v_mov_b32 v17, 0\n" \
  "v_mov_b32 v18, 0\n" \
  "v_mov_b32 v19, 0\n" \
  "ds_write_b128 %[l16], v[16:19] offset:30720\n" \
  "ds_write_b128 %[l16], v[16:19] offset:31744\n" \
  "s_add_u32 s53, s58, 0\n" \
  "s_add_u32 m0, s52, 0\n" \
  "s_nop 0\n" \
  "buffer_load_dwordx4 %[wo], s[44:47], s53 offen lds\n" \
  "s_add_u32 s53, s58, 131072\n" \
  "s_add_u32 m0, s52, 1024\n" \
  "s_nop 0\n" \
  "buffer_load_dwordx4 %[wo], s[44:47], s53 offen lds\n" \
  "s_add_u32 s53, s59, 0\n" \
  "s_add_u32 m0, s52, 2048\n" \
  "s_nop 0\n" \
  "buffer_load_dwordx4 %[wo], s[48:51], s53 offen lds\n" \
  "s_add_u32 s53, s59, 131072\n" \
  "s_add_u32 m0, s52, 3072\n" \
  "s_nop 0\n" \
  "buffer_load_dwordx4 %[wo], s[48:51], s53 offen lds\n" \
  "s_add_u32 s53, s59, 0\n" \
  "s_add_u32 m0, s52, 4096\n" \
  "s_nop 0\n" \
  "buffer_load_dwordx4 %[wo], s[44:47], s53 offen lds\n" \
  "s_add_u32 s53, s59, 131072\n" \
  "s_add_u32 m0, s52, 5120\n" \
  "s_nop 0\n" \
  "buffer_load_dwordx4 %[wo], s[44:47], s53 offen lds\n" \
  "s_add_u32 s53, s60, 0\n" \
  "s_add_u32 m0, s52, 6144\n" \
  "s_nop 0\n" \
  "buffer_load_dwordx4 %[wo], s[48:51], s53 offen lds\n" \
  "s_add_u32 s53, s60, 131072\n" \
  "s_add_u32 m0, s52, 7168\n" \
  "s_nop 0\n" \
  "buffer_load_dwordx4 %[wo], s[48:51], s53 offen lds\n" \
  "s_add_u32 s53, s60, 0\n" \
  "s_add_u32 m0, s52, 8192\n" \
  "s_nop 0\n" \
  "buffer_load_dwordx4 %[wo], s[44:47], s53 offen lds\n" \
  "s_add_u32 s53, s60, 131072\n" \
  "s_add_u32 m0, s52, 9216\n" \
  "s_nop 0\n" \
  "buffer_load_dwordx4 %[wo], s[44:47], s53 offen lds\n" \
  "s_add_u32 s53, s61, 0\n" \
  "s_add_u32 m0, s52, 10240\n" \
  "s_nop 0\n" \
  "buffer_load_dwordx4 %[wo], s[48:51], s53 offen lds\n" \
  "s_add_u32 s53, s61, 131072\n" \
  "s_add_u32 m0, s52, 11264\n" \
  "s_nop 0\n" \
  "buffer_load_dwordx4 %[wo], s[48:51], s53 offen lds\n" \
  "s_add_u32 s53, s61, 0\n" \
  "s_add_u32 m0, s52, 12288\n" \
  "s_nop 0\n" \
  "buffer_load_dwordx4 %[wo], s[44:47], s53 offen lds\n" \
  "s_add_u32 s53, s61, 131072\n" \
  "s_add_u32 m0, s52, 13312\n" \
  "s_nop 0\n" \
  "buffer_load_dwordx4 %[wo], s[44:47], s53 offen lds\n" \
  "s_add_u32 s53, s58, 0\n" \
  "s_add_u32 m0, s52, 14336\n" \
  "s_nop 0\n" \
  "buffer_load_dwordx4 %[wo], s[48:51], s53 offen lds\n" \
  "s_add_u32 s53, s58, 131072\n" \
  "s_add_u32 m0, s52, 15360\n" \
  "s_nop 0\n" \
  "buffer_load_dwordx4 %[wo], s[48:51], s53 offen lds\n" \
  "s_add_u32 s53, s58, 262144\n" \
  "s_add_u32 m0, s52, 16384\n" \
  "s_nop 0\n" \
  "buffer_load_dwordx4 %[wo], s[44:47], s53 offen lds\n" \
  "s_add_u32 s53, s58, 393216\n" \
  "s_add_u32 m0, s52, 17408\n" \
  "s_nop 0\n" \
  "buffer_load_dwordx4 %[wo], s[44:47], s53 offen lds\n" \
  "s_add_u32 s53, s59, 262144\n" \
  "s_add_u32 m0, s52, 18432\n" \
  "s_nop 0\n" \
  "buffer_load_dwordx4 %[wo], s[48:51], s53 offen lds\n" \
  "s_add_u32 s53, s59, 393216\n" \
  "s_add_u32 m0, s52, 19456\n" \
  "s_nop 0\n" \
  "buffer_load_dwordx4 %[wo], s[48:51], s53 offen lds\n" \
  "s_add_u32 s53, s59, 262144\n" \
  "s_add_u32 m0, s52, 20480\n" \
  "s_nop 0\n" \
  "buffer_load_dwordx4 %[wo], s[44:47], s53 offen lds\n" \
  "s_add_u32 s53, s59, 393216\n" \
  "s_add_u32 m0, s52, 21504\n" \
  "s_nop 0\n" \
  "buffer_load_dwordx4 %[wo], s[44:47], s53 offen lds\n" \
  "s_add_u32 s53, s60, 262144\n" \
  "s_add_u32 m0, s52, 22528\n" \
  "s_nop 0\n" \
  "buffer_load_dwordx4 %[wo], s[48:51], s53 offen lds\n" \
  "s_add_u32 s53, s60, 393216\n" \
  "s_add_u32 m0, s52, 23552\n" \
  "s_nop 0\n" \
  "buffer_load_dwordx4 %[wo], s[48:51], s53 offen lds\n" \
  "s_waitcnt vmcnt(20)\n" \
  "ds_read_b64_tr_b16 a[32:33], %[tr] offset:0\n" \
  "ds_read_b64_tr_b16 a[34:35], %[tr] offset:128\n" \
  "ds_read_b64_tr_b16 a[36:37], %[tr] offset:1024\n" \
  "ds_read_b64_tr_b16 a[38:39], %[tr] offset:1152\n" \
  "ds_read_b64_tr_b16 a[0:1], %[tr] offset:2048\n" \
  "ds_read_b64_tr_b16 a[2:3], %[tr] offset:2176\n" \
  "ds_read_b64_tr_b16 a[4:5], %[tr] offset:3072\n" \
  "ds_read_b64_tr_b16 a[6:7], %[tr] offset:3200\n" \
  "s_add_u32 s53, s60, 262144\n" \
  "s_add_u32 m0, s52, 24576\n" \
  "s_nop 0\n" \
  "buffer_load_dwordx4 %[wo], s[44:47], s53 offen lds\n" \
  "s_add_u32 s53, s60, 393216\n" \
  "s_add_u32 m0, s52, 25600\n" \
  "s_nop 0\n" \
  "buffer_load_dwordx4 %[wo], s[44:47], s53 offen lds\n" \
  "s_add_u32 s53, s61, 262144\n" \
  "s_add_u32 m0, s52, 26624\n" \
  "s_nop 0\n" \
  "buffer_load_dwordx4 %[wo], s[48:51], s53 offen lds\n" \
  "s_add_u32 s53, s61, 393216\n" \
  "s_add_u32 m0, s52, 27648\n" \
  "s_nop 0\n" \
  "buffer_load_dwordx4 %[wo], s[48:51], s53 offen lds\n" \
  "s_waitcnt vmcnt(20)\n" \
  "ds_read_b64_tr_b16 a[40:41], %[tr] offset:4096\n" \
  "ds_read_b64_tr_b16 a[42:43], %[tr] offset:4224\n" \
  "ds_read_b64_tr_b16 a[44:45], %[tr] offset:5120\n" \
  "ds_read_b64_tr_b16 a[46:47], %[tr] offset:5248\n" \
  "ds_read_b64_tr_b16 a[8:9], %[tr] offset:6144\n" \
  "s_waitcnt lgkmcnt(14)\n" \
  "ds_read_b64_tr_b16 a[10:11], %[tr] offset:6272\n" \
  "s_waitcnt lgkmcnt(14)\n" \
  "ds_read_b64_tr_b16 a[12:13], %[tr] offset:7168\n" \
  "s_waitcnt lgkmcnt(14)\n" \
  "ds_read_b64_tr_b16 a[14:15], %[tr] offset:7296\n" \
  "s_waitcnt lgkmcnt(8)\n" \
  "s_add_u32 s53, s61, 262144\n" \
  "s_add_u32 m0, s52, 0\n" \
  "s_nop 0\n" \
  "buffer_load_dwordx4 %[wo], s[44:47], s53 offen lds\n" \
  "s_add_u32 s53, s61, 393216\n" \
  "s_add_u32 m0, s52, 1024\n" \
  "s_nop 0\n" \
  "buffer_load_dwordx4 %[wo], s[44:47], s53 offen lds\n" \
  "s_add_u32 s53, s58, 262144\n" \
  "s_add_u32 m0, s52, 2048\n" \
  "s_nop 0\n" \
  "buffer_load_dwordx4 %[wo], s[48:51], s53 offen lds\n" \
  "s_add_u32 s53, s58, 393216\n" \
  "s_add_u32 m0, s52, 3072\n" \
  "s_nop 0\n" \
  "buffer_load_dwordx4 %[wo], s[48:51], s53 offen lds\n" \
  "s_waitcnt vmcnt(20)\n" \
  "ds_read_b64_tr_b16 a[48:49], %[tr] offset:8192\n" \
  "ds_read_b64_tr_b16 a[50:51], %[tr] offset:8320\n" \
  "ds_read_b64_tr_b16 a[52:53], %[tr] offset:9216\n" \
  "ds_read_b64_tr_b16 a[54:55], %[tr] offset:9344\n" \
  "ds_read_b64_tr_b16 a[16:17], %[tr] offset:10240\n" \
  "ds_read_b64_tr_b16 a[18:19], %[tr] offset:10368\n" \
  "ds_read_b64_tr_b16 a[20:21], %[tr] offset:11264\n" \
  "s_waitcnt lgkmcnt(14)\n" \
  "ds_read_b64_tr_b16 a[22:23], %[tr] offset:11392\n" \
  "s_waitcnt lgkmcnt(8)\n" \
  "s_add_u32 s53, s58, 524288\n" \
  "s_add_u32 m0, s52, 4096\n" \
  "s_nop 0\n" \
  "buffer_load_dwordx4 %[wo], s[44:47], s53 offen lds\n" \
  "s_add_u32 s53, s58, 655360\n" \
  "s_add_u32 m0, s52, 5120\n" \
  "s_nop 0\n" \
  "buffer_load_dwordx4 %[wo], s[44:47], s53 offen lds\n" \
  "s_add_u32 s53, s59, 524288\n" \
  "s_add_u32 m0, s52, 6144\n" \
  "s_nop 0\n" \
  "buffer_load_dwordx4 %[wo], s[48:51], s53 offen lds\n" \
  "s_add_u32 s53, s59, 655360\n" \
  "s_add_u32 m0, s52, 7168\n" \
  "s_nop 0\n" \
  "buffer_load_dwordx4 %[wo], s[48:51], s53 offen lds\n" \
  "s_waitcnt vmcnt(20)\n" \
  "ds_read_b64_tr_b16 a[56:57], %[tr] offset:12288\n" \
  "ds_read_b64_tr_b16 a[58:59], %[tr] offset:12416\n" \
  "ds_read_b64_tr_b16 a[60:61], %[tr] offset:13312\n" \
  "ds_read_b64_tr_b16 a[62:63], %[tr] offset:13440\n" \
  "ds_read_b64_tr_b16 a[24:25], %[tr] offset:14336\n" \
  "ds_read_b64_tr_b16 a[26:27], %[tr] offset:14464\n" \
  "ds_read_b64_tr_b16 a[28:29], %[tr] offset:15360\n" \
  "s_waitcnt lgkmcnt(14)\n" \
  "ds_read_b64_tr_b16 a[30:31], %[tr] offset:15488\n" \
  "s_waitcnt lgkmcnt(8)\n" \
  "s_add_u32 s53, s59, 524288\n" \
  "s_add_u32 m0, s52, 8192\n" \
  "s_nop 0\n" \
  "buffer_load_dwordx4 %[wo], s[44:47], s53 offen lds\n" \
  "s_add_u32 s53, s59, 655360\n" \
  "s_add_u32 m0, s52, 9216\n" \
  "s_nop 0\n" \
  "buffer_load_dwordx4 %[wo], s[44:47], s53 offen lds\n" \
  "s_add_u32 s53, s60, 524288\n" \
  "s_add_u32 m0, s52, 10240\n" \
  "s_nop 0\n" \
  "buffer_load_dwordx4 %[wo], s[48:51], s53 offen lds\n" \
  "s_add_u32 s53, s60, 655360\n" \
  "s_add_u32 m0, s52, 11264\n" \
  "s_nop 0\n" \
  "buffer_load_dwordx4 %[wo], s[48:51], s53 offen lds\n" \
  "s_waitcnt vmcnt(20)\n" \
  "ds_read_b64_tr_b16 a[96:97], %[tr] offset:16384\n" \
  "ds_read_b64_tr_b16 a[98:99], %[tr] offset:16512\n" \
  "ds_read_b64_tr_b16 a[100:101], %[tr] offset:17408\n" \
  "ds_read_b64_tr_b16 a[102:103], %[tr] offset:17536\n" \
  "ds_read_b64_tr_b16 a[64:65], %[tr] offset:18432\n" \
  "ds_read_b64_tr_b16 a[66:67], %[tr] offset:18560\n" \
  "ds_read_b64_tr_b16 a[68:69], %[tr] offset:19456\n" \
  "s_waitcnt lgkmcnt(14)\n" \
  "ds_read_b64_tr_b16 a[70:71], %[tr] offset:19584\n" \
  "s_waitcnt lgkmcnt(8)\n" \
  "s_add_u32 s53, s60, 524288\n" \
  "s_add_u32 m0, s52, 12288\n" \
  "s_nop 0\n" \
  "buffer_load_dwordx4 %[wo], s[44:47], s53 offen lds\n" \
  "s_add_u32 s53, s60, 655360\n" \
  "s_add_u32 m0, s52, 13312\n" \
  "s_nop 0\n" \
  "buffer_load_dwordx4 %[wo], s[44:47], s53 offen lds\n" \
  "s_add_u32 s53, s61, 524288\n" \
  "s_add_u32 m0, s52, 14336\n" \
  "s_nop 0\n" \
  "buffer_load_dwordx4 %[wo], s[48:51], s53 offen lds\n" \
  "s_add_u32 s53, s61, 655360\n" \
  "s_add_u32 m0, s52, 15360\n" \
  "s_nop 0\n" \
  "buffer_load_dwordx4 %[wo], s[48:51], s53 offen lds\n" \
  "s_waitcnt vmcnt(20)\n" \
  "ds_read_b64_tr_b16 a[104:105], %[tr] offset:20480\n" \
  "ds_read_b64_tr_b16 a[106:107], %[tr] offset:20608\n" \
  "ds_read_b64_tr_b16 a[108:109], %[tr] offset:21504\n" \
  "ds_read_b64_tr_b16 a[110:111], %[tr] offset:21632\n" \
  "ds_read_b64_tr_b16 a[72:73], %[tr] offset:22528\n" \
  "ds_read_b64_tr_b16 a[74:75], %[tr] offset:22656\n" \
  "ds_read_b64_tr_b16 a[76:77], %[tr] offset:23552\n" \
  "s_waitcnt lgkmcnt(14)\n" \
  "ds_read_b64_tr_b16 a[78:79], %[tr] offset:23680\n" \
  "s_waitcnt lgkmcnt(8)\n" \
  "s_add_u32 s53, s61, 524288\n" \
  "s_add_u32 m0, s52, 16384\n" \
  "s_nop 0\n" \
  "buffer_load_dwordx4 %[wo], s[44:47], s53 offen lds\n" \
  "s_add_u32 s53, s61, 655360\n" \
  "s_add_u32 m0, s52, 17408\n" \
  "s_nop 0\n" \
  "buffer_load_dwordx4 %[wo], s[44:47], s53 offen lds\n" \
  "s_add_u32 s53, s58, 524288\n" \
  "s_add_u32 m0, s52, 18432\n" \
  "s_nop 0\n" \
  "buffer_load_dwordx4 %[wo], s[48:51], s53 offen lds\n" \
  "s_add_u32 s53, s58, 655360\n" \
  "s_add_u32 m0, s52, 19456\n" \
  "s_nop 0\n" \
  "buffer_load_dwordx4 %[wo], s[48:51], s53 offen lds\n" \
  "s_waitcnt vmcnt(20)\n" \
  "ds_read_b64_tr_b16 a[112:113], %[tr] offset:24576\n" \
  "ds_read_b64_tr_b16 a[114:115], %[tr] offset:24704\n" \
  "ds_read_b64_tr_b16 a[116:117], %[tr] offset:25600\n" \
  "ds_read_b64_tr_b16 a[118:119], %[tr] offset:25728\n" \
  "ds_read_b64_tr_b16 a[80:81], %[tr] offset:26624\n" \
  "ds_read_b64_tr_b16 a[82:83], %[tr] offset:26752\n" \
  "ds_read_b64_tr_b16 a[84:85], %[tr] offset:27648\n" \
  "s_waitcnt lgkmcnt(14)\n" \
  "ds_read_b64_tr_b16 a[86:87], %[tr] offset:27776\n" \
  "s_waitcnt lgkmcnt(8)\n" \
  "s_add_u32 s53, s58, 786432\n" \
  "s_add_u32 m0, s52, 20480\n" \
  "s_nop 0\n" \
  "buffer_load_dwordx4 %[wo], s[44:47], s53 offen lds\n" \
  "s_add_u32 s53, s58, 917504\n" \
  "s_add_u32 m0, s52, 21504\n" \
  "s_nop 0\n" \
  "buffer_load_dwordx4 %[wo], s[44:47], s53 offen lds\n" \
  "s_add_u32 s53, s59, 786432\n" \
  "s_add_u32 m0, s52, 22528\n" \
  "s_nop 0\n" \
  "buffer_load_dwordx4 %[wo], s[48:51], s53 offen lds\n" \
  "s_add_u32 s53, s59, 917504\n" \
  "s_add_u32 m0, s52, 23552\n" \
  "s_nop 0\n" \
  "buffer_load_dwordx4 %[wo], s[48:51], s53 offen lds\n" \
  "s_waitcnt vmcnt(20)\n" \
  "ds_read_b64_tr_b16 a[120:121], %[tr] offset:0\n" \
  "ds_read_b64_tr_b16 a[122:123], %[tr] offset:128\n" \
  "ds_read_b64_tr_b16 a[124:125], %[tr] offset:1024\n" \
  "ds_read_b64_tr_b16 a[126:127], %[tr] offset:1152\n" \
  "ds_read_b64_tr_b16 a[88:89], %[tr] offset:2048\n" \
  "ds_read_b64_tr_b16 a[90:91], %[tr] offset:2176\n" \
  "ds_read_b64_tr_b16 a[92:93], %[tr] offset:3072\n" \
  "s_waitcnt lgkmcnt(14)\n" \
  "ds_read_b64_tr_b16 a[94:95], %[tr] offset:3200\n" \
  "s_waitcnt lgkmcnt(8)\n" \
  "s_add_u32 s53, s59, 786432\n" \
  "s_add_u32 m0, s52, 24576\n" \
  "s_nop 0\n" \
  "buffer_load_dwordx4 %[wo], s[44:47], s53 offen lds\n" \
  "s_add_u32 s53, s59, 917504\n" \
  "s_add_u32 m0, s52, 25600\n" \
  "s_nop 0\n" \
  "buffer_load_dwordx4 %[wo], s[44:47], s53 offen lds\n" \
  "s_add_u32 s53, s60, 786432\n" \
  "s_add_u32 m0, s52, 26624\n" \
  "s_nop 0\n" \
  "buffer_load_dwordx4 %[wo], s[48:51], s53 offen lds\n" \
  "s_add_u32 s53, s60, 917504\n" \
  "s_add_u32 m0, s52, 27648\n" \
  "s_nop 0\n" \
  "buffer_load_dwordx4 %[wo], s[48:51], s53 offen lds\n" \
  "s_waitcnt vmcnt(20)\n" \
  "ds_read_b64_tr_b16 a[160:161], %[tr] offset:4096\n" \
  "ds_read_b64_tr_b16 a[162:163], %[tr] offset:4224\n" \
  "ds_read_b64_tr_b16 a[164:165], %[tr] offset:5120\n" \
  "ds_read_b64_tr_b16 a[166:167], %[tr] offset:5248\n" \
  "ds_read_b64_tr_b16 a[128:129], %[tr] offset:6144\n" \
  "ds_read_b64_tr_b16 a[130:131], %[tr] offset:6272\n" \
  "ds_read_b64_tr_b16 a[132:133], %[tr] offset:7168\n" \
  "s_waitcnt lgkmcnt(14)\n" \
  "ds_read_b64_tr_b16 a[134:135], %[tr] offset:7296\n" \
  "s_waitcnt lgkmcnt(8)\n" \
  "s_add_u32 s53, s60, 786432\n" \
  "s_add_u32 m0, s52, 0\n" \
  "s_nop 0\n" \
  "buffer_load_dwordx4 %[wo], s[44:47], s53 offen lds\n" \
  "s_add_u32 s53, s60, 917504\n" \
  "s_add_u32 m0, s52, 1024\n" \
  "s_nop 0\n" \
  "buffer_load_dwordx4 %[wo], s[44:47], s53 offen lds\n" \
  "s_add_u32 s53, s61, 786432\n" \
  "s_add_u32 m0, s52, 2048\n" \
  "s_nop 0\n" \
  "buffer_load_dwordx4 %[wo], s[48:51], s53 offen lds\n" \
  "s_add_u32 s53, s61, 917504\n" \
  "s_add_u32 m0, s52, 3072\n" \
  "s_nop 0\n" \
  "buffer_load_dwordx4 %[wo], s[48:51], s53 offen lds\n" \
  "s_waitcnt vmcnt(20)\n" \
  "ds_read_b64_tr_b16 a[168:169], %[tr] offset:8192\n" \
  "ds_read_b64_tr_b16 a[170:171], %[tr] offset:8320\n" \
  "ds_read_b64_tr_b16 a[172:173], %[tr] offset:9216\n" \
  "ds_read_b64_tr_b16 a[174:175], %[tr] offset:9344\n" \
  "ds_read_b64_tr_b16 a[136:137], %[tr] offset:10240\n" \
  "ds_read_b64_tr_b16 a[138:139], %[tr] offset:10368\n" \
  "ds_read_b64_tr_b16 a[140:141], %[tr] offset:11264\n" \
  "s_waitcnt lgkmcnt(14)\n" \
  "ds_read_b64_tr_b16 a[142:143], %[tr] offset:11392\n" \
  "s_waitcnt lgkmcnt(8)\n" \
  "s_add_u32 s53, s61, 786432\n" \
  "s_add_u32 m0, s52, 4096\n" \
  "s_nop 0\n" \
  "buffer_load_dwordx4 %[wo], s[44:47], s53 offen lds\n" \
  "s_add_u32 s53, s61, 917504\n" \
  "s_add_u32 m0, s52, 5120\n" \
  "s_nop 0\n" \
  "buffer_load_dwordx4 %[wo], s[44:47], s53 offen lds\n" \
  "s_add_u32 s53, s58, 786432\n" \
  "s_add_u32 m0, s52, 6144\n" \
  "s_nop 0\n" \
  "buffer_load_dwordx4 %[wo], s[48:51], s53 offen lds\n" \
  "s_add_u32 s53, s58, 917504\n" \
  "s_add_u32 m0, s52, 7168\n" \
  "s_nop 0\n" \
  "buffer_load_dwordx4 %[wo], s[48:51], s53 offen lds\n" \
  "s_waitcnt vmcnt(20)\n" \
  "ds_read_b64_tr_b16 a[176:177], %[tr] offset:12288\n" \
  "ds_read_b64_tr_b16 a[178:179], %[tr] offset:12416\n" \
  "ds_read_b64_tr_b16 a[180:181], %[tr] offset:13312\n" \
  "ds_read_b64_tr_b16 a[182:183], %[tr] offset:13440\n" \
  "ds_read_b64_tr_b16 a[144:145], %[tr] offset:14336\n" \
  "ds_read_b64_tr_b16 a[146:147], %[tr] offset:14464\n" \
  "ds_read_b64_tr_b16 a[148:149], %[tr] offset:15360\n" \
  "s_waitcnt lgkmcnt(14)\n" \
  "ds_read_b64_tr_b16 a[150:151], %[tr] offset:15488\n" \
  "s_waitcnt lgkmcnt(8)\n" \
  "s_add_u32 s53, s58, 1048576\n" \
  "s_add_u32 m0, s52, 8192\n" \
  "s_nop 0\n" \
  "buffer_load_dwordx4 %[wo], s[44:47], s53 offen lds\n" \
  "s_add_u32 s53, s58, 1179648\n" \
  "s_add_u32 m0, s52, 9216\n" \
  "s_nop 0\n" \
  "buffer_load_dwordx4 %[wo], s[44:47], s53 offen lds\n" \
  "s_add_u32 s53, s59, 1048576\n" \
  "s_add_u32 m0, s52, 10240\n" \
  "s_nop 0\n" \
  "buffer_load_dwordx4 %[wo], s[48:51], s53 offen lds\n" \
  "s_add_u32 s53, s59, 1179648\n" \
  "s_add_u32 m0, s52, 11264\n" \
  "s_nop 0\n" \
  "buffer_load_dwordx4 %[wo], s[48:51], s53 offen lds\n" \
  "s_waitcnt vmcnt(20)\n" \
  "ds_read_b64_tr_b16 a[184:185], %[tr] offset:16384\n" \
  "ds_read_b64_tr_b16 a[186:187], %[tr] offset:16512\n" \
  "ds_read_b64_tr_b16 a[188:189], %[tr] offset:17408\n" \
  "ds_read_b64_tr_b16 a[190:191], %[tr] offset:17536\n" \
  "ds_read_b64_tr_b16 a[152:153], %[tr] offset:18432\n" \
  "ds_read_b64_tr_b16 a[154:155], %[tr] offset:18560\n" \
  "ds_read_b64_tr_b16 a[156:157], %[tr] offset:19456\n" \
  "s_waitcnt lgkmcnt(14)\n" \
  "ds_read_b64_tr_b16 a[158:159], %[tr] offset:19584\n" \
  "s_waitcnt lgkmcnt(8)\n" \
  "s_add_u32 s53, s59, 1048576\n" \
  "s_add_u32 m0, s52, 12288\n" \
  "s_nop 0\n" \
  "buffer_load_dwordx4 %[wo], s[44:47], s53 offen lds\n" \
  "s_add_u32 s53, s59, 1179648\n" \
  "s_add_u32 m0, s52, 13312\n" \
  "s_nop 0\n" \
  "buffer_load_dwordx4 %[wo], s[44:47], s53 offen lds\n" \
  "s_add_u32 s53, s60, 1048576\n" \
  "s_add_u32 m0, s52, 14336\n" \
  "s_nop 0\n" \
  "buffer_load_dwordx4 %[wo], s[48:51], s53 offen lds\n" \
  "s_add_u32 s53, s60, 1179648\n" \
  "s_add_u32 m0, s52, 15360\n" \
  "s_nop 0\n" \
  "buffer_load_dwordx4 %[wo], s[48:51], s53 offen lds\n" \
  "s_waitcnt vmcnt(20)\n" \
  "ds_read_b64_tr_b16 a[224:225], %[tr] offset:20480\n" \
  "ds_read_b64_tr_b16 a[226:227], %[tr] offset:20608\n" \
  "ds_read_b64_tr_b16 a[228:229], %[tr] offset:21504\n" \
  "ds_read_b64_tr_b16 a[230:231], %[tr] offset:21632\n" \
  "ds_read_b64_tr_b16 a[192:193], %[tr] offset:22528\n" \
  "ds_read_b64_tr_b16 a[194:195], %[tr] offset:22656\n" \
  "ds_read_b64_tr_b16 a[196:197], %[tr] offset:23552\n" \
  "s_waitcnt lgkmcnt(14)\n" \
  "ds_read_b64_tr_b16 a[198:199], %[tr] offset:23680\n" \
  "s_waitcnt lgkmcnt(8)\n" \
  "s_add_u32 s53, s60, 1048576\n" \
  "s_add_u32 m0, s52, 16384\n" \
  "s_nop 0\n" \
  "buffer_load_dwordx4 %[wo], s[44:47], s53 offen lds\n" \
  "s_add_u32 s53, s60, 1179648\n" \
  "s_add_u32 m0, s52, 17408\n" \
  "s_nop 0\n" \
  "buffer_load_dwordx4 %[wo], s[44:47], s53 offen lds\n" \
  "s_add_u32 s53, s61, 1048576\n" \
  "s_add_u32 m0, s52, 18432\n" \
  "s_nop 0\n" \
  "buffer_load_dwordx4 %[wo], s[48:51], s53 offen lds\n" \
  "s_add_u32 s53, s61, 1179648\n" \
  "s_add_u32 m0, s52, 19456\n" \
  "s_nop 0\n" \
  "buffer_load_dwordx4 %[wo], s[48:51], s53 offen lds\n" \
  "s_waitcnt vmcnt(20)\n" \
  "ds_read_b64_tr_b16 a[232:233], %[tr] offset:24576\n" \
  "ds_read_b64_tr_b16 a[234:235], %[tr] offset:24704\n" \
  "ds_read_b64_tr_b16 a[236:237], %[tr] offset:25600\n" \
  "ds_read_b64_tr_b16 a[238:239], %[tr] offset:25728\n" \
  "ds_read_b64_tr_b16 a[200:201], %[tr] offset:26624\n" \
  "ds_read_b64_tr_b16 a[202:203], %[tr] offset:26752\n" \
  "ds_read_b64_tr_b16 a[204:205], %[tr] offset:27648\n" \
  "s_waitcnt lgkmcnt(14)\n" \
  "ds_read_b64_tr_b16 a[206:207], %[tr] offset:27776\n" \
  "s_waitcnt lgkmcnt(8)\n" \
  "s_add_u32 s53, s61, 1048576\n" \
  "s_add_u32 m0, s52, 20480\n" \
  "s_nop 0\n" \
  "buffer_load_dwordx4 %[wo], s[44:47], s53 offen lds\n" \
  "s_add_u32 s53, s61, 1179648\n" \
  "s_add_u32 m0, s52, 21504\n" \
  "s_nop 0\n" \
  "buffer_load_dwordx4 %[wo], s[44:47], s53 offen lds\n" \
  "s_add_u32 s53, s58, 1048576\n" \
  "s_add_u32 m0, s52, 22528\n" \
  "s_nop 0\n" \
  "buffer_load_dwordx4 %[wo], s[48:51], s53 offen lds\n" \
  "s_add_u32 s53, s58, 1179648\n" \
  "s_add_u32 m0, s52, 23552\n" \
  "s_nop 0\n" \
  "buffer_load_dwordx4 %[wo], s[48:51], s53 offen lds\n" \
  "s_waitcnt vmcnt(20)\n" \
  "ds_read_b64_tr_b16 a[240:241], %[tr] offset:0\n" \
  "ds_read_b64_tr_b16 a[242:243], %[tr] offset:128\n" \
  "ds_read_b64_tr_b16 a[244:245], %[tr] offset:1024\n" \
  "ds_read_b64_tr_b16 a[246:247], %[tr] offset:1152\n" \
  "ds_read_b64_tr_b16 a[208:209], %[tr] offset:2048\n" \
  "ds_read_b64_tr_b16 a[210:211], %[tr] offset:2176\n" \
  "ds_read_b64_tr_b16 a[212:213], %[tr] offset:3072\n" \
  "s_waitcnt lgkmcnt(14)\n" \
  "ds_read_b64_tr_b16 a[214:215], %[tr] offset:3200\n" \
  "s_waitcnt vmcnt(16)\n" \
  "s_waitcnt lgkmcnt(14)\n" \
  "ds_read_b64_tr_b16 a[248:249], %[tr] offset:4096\n" \
  "s_waitcnt lgkmcnt(14)\n" \
  "ds_read_b64_tr_b16 a[250:251], %[tr] offset:4224\n" \
  "s_waitcnt lgkmcnt(14)\n" \
  "ds_read_b64_tr_b16 a[252:253], %[tr] offset:5120\n" \
  "s_waitcnt lgkmcnt(14)\n" \
  "ds_read_b64_tr_b16 a[254:255], %[tr] offset:5248\n" \
  "s_waitcnt lgkmcnt(14)\n" \
  "ds_read_b64_tr_b16 a[216:217], %[tr] offset:6144\n" \
  "s_waitcnt lgkmcnt(14)\n" \
  "ds_read_b64_tr_b16 a[218:219], %[tr] offset:6272\n" \
  "s_waitcnt lgkmcnt(14)\n" \
  "ds_read_b64_tr_b16 a[220:221], %[tr] offset:7168\n" \
  "s_waitcnt lgkmcnt(14)\n" \
  "ds_read_b64_tr_b16 a[222:223], %[tr] offset:7296\n" \
  "s_waitcnt vmcnt(12)\n" \
  "s_waitcnt lgkmcnt(14)\n" \
  "ds_read_b64_tr_b16 v[224:225], %[tr] offset:8192\n" \
  "s_waitcnt lgkmcnt(14)\n" \
  "ds_read_b64_tr_b16 v[226:227], %[tr] offset:8320\n" \
  "s_waitcnt lgkmcnt(14)\n" \
  "ds_read_b64_tr_b16 v[228:229], %[tr] offset:9216\n" \
  "s_waitcnt lgkmcnt(14)\n" \
  "ds_read_b64_tr_b16 v[230:231], %[tr] offset:9344\n" \
  "s_waitcnt lgkmcnt(14)\n" \
  "ds_read_b64_tr_b16 v[192:193], %[tr] offset:10240\n" \
  "s_waitcnt lgkmcnt(14)\n" \
  "ds_read_b64_tr_b16 v[194:195], %[tr] offset:10368\n" \
  "s_waitcnt lgkmcnt(14)\n" \
  "ds_read_b64_tr_b16 v[196:197], %[tr] offset:11264\n" \
  "s_waitcnt lgkmcnt(14)\n" \
  "ds_read_b64_tr_b16 v[198:199], %[tr] offset:11392\n" \
  "s_waitcnt vmcnt(8)\n" \
  "s_waitcnt lgkmcnt(14)\n" \
  "ds_read_b64_tr_b16 v[232:233], %[tr] offset:12288\n" \
  "s_waitcnt lgkmcnt(14)\n" \
  "ds_read_b64_tr_b16 v[234:235], %[tr] offset:12416\n" \
  "s_waitcnt lgkmcnt(14)\n" \
  "ds_read_b64_tr_b16 v[236:237], %[tr] offset:13312\n" \
  "s_waitcnt lgkmcnt(14)\n" \
  "ds_read_b64_tr_b16 v[238:239], %[tr] offset:13440\n" \
  "s_waitcnt lgkmcnt(14)\n" \
  "ds_read_b64_tr_b16 v[200:201], %[tr] offset:14336\n" \
  "s_waitcnt lgkmcnt(14)\n" \
  "ds_read_b64_tr_b16 v[202:203], %[tr] offset:14464\n" \
  "s_waitcnt lgkmcnt(14)\n" \
  "ds_read_b64_tr_b16 v[204:205], %[tr] offset:15360\n" \
  "s_waitcnt lgkmcnt(14)\n" \
  "ds_read_b64_tr_b16 v[206:207], %[tr] offset:15488\n" \
  "s_waitcnt vmcnt(4)\n" \
  "s_waitcnt lgkmcnt(14)\n" \
  "ds_read_b64_tr_b16 v[240:241], %[tr] offset:16384\n" \
  "s_waitcnt lgkmcnt(14)\n" \
  "ds_read_b64_tr_b16 v[242:243], %[tr] offset:16512\n" \
  "s_waitcnt lgkmcnt(14)\n" \
  "ds_read_b64_tr_b16 v[244:245], %[tr] offset:17408\n" \
  "s_waitcnt lgkmcnt(14)\n" \
  "ds_read_b64_tr_b16 v[246:247], %[tr] offset:17536\n" \
  "s_waitcnt lgkmcnt(14)\n" \
  "ds_read_b64_tr_b16 v[208:209], %[tr] offset:18432\n" \
  "s_waitcnt lgkmcnt(14)\n" \
  "ds_read_b64_tr_b16 v[210:211], %[tr] offset:18560\n" \
  "s_waitcnt lgkmcnt(14)\n" \
  "ds_read_b64_tr_b16 v[212:213], %[tr] offset:19456\n" \
  "s_waitcnt lgkmcnt(14)\n" \
  "ds_read_b64_tr_b16 v[214:215], %[tr] offset:19584\n" \
  "s_waitcnt vmcnt(0)\n" \
  "s_waitcnt lgkmcnt(14)\n" \
  "ds_read_b64_tr_b16 v[248:249], %[tr] offset:20480\n" \
  "s_waitcnt lgkmcnt(14)\n" \
  "ds_read_b64_tr_b16 v[250:251], %[tr] offset:20608\n" \
  "s_waitcnt lgkmcnt(14)\n" \
  "ds_read_b64_tr_b16 v[252:253], %[tr] offset:21504\n" \
  "s_waitcnt lgkmcnt(14)\n" \
  "ds_read_b64_tr_b16 v[254:255], %[tr] offset:21632\n" \
  "s_waitcnt lgkmcnt(14)\n" \
  "ds_read_b64_tr_b16 v[216:217], %[tr] offset:22528\n" \
  "s_waitcnt lgkmcnt(14)\n" \
  "ds_read_b64_tr_b16 v[218:219], %[tr] offset:22656\n" \
  "s_waitcnt lgkmcnt(14)\n" \
  "ds_read_b64_tr_b16 v[220:221], %[tr] offset:23552\n" \
  "s_waitcnt lgkmcnt(14)\n" \
  "ds_read_b64_tr_b16 v[222:223], %[tr] offset:23680\n" \
  "s_lshr_b32 s55, %[id], 3\n" \
  "s_lshl_b32 s44, s55, 14\n" \
  "s_and_b32 s55, %[id], 7\n" \
  "s_mul_i32 s55, s55, 640\n" \
  "s_add_u32 s44, s44, s55\n" \
  "s_lshl_b32 s58, %[id], 19\n" \
  "s_lshl_b32 s55, %[w], 10\n" \
  "s_add_u32 s58, s58, s55\n" \
  "s_add_u32 s58, s58, 31457280\n" \
  "s_lshl_b32 s59, %[id], 17\n" \
  "s_add_u32 s59, s59, 10485760\n" \
  "s_lshl_b32 s55, %[id], 9\n" \
  "s_lshl_b32 s60, %[w], 7\n" \
  "s_add_u32 s60, s60, s55\n" \
  "s_add_u32 s60, s60, 6291456\n" \
  "s_add_u32 s63, s60, 32768\n" \
  "s_sub_u32 s60, s60, 128\n" \
  "s_lshl_b32 s45, %[w], 11\n" \
  "v_mov_b32 v172, 0x3e0293ee\n" \
  "s_waitcnt vmcnt(0) lgkmcnt(0)\n" \
  "s_mov_b32 s54, 6404096\n" \
  "v_lshlrev_b32 v180, 3, %[l16]\n" \
  "v_min_u32 v180, 1408, v180\n" \
  "Lasel%=:\n" \
  "buffer_load_dword v181, v180, s[40:43], s54 offen sc1\n" \
  "s_sub_u32 s66, s66, 1\n" \
  "s_waitcnt vmcnt(0)\n" \
  "v_cmp_eq_u32 vcc, 0, v181\n" \
  "s_cmp_lt_i32 s66, 1\n" \
  "s_cbranch_scc1 Lasok%=\n" \
  "s_cbranch_vccnz Lasel%=\n" \
  "Lasok%=:\n" \
  "v_cmp_eq_u32 vcc, 3, v181\n" \
  "s_nop 4\n" \
  "s_mov_b32 s65, vcc_lo\n" \
  "v_lshlrev_b32 v180, 14, v180\n" \
  "s_mov_b32 s54, 201333760\n" \
  "buffer_load_dword v181, v180, s[40:43], s54 offen sc1\n" \
  "s_waitcnt vmcnt(0)\n" \
  "s_mov_b32 s97, s71\n" \
  "s_and_b32 s49, s65, 1\n" \
  "s_lshl_b32 s49, s49, 13\n" \
  "s_add_u32 s49, s49, 201462784\n" \
  "s_mov_b32 s64, s49\n" \
  "s_and_b32 s55, %[id], 3\n" \
  "s_lshl_b32 s55, s55, 15\n" \
  "s_add_u32 s64, s64, s55\n" \
  "s_and_b32 s55, %[id], 12\n" \
  "s_lshl_b32 s55, s55, 15\n" \
  "s_add_u32 s64, s64, s55\n" \
  "s_lshr_b32 s55, %[id], 4\n" \
  "s_lshl_b32 s55, s55, 14\n" \
  "s_add_u32 s49, s49, s55\n" \
  "s_and_b32 s55, %[id], 15\n" \
  "s_lshl_b32 s55, s55, 7\n" \
  "s_add_u32 s49, s49, s55\n" \
  "s_lshl_b32 s55, %[w], 2\n" \
  "s_add_u32 s49, s49, s55\n" \
  "v_lshlrev_b32 v168, 11, %[l16]\n" \
  "v_mov_b32 v169, 0x80000000\n" \
  "v_cmp_gt_u32 vcc, 0x100, %[l16]\n" \
  "s_nop 1\n" \
  "v_cndmask_b32 v168, v169, v168, vcc\n" \
  "v_and_b32 v169, 0x100, %[l16]\n" \
  "v_lshlrev_b32 v169, 6, v169\n" \
  "v_and_b32 v170, 0xf0, %[l16]\n" \
  "v_lshl_or_b32 v169, v170, 3, v169\n" \
  "s_cmp_eq_u32 %[w], 0\n" \
  "s_cselect_b32 s55, 0x80000000, 0\n" \
  "v_or_b32 v171, s55, %[fo]\n" \
  "La0%=:\n" \
  "buffer_load_dword v181, %[qf], s[40:43], s60 offen sc1\n" \
  "s_sub_u32 s66, s66, 1\n" \
  "s_waitcnt vmcnt(0)\n" \
  "v_readlane_b32 s69, v181, 0\n" \
  "v_readlane_b32 s55, v181, 1\n" \
  "s_nop 3\n" \
  "s_min_u32 s69, s69, s55\n" \
  "s_cmp_lt_i32 s66, 1\n" \
  "s_cselect_b32 s55, 1, s69\n" \
  "s_cmp_eq_u32 s55, 0\n" \
  "s_cbranch_scc1 La0%=\n" \
  "s_mov_b32 m0, s45\n" \
  "s_add_u32 s54, s58, 4096\n" \
  "buffer_load_dwordx4 %[l16], s[40:43], s58 offen sc1 lds\n" \
  "s_add_u32 m0, s45, 1024\n" \
  "s_nop 0\n" \
  "buffer_load_dwordx4 %[l16], s[40:43], s54 offen sc1 lds\n" \
  "s_add_u32 s58, s58, 8192\n" \
  "s_xor_b32 s45, s45, 0xa000\n" \
  "s_cmp_eq_u32 %[w], 0\n" \
  "s_cselect_b32 s58, 0, s58\n" \
  "s_waitcnt vmcnt(0)\n" \
  "s_barrier\n" \
  "s_branch Lanext%=\n" \
  "Latop%=:\n" \
  "buffer_load_dwordx4 v[16:19], %[frag], s[40:43], s56 offen offset:0 sc1\n" \
  "v_mov_b32 v36, 0\n" \
  "v_mov_b32 v37, 0\n" \
  "v_mov_b32 v38, 0\n" \
  "v_mov_b32 v39, 0\n" \
  "v_mov_b32 v40, 0\n" \
  "v_mov_b32 v41, 0\n" \
  "v_mov_b32 v42, 0\n" \
  "buffer_load_dwordx4 v[20:23], %[frag], s[40:43], s56 offen offset:512 sc1\n" \
  "v_mov_b32 v43, 0\n" \
  "v_mov_b32 v44, 0\n" \
  "v_mov_b32 v45, 0\n" \
  "v_mov_b32 v46, 0\n" \
  "v_mov_b32 v47, 0\n" \
  "v_mov_b32 v48, 0\n" \
  "v_mov_b32 v49, 0\n" \
  "buffer_load_dwordx4 v[24:27], %[frag], s[40:43], s56 offen offset:1024 sc1\n" \
  "v_mov_b32 v50, 0\n" \
  "v_mov_b32 v51, 0\n" \
  "v_mov_b32 v52, 0\n" \
  "v_mov_b32 v53, 0\n" \
  "v_mov_b32 v54, 0\n" \
  "v_mov_b32 v55, 0\n" \
  "v_mov_b32 v56, 0\n" \
  "buffer_load_dwordx4 v[28:31], %[frag], s[40:43], s56 offen offset:1536 sc1\n" \
  "v_mov_b32 v57, 0\n" \
  "v_mov_b32 v58, 0\n" \
  "v_mov_b32 v59, 0\n" \
  "v_mov_b32 v60, 0\n" \
  "v_mov_b32 v61, 0\n" \
  "v_mov_b32 v62, 0\n" \
  "v_mov_b32 v63, 0\n" \
  "buffer_load_dwordx4 v[32:35], %[frag], s[40:43], s56 offen offset:2048 sc1\n" \
  "v_mov_b32 v64, 0\n" \
  "v_mov_b32 v65, 0\n" \
  "v_mov_b32 v66, 0\n" \
  "v_mov_b32 v67, 0\n" \
  "s_waitcnt vmcnt(4)\n" \
  "v_smfmac_f32_16x16x64_bf16 v[36:39], v[16:19], a[0:7], %[idx]\n" \
  "v_smfmac_f32_16x16x64_bf16 v[40:43], v[16:19], a[8:15], %[idx]\n" \
  "v_smfmac_f32_16x16x64_bf16 v[44:47], v[16:19], a[16:23], %[idx]\n" \
  "s_waitcnt vmcnt(3)\n" \
  "v_smfmac_f32_16x16x64_bf16 v[36:39], v[20:23], a[64:71], %[idx]\n" \
  "v_smfmac_f32_16x16x64_bf16 v[40:43], v[20:23], a[72:79], %[idx]\n" \
  "v_smfmac_f32_16x16x64_bf16 v[44:47], v[20:23], a[80:87], %[idx]\n" \
  "s_waitcnt vmcnt(2)\n" \
  "v_smfmac_f32_16x16x64_bf16 v[36:39], v[24:27], a[128:135], %[idx]\n" \
  "v_smfmac_f32_16x16x64_bf16 v[40:43], v[24:27], a[136:143], %[idx]\n" \
  "v_smfmac_f32_16x16x64_bf16 v[44:47], v[24:27], a[144:151], %[idx]\n" \
  "s_waitcnt vmcnt(1)\n" \
  "v_smfmac_f32_16x16x64_bf16 v[36:39], v[28:31], a[192:199], %[idx]\n" \
  "v_smfmac_f32_16x16x64_bf16 v[40:43], v[28:31], a[200:207], %[idx]\n" \
  "v_smfmac_f32_16x16x64_bf16 v[44:47], v[28:31], a[208:215], %[idx]\n" \
  "s_waitcnt vmcnt(0)\n" \
  "v_smfmac_f32_16x16x64_bf16 v[36:39], v[32:35], v[192:199], %[idx]\n" \
  "s_memrealtime s[88:89]\n" \
  "v_smfmac_f32_16x16x64_bf16 v[40:43], v[32:35], v[200:207], %[idx]\n" \
  "v_smfmac_f32_16x16x64_bf16 v[44:47], v[32:35], v[208:215], %[idx]\n" \
  "v_cmp_u_f32 s[50:51], v36, v37\n" \
  "v_cmp_u_f32 vcc, v38, v39\n" \
  "v_smfmac_f32_16x16x64_bf16 v[52:55], v[16:19], a[32:39], %[idx]\n" \
  "ds_write_b128 v177, v[36:39]\n" \
  "v_smfmac_f32_16x16x64_bf16 v[52:55], v[20:23], a[96:103], %[idx]\n" \
  "s_or_b64 vcc, vcc, s[50:51]\n" \
  "s_cbranch_scc1 Larst%=\n" \
  "Laok%=:\n" \
  "ds_write_b128 v178, v[40:43]\n" \
  "v_smfmac_f32_16x16x64_bf16 v[52:55], v[24:27], a[160:167], %[idx]\n" \
  "ds_write_b128 v179, v[44:47]\n" \
  "v_smfmac_f32_16x16x64_bf16 v[52:55], v[28:31], a[224:231], %[idx]\n" \
  "v_mov_b32 v181, s62\n" \
  "buffer_store_dword v181, v171, s[40:43], s63 offen sc1\n" \
  "v_smfmac_f32_16x16x64_bf16 v[52:55], v[32:35], v[224:231], %[idx]\n" \
  "v_smfmac_f32_16x16x64_bf16 v[48:51], v[16:19], a[24:31], %[idx]\n" \
  "s_waitcnt lgkmcnt(0)\n" \
  "s_barrier\n" \
  "ds_read_b128 v[68:71], v176 offset:0\n" \
  "ds_read_b128 v[72:75], v176 offset:1024\n" \
  "v_smfmac_f32_16x16x64_bf16 v[48:51], v[20:23], a[88:95], %[idx]\n" \
  "ds_read_b128 v[76:79], v176 offset:2048\n" \
  "s_add_u32 s88, s88, s98\n" \
  "s_and_b32 s88, s88, 0x1ffffff\n" \
  "s_or_b32 s88, s88, s52\n" \
  "v_mov_b32 v170, s88\n" \
  "v_smfmac_f32_16x16x64_bf16 v[48:51], v[24:27], a[152:159], %[idx]\n" \
  "v_cvt_pk_bf16_f32 v82, v52, v53\n" \
  "v_cvt_pk_bf16_f32 v83, v54, v55\n" \
  "buffer_store_dword v170, v168, s[40:43], s49 offen sc1\n" \
  "v_smfmac_f32_16x16x64_bf16 v[48:51], v[28:31], a[216:223], %[idx]\n" \
  "ds_write_b64 v183, v[82:83] offset:0\n" \
  "v_smfmac_f32_16x16x64_bf16 v[48:51], v[32:35], v[216:223], %[idx]\n" \
  "ds_read_b64_tr_b16 v[96:97], %[q0] offset:0\n" \
  "ds_read_b64_tr_b16 v[98:99], %[q1] offset:0\n" \
  "v_smfmac_f32_16x16x64_bf16 v[56:59], v[16:19], a[40:47], %[idx]\n" \
  "s_waitcnt lgkmcnt(4)\n" \
  "v_pk_add_f32 v[68:69], v[68:69], v[72:73]\n" \
  "v_smfmac_f32_16x16x64_bf16 v[56:59], v[20:23], a[104:111], %[idx]\n" \
  "v_pk_add_f32 v[70:71], v[70:71], v[74:75]\n" \
  "s_waitcnt lgkmcnt(3)\n" \
  "v_pk_add_f32 v[68:69], v[68:69], v[76:77]\n" \
  "v_smfmac_f32_16x16x64_bf16 v[56:59], v[24:27], a[168:175], %[idx]\n" \
  "v_pk_add_f32 v[70:71], v[70:71], v[78:79]\n" \
  "v_pk_add_f32 v[68:69], v[68:69], v[48:49]\n" \
  "v_smfmac_f32_16x16x64_bf16 v[56:59], v[28:31], a[232:239], %[idx]\n" \
  "v_pk_add_f32 v[70:71], v[70:71], v[50:51]\n" \
  "v_smfmac_f32_16x16x64_bf16 v[56:59], v[32:35], v[232:239], %[idx]\n" \
  "v_cvt_pk_bf16_f32 v80, v68, v69\n" \
  "v_cvt_pk_bf16_f32 v81, v70, v71\n" \
  "v_smfmac_f32_16x16x64_bf16 v[60:63], v[16:19], a[48:55], %[idx]\n" \
  "ds_write_b64 v182, v[80:81]\n" \
  "v_smfmac_f32_16x16x64_bf16 v[60:63], v[20:23], a[112:119], %[idx]\n" \
  "v_cvt_pk_bf16_f32 v84, v56, v57\n" \
  "v_cvt_pk_bf16_f32 v85, v58, v59\n" \
  "v_smfmac_f32_16x16x64_bf16 v[60:63], v[24:27], a[176:183], %[idx]\n" \
  "ds_write_b64 v183, v[84:85] offset:512\n" \
  "v_smfmac_f32_16x16x64_bf16 v[60:63], v[28:31], a[240:247], %[idx]\n" \
  "s_waitcnt lgkmcnt(1)\n" \
  "s_barrier\n" \
  "ds_read_b64_tr_b16 v[100:101], %[q0] offset:512\n" \
  "ds_read_b64_tr_b16 v[102:103], %[q1] offset:512\n" \
  "v_smfmac_f32_16x16x64_bf16 v[60:63], v[32:35], v[240:247], %[idx]\n" \
  "ds_read_b64_tr_b16 v[112:113], v184 offset:0\n" \
  "ds_read_b64_tr_b16 v[114:115], v184 offset:16\n" \
  "v_smfmac_f32_16x16x64_bf16 v[64:67], v[16:19], a[56:63], %[idx]\n" \
  "ds_read_b64_tr_b16 v[116:117], v185 offset:0\n" \
  "ds_read_b64_tr_b16 v[118:119], v185 offset:16\n" \
  "v_smfmac_f32_16x16x64_bf16 v[64:67], v[20:23], a[120:127], %[idx]\n" \
  "v_cvt_pk_bf16_f32 v86, v60, v61\n" \
  "v_cvt_pk_bf16_f32 v87, v62, v63\n" \
  "v_smfmac_f32_16x16x64_bf16 v[64:67], v[24:27], a[184:191], %[idx]\n" \
  "ds_write_b64 v183, v[86:87] offset:1024\n" \
  "ds_read_b64_tr_b16 v[104:105], %[q0] offset:1024\n" \
  "v_smfmac_f32_16x16x64_bf16 v[64:67], v[28:31], a[248:255], %[idx]\n" \
  "ds_read_b64_tr_b16 v[106:107], %[q1] offset:1024\n" \
  "ds_read_b64_tr_b16 v[120:121], v186 offset:0\n" \
  "v_smfmac_f32_16x16x64_bf16 v[64:67], v[32:35], v[248:255], %[idx]\n" \
  "ds_read_b64_tr_b16 v[122:123], v186 offset:16\n" \
  "ds_read_b64_tr_b16 v[124:125], v187 offset:0\n" \
  "v_mfma_f32_16x16x32_bf16 v[160:163], v[128:131], v[96:99], 0\n" \
  "ds_read_b64_tr_b16 v[126:127], v187 offset:16\n" \
  "v_mfma_f32_16x16x32_bf16 v[164:167], v[132:135], v[96:99], 0\n" \
  "v_cvt_pk_bf16_f32 v88, v64, v65\n" \
  "v_cvt_pk_bf16_f32 v89, v66, v67\n" \
  "ds_write_b64 v183, v[88:89] offset:1536\n" \
  "s_waitcnt lgkmcnt(12)\n" \
  "v_mfma_f32_16x16x32_bf16 v[160:163], v[136:139], v[100:103], v[160:163]\n" \
  "ds_read_b64_tr_b16 v[108:109], %[q0] offset:1536\n" \
  "ds_read_b64_tr_b16 v[110:111], %[q1] offset:1536\n" \
  "v_mfma_f32_16x16x32_bf16 v[164:167], v[140:143], v[100:103], v[164:167]\n" \
  "s_waitcnt lgkmcnt(12)\n" \
  "v_mfma_f32_16x16x32_bf16 v[92:95], v[112:115], v[96:99], 0\n" \
  "s_waitcnt lgkmcnt(10)\n" \
  "v_mfma_f32_16x16x32_bf16 v[92:95], v[116:119], v[100:103], v[92:95]\n" \
  "s_waitcnt lgkmcnt(7)\n" \
  "v_mfma_f32_16x16x32_bf16 v[160:163], v[144:147], v[104:107], v[160:163]\n" \
  "v_mfma_f32_16x16x32_bf16 v[164:167], v[148:151], v[104:107], v[164:167]\n" \
  "s_waitcnt lgkmcnt(5)\n" \
  "v_mfma_f32_16x16x32_bf16 v[92:95], v[120:123], v[104:107], v[92:95]\n" \
  "s_waitcnt lgkmcnt(0)\n" \
  "v_mfma_f32_16x16x32_bf16 v[160:163], v[152:155], v[108:111], v[160:163]\n" \
  "v_mfma_f32_16x16x32_bf16 v[164:167], v[156:159], v[108:111], v[164:167]\n" \
  "v_mfma_f32_16x16x32_bf16 v[92:95], v[124:127], v[108:111], v[92:95]\n" \
  "s_nop 3\n" \
  "v_add_f32_dpp v140, v164, v160 row_ror:8 row_mask:0xf bank_mask:0xf\n" \
  "v_add_f32_dpp v141, v165, v161 row_ror:8 row_mask:0xf bank_mask:0xf\n" \
  "v_add_f32_dpp v142, v166, v162 row_ror:8 row_mask:0xf bank_mask:0xf\n" \
  "v_add_f32_dpp v143, v167, v163 row_ror:8 row_mask:0xf bank_mask:0xf\n" \
  "s_cmp_lg_u32 %[w], 0\n" \
  "ds_write_b128 v174, v[140:143]\n" \
  "v_add_f32_dpp v144, v92, v92 row_ror:8 row_mask:0xf bank_mask:0x3\n" \
  "v_add_f32_dpp v144, v94, v94 row_ror:8 row_mask:0xf bank_mask:0xc\n" \
  "v_add_f32_dpp v145, v93, v93 row_ror:8 row_mask:0xf bank_mask:0x3\n" \
  "v_add_f32_dpp v145, v95, v95 row_ror:8 row_mask:0xf bank_mask:0xc\n" \
  "ds_write_b64 v173, v[144:145]\n" \
  "s_waitcnt lgkmcnt(0)\n" \
  "s_barrier\n" \
  "s_cbranch_scc1 Laidle%=\n" \
  "ds_read_b128 v[36:39], %[l16] offset:33792\n" \
  "ds_read_b64 v[48:49], v175 offset:512\n" \
  "ds_read_b128 v[40:43], %[l16] offset:34816\n" \
  "ds_read_b64 v[50:51], v175 offset:1024\n" \
  "ds_read_b128 v[44:47], %[l16] offset:35840\n" \
  "ds_read_b64 v[52:53], v175 offset:1536\n" \
  "s_waitcnt lgkmcnt(5)\n" \
  "v_pk_add_f32 v[140:141], v[140:141], v[36:37]\n" \
  "v_pk_add_f32 v[142:143], v[142:143], v[38:39]\n" \
  "s_waitcnt lgkmcnt(4)\n" \
  "v_pk_add_f32 v[144:145], v[144:145], v[48:49]\n" \
  "s_waitcnt lgkmcnt(1)\n" \
  "v_pk_add_f32 v[40:41], v[40:41], v[44:45]\n" \
  "v_pk_add_f32 v[42:43], v[42:43], v[46:47]\n" \
  "s_waitcnt lgkmcnt(0)\n" \
  "v_pk_add_f32 v[50:51], v[50:51], v[52:53]\n" \
  "v_pk_add_f32 v[140:141], v[140:141], v[40:41]\n" \
  "v_pk_add_f32 v[142:143], v[142:143], v[42:43]\n" \
  "v_pk_add_f32 v[144:145], v[144:145], v[50:51]\n" \
  "v_max3_f32 v56, v140, v141, v142\n" \
  "v_max3_f32 v56, v56, v143, v144\n" \
  "v_max_f32 v56, v56, v145\n" \
  "s_nop 1\n" \
  "v_max_f32_dpp v57, v56, v56 row_ror:8 row_mask:0xf bank_mask:0xf\n" \
  "v_mov_b32 v56, v57\n" \
  "s_nop 1\n" \
  "v_permlane16_swap_b32 v56, v57\n" \
  "v_max_f32 v56, v56, v57\n" \
  "v_mov_b32 v57, v56\n" \
  "s_nop 1\n" \
  "v_permlane32_swap_b32 v56, v57\n" \
  "v_max_f32 v56, v56, v57\n" \
  "v_mul_f32 v56, v172, v56\n" \
  "v_fma_f32 v140, v140, v172, -v56\n" \
  "v_fma_f32 v141, v141, v172, -v56\n" \
  "v_fma_f32 v142, v142, v172, -v56\n" \
  "v_fma_f32 v143, v143, v172, -v56\n" \
  "v_fma_f32 v144, v144, v172, -v56\n" \
  "v_fma_f32 v145, v145, v172, -v56\n" \
  "v_exp_f32 v140, v140\n" \
  "v_exp_f32 v141, v141\n" \
  "v_exp_f32 v142, v142\n" \
  "v_exp_f32 v143, v143\n" \
  "v_exp_f32 v144, v144\n" \
  "v_exp_f32 v145, v145\n" \
  "v_add_f32 v58, v140, v141\n" \
  "v_add_f32 v59, v142, v143\n" \
  "v_add_f32 v60, v144, v145\n" \
  "v_add_f32 v58, v58, v59\n" \
  "v_fma_f32 v58, v60, %[msk], v58\n" \
  "s_nop 1\n" \
  "v_add_f32_dpp v59, v58, v58 row_ror:8 row_mask:0xf bank_mask:0xf\n" \
  "v_mov_b32 v58, v59\n" \
  "s_nop 1\n" \
  "v_permlane16_swap_b32 v58, v59\n" \
  "v_add_f32 v58, v58, v59\n" \
  "v_mov_b32 v59, v58\n" \
  "s_nop 1\n" \
  "v_permlane32_swap_b32 v58, v59\n" \
  "v_add_f32 v58, v58, v59\n" \
  "v_rcp_f32 v58, v58\n" \
  "s_nop 0\n" \
  "v_pk_mul_f32 v[144:145], v[144:145], v[58:59] op_sel_hi:[1,0]\n" \
  "v_pk_mul_f32 v[140:141], v[140:141], v[58:59] op_sel_hi:[1,0]\n" \
  "v_cvt_pk_bf16_f32 v66, v144, v145\n" \
  "v_cvt_pk_bf16_f32 v65, v144, v145\n" \
  "v_pk_mul_f32 v[142:143], v[142:143], v[58:59] op_sel_hi:[1,0]\n" \
  "v_cvt_pk_bf16_f32 v62, v140, v141\n" \
  "v_permlane16_swap_b32 v66, v65\n" \
  "v_cvt_pk_bf16_f32 v63, v142, v143\n" \
  "v_mov_b32_dpp v64, v62 row_ror:8 row_mask:0xf bank_mask:0x3\n" \
  "v_mov_b32_dpp v64, v65 row_ror:8 row_mask:0xf bank_mask:0xc\n" \
  "v_mov_b32_dpp v62, v66 row_ror:8 row_mask:0xf bank_mask:0xc\n" \
  "v_mov_b32_dpp v65, v63 row_ror:8 row_mask:0xf bank_mask:0x3\n" \
  "v_mov_b32_dpp v63, v66 quad_perm:[0,1,2,3] row_mask:0xf bank_mask:0xc\n" \
  "buffer_store_dwordx4 v[62:65], %[pox], s[40:43], s61 offen sc1\n" \
  "buffer_store_dwordx4 v[62:65], %[pox], s[40:43], s46 offen sc1\n" \
  "buffer_store_dwordx4 v[62:65], %[pox], s[40:43], s47 offen sc1\n" \
  "buffer_store_dwordx4 v[62:65], %[pox], s[40:43], s48 offen sc1\n" \
  "buffer_load_dwordx4 v[36:39], v169, s[40:43], s64 offen sc1\n" \
  "s_branch Lajoin%=\n" \
  "Laidle%=:\n" \
  "s_mov_b32 s53, s62\n" \
  "buffer_store_dwordx4 v[112:115], %[kmo], s[40:43], s59 offen sc1\n" \
  "buffer_store_dwordx4 v[124:127], %[kmo2], s[40:43], s59 offen sc1\n" \
  "s_add_u32 s55, s62, 3\n" \
  "s_min_u32 s55, s55, 64\n" \
  "s_cmp_ge_u32 s69, s55\n" \
  "s_cbranch_scc0 Lacnt%=\n" \
  "Lago%=:\n" \
  "s_mov_b32 m0, s45\n" \
  "s_add_u32 s54, s58, 4096\n" \
  "buffer_load_dwordx4 %[l16], s[40:43], s58 offen sc1 lds\n" \
  "s_add_u32 m0, s45, 1024\n" \
  "s_nop 0\n" \
  "buffer_load_dwordx4 %[l16], s[40:43], s54 offen sc1 lds\n" \
  "s_add_u32 s58, s58, 8192\n" \
  "s_xor_b32 s45, s45, 0xa000\n" \
  "s_cmp_lg_u32 %[w], 1\n" \
  "s_cbranch_scc1 Lak1%=\n" \
  "s_xor_b32 m0, s45, 0xa800\n" \
  "s_sub_u32 s54, s58, 9216\n" \
  "buffer_load_dwordx4 %[l16], s[40:43], s54 offen sc1 lds\n" \
  "s_xor_b32 m0, s45, 0xac00\n" \
  "s_add_u32 s54, s54, 4096\n" \
  "buffer_load_dwordx4 %[l16], s[40:43], s54 offen sc1 lds\n" \
  "Lak1%=:\n" \
  "s_cmp_eq_u32 s53, 0\n" \
  "s_mov_b32 s53, 1\n" \
  "s_cbranch_scc1 Lago%=\n" \
  "Lajoin%=:\n" \
  "s_add_u32 s59, s59, 2048\n" \
  "s_add_u32 s62, s62, 1\n" \
  "v_xor_b32 v188, 0xa000, v188\n" \
  "v_xor_b32 v189, 0xa000, v189\n" \
  "v_xor_b32 v190, 0xa000, v190\n" \
  "v_xor_b32 v191, 0xa000, v191\n" \
  "s_cmp_eq_u32 s62, 64\n" \
  "s_cbranch_scc1 Laend%=\n" \
  "s_cmp_lg_u32 s62, 1\n" \
  "s_cbranch_scc1 Lanext%=\n" \
  "s_waitcnt vmcnt(0)\n" \
  "s_barrier\n" \
  "Lanext%=:\n" \
  "ds_read2_b64 v[128:131], v188 offset0:0 offset1:129\n" \
  "ds_read2_b64 v[132:135], v188 offset0:128 offset1:1\n" \
  "ds_read2_b64 v[136:139], v189 offset0:0 offset1:129\n" \
  "ds_read2_b64 v[140:143], v189 offset0:128 offset1:1\n" \
  "ds_read2_b64 v[144:147], v190 offset0:0 offset1:129\n" \
  "ds_read2_b64 v[148:151], v190 offset0:128 offset1:1\n" \
  "ds_read2_b64 v[152:155], v191 offset0:0 offset1:129\n" \
  "ds_read2_b64 v[156:159], v191 offset0:128 offset1:1\n" \
  "s_memtime s[50:51]\n" \
  "s_memrealtime s[88:89]\n" \
  "s_lshr_b32 s55, s62, 4\n" \
  "s_lshr_b32 s55, s65, s55\n" \
  "s_and_b32 s55, s55, 1\n" \
  "s_lshl_b32 s55, s55, 13\n" \
  "s_add_u32 s55, s55, s57\n" \
  "s_lshl_b32 s56, s62, 17\n" \
  "s_add_u32 s56, s56, s55\n" \
  "s_add_u32 s56, s56, 201326592\n" \
  "s_lshr_b32 s55, s62, 3\n" \
  "s_add_u32 s55, s55, 4\n" \
  "s_lshr_b32 s55, s65, s55\n" \
  "s_and_b32 s55, s55, 1\n" \
  "s_lshl_b32 s55, s55, 13\n" \
  "s_add_u32 s55, s55, s44\n" \
  "s_lshl_b32 s61, s62, 18\n" \
  "s_add_u32 s61, s61, s55\n" \
  "s_add_u32 s61, s61, 209715200\n" \
  "s_add_u32 s46, s61, 65536\n" \
  "s_add_u32 s47, s61, 131072\n" \
  "s_add_u32 s48, s61, 196608\n" \
  "s_waitcnt lgkmcnt(0)\n" \
  "s_sub_u32 s50, s50, s70\n" \
  "s_sub_u32 s88, s88, s97\n" \
  "v_cvt_f32_u32 v40, s50\n" \
  "v_cvt_f32_u32 v41, s88\n" \
  "v_rcp_f32 v40, v40\n" \
  "v_cvt_f32_u32 v42, %[ca]\n" \
  "v_mul_f32 v40, v40, v41\n" \
  "v_mul_f32 v41, 0x44160000, v40\n" \
  "v_mul_f32 v40, v42, v40\n" \
  "v_cvt_u32_f32 v41, v41\n" \
  "v_cvt_u32_f32 v40, v40\n" \
  "v_readfirstlane_b32 s99, v41\n" \
  "v_readfirstlane_b32 s98, v40\n" \
  "s_cmp_lg_u32 %[w], 0\n" \
  "s_cbranch_scc1 Lagt%=\n" \
  "s_cmp_eq_u32 s62, 0\n" \
  "s_cbranch_scc1 Lagt%=\n" \
  "Latc%=:\n" \
  "s_waitcnt vmcnt(0)\n" \
  "v_min3_u32 v180, v36, v37, v38\n" \
  "v_max3_u32 v36, v36, v37, v38\n" \
  "v_min_u32 v180, v180, v39\n" \
  "v_max_u32 v36, v36, v39\n" \
  "v_cmp_gt_u32 vcc, s52, v180\n" \
  "s_nop 0\n" \
  "v_max_u32_dpp v36, v36, v36 row_ror:8 row_mask:0xf bank_mask:0xf\n" \
  "s_nop 1\n" \
  "v_max_u32_dpp v36, v36, v36 row_ror:4 row_mask:0xf bank_mask:0xf\n" \
  "s_cbranch_vccnz Latr%=\n" \
  "Latk%=:\n" \
  "s_nop 0\n" \
  "v_max_u32_dpp v36, v36, v36 row_ror:2 row_mask:0xf bank_mask:0xf\n" \
  "s_nop 1\n" \
  "v_max_u32_dpp v36, v36, v36 row_ror:1 row_mask:0xf bank_mask:0xf\n" \
  "s_nop 0\n" \
  "v_readlane_b32 s53, v36, 0\n" \
  "v_readlane_b32 s55, v36, 16\n" \
  "s_nop 3\n" \
  "s_max_u32 s53, s53, s55\n" \
  "s_sub_u32 s55, s53, s45\n" \
  "s_and_b32 s55, s55, 0x1ffffff\n" \
  "s_sub_u32 s55, s55, s98\n" \
  "s_cmp_gt_i32 s55, 50\n" \
  "s_cselect_b32 s55, 8, 0\n" \
  "s_cselect_b32 s54, 16, -1\n" \
  "s_add_i32 s58, s58, s54\n" \
  "s_max_i32 s58, s58, 0\n" \
  "s_min_i32 s58, s58, 48\n" \
  "s_lshr_b32 s54, s58, 2\n" \
  "s_add_u32 s55, s55, s54\n" \
  "s_add_u32 s53, s53, %[d1]\n" \
  "s_add_u32 s53, s53, s99\n" \
  "s_add_u32 s53, s53, s55\n" \
  "s_mov_b32 s45, s53\n" \
  "Lau%=:\n" \
  "s_memrealtime s[88:89]\n" \
  "s_waitcnt lgkmcnt(0)\n" \
  "s_sub_u32 s55, s53, s88\n" \
  "s_and_b32 s55, s55, 0x1ffffff\n" \
  "s_sub_u32 s55, s55, 1\n" \
  "s_cmp_lt_u32 s55, 400\n" \
  "s_cbranch_scc1 Lau%=\n" \
  "Lagt%=:\n" \
  "s_add_u32 s52, s62, 1\n" \
  "s_lshl_b32 s52, s52, 25\n" \
  "s_waitcnt lgkmcnt(0)\n" \
  "s_barrier\n" \
  "s_branch Latop%=\n" \
  "Latr%=:\n" \
  "s_add_u32 s68, s68, 1\n" \
  "s_sub_u32 s66, s66, 1\n" \
  "s_cmp_gt_i32 s66, 0\n" \
  "s_cbranch_scc0 Latk%=\n" \
  "s_sleep 2\n" \
  "buffer_load_dwordx4 v[36:39], v169, s[40:43], s64 offen sc1\n" \
  "s_branch Latc%=\n" \
  "Larst%=:\n" \
  "v_max_u32 v180, v16, v17\n" \
  "v_max3_u32 v180, v180, v18, v19\n" \
  "v_max3_u32 v180, v180, v20, v21\n" \
  "v_max3_u32 v180, v180, v22, v23\n" \
  "v_max3_u32 v180, v180, v24, v25\n" \
  "v_max3_u32 v180, v180, v26, v27\n" \
  "v_max3_u32 v180, v180, v28, v29\n" \
  "v_max3_u32 v180, v180, v30, v31\n" \
  "v_max3_u32 v180, v180, v32, v33\n" \
  "v_max3_u32 v180, v180, v34, v35\n" \
  "v_cmp_eq_u32 vcc, -1, v180\n" \
  "s_cbranch_vccz Laok%=\n" \
  "s_add_u32 s67, s67, 1\n" \
  "s_sub_u32 s66, s66, 1\n" \
  "s_cmp_gt_i32 s66, 0\n" \
  "s_cbranch_scc1 Latop%=\n" \
  "s_branch Laok%=\n" \
  "Lacnt%=:\n" \
  "buffer_load_dword v181, %[qf], s[40:43], s60 offen sc1\n" \
  "s_add_u32 s68, s68, 1\n" \
  "s_sub_u32 s66, s66, 1\n" \
  "s_waitcnt vmcnt(0)\n" \
  "v_readlane_b32 s69, v181, 0\n" \
  "v_readlane_b32 s54, v181, 1\n" \
  "s_nop 3\n" \
  "s_min_u32 s69, s69, s54\n" \
  "s_cmp_ge_u32 s69, s55\n" \
  "s_cbranch_scc1 Lago%=\n" \
  "s_cmp_gt_i32 s66, 0\n" \
  "s_cbranch_scc1 Lacnt%=\n" \
  "s_branch Lago%=\n" \
  "Laend%=:\n" \
  "v_mov_b32 v181, s62\n" \
  "s_waitcnt vmcnt(0)\n" \
  "buffer_store_dword v181, v171, s[40:43], s63 offen sc1\n" \
  "s_waitcnt vmcnt(0) lgkmcnt(0)\n"

#define ASM_RB \
  "s_nop 4\n" \
  "s_mov_b64 s[40:41], %[ws]\n" \
  "s_mov_b32 s42, 0x0ff00000\n" \
  "s_mov_b32 s43, 0x20000\n" \
  "s_lshr_b32 s55, %[w], 1\n" \
  "s_lshl_b32 s57, s55, 14\n" \
  "s_and_b32 s55, %[w], 1\n" \
  "s_mul_i32 s55, s55, 2560\n" \
  "s_add_u32 s57, s57, s55\n" \
  "s_lshl_b32 s55, %[cp], 15\n" \
  "s_add_u32 s57, s57, s55\n" \
  "s_mov_b32 s62, 0\n" \
  "s_mov_b32 s66, 131072\n" \
  "s_mov_b32 s67, 0\n" \
  "s_mov_b32 s68, 0\n" \
  "s_mul_i32 s55, %[w], 2560\n" \
  "v_add_u32 v182, s55, %[frag]\n" \
  "s_add_u32 s55, %[w], 1\n" \
  "s_and_b32 s55, s55, 3\n" \
  "s_mul_i32 s55, s55, 2560\n" \
  "v_add_u32 v183, s55, %[frag]\n" \
  "s_add_u32 s55, %[w], 2\n" \
  "s_and_b32 s55, s55, 3\n" \
  "s_mul_i32 s55, s55, 2560\n" \
  "v_add_u32 v184, s55, %[frag]\n" \
  "s_add_u32 s55, %[w], 3\n" \
  "s_and_b32 s55, s55, 3\n" \
  "s_mul_i32 s55, s55, 2560\n" \
  "v_add_u32 v185, s55, %[frag]\n" \
  "s_lshl_b32 s54, %[id], 2\n" \
  "s_add_u32 s54, s54, %[w]\n" \
  "s_mul_i32 s54, s54, 81920\n" \
  "s_add_u32 s54, s54, 85983232\n" \
  "s_add_u32 s55, %[w], 0\n" \
  "s_and_b32 s55, s55, 3\n" \
  "s_mul_i32 s58, s55, 20480\n" \
  "s_add_u32 s58, s58, s54\n" \
  "s_add_u32 s55, %[w], 1\n" \
  "s_and_b32 s55, s55, 3\n" \
  "s_mul_i32 s59, s55, 20480\n" \
  "s_add_u32 s59, s59, s54\n" \
  "s_add_u32 s55, %[w], 2\n" \
  "s_and_b32 s55, s55, 3\n" \
  "s_mul_i32 s60, s55, 20480\n" \
  "s_add_u32 s60, s60, s54\n" \
  "s_add_u32 s55, %[w], 3\n" \
  "s_and_b32 s55, s55, 3\n" \
  "s_mul_i32 s61, s55, 20480\n" \
  "s_add_u32 s61, s61, s54\n" \
  "s_add_u32 s53, s58, 0\n" \
  "buffer_load_dwordx4 a[0:3], %[l16], s[40:43], s53 offen offset:0\n" \
  "buffer_load_dwordx4 a[4:7], %[l16], s[40:43], s53 offen offset:1024\n" \
  "buffer_load_dwordx4 a[8:11], %[l16], s[40:43], s53 offen offset:2048\n" \
  "buffer_load_dwordx4 a[12:15], %[l16], s[40:43], s53 offen offset:3072\n" \
  "s_add_u32 s53, s58, 4096\n" \
  "buffer_load_dwordx4 a[16:19], %[l16], s[40:43], s53 offen offset:0\n" \
  "buffer_load_dwordx4 a[20:23], %[l16], s[40:43], s53 offen offset:1024\n" \
  "buffer_load_dwordx4 a[24:27], %[l16], s[40:43], s53 offen offset:2048\n" \
  "buffer_load_dwordx4 a[28:31], %[l16], s[40:43], s53 offen offset:3072\n" \
  "s_add_u32 s53, s58, 8192\n" \
  "buffer_load_dwordx4 a[32:35], %[l16], s[40:43], s53 offen offset:0\n" \
  "buffer_load_dwordx4 a[36:39], %[l16], s[40:43], s53 offen offset:1024\n" \
  "buffer_load_dwordx4 a[40:43], %[l16], s[40:43], s53 offen offset:2048\n" \
  "buffer_load_dwordx4 a[44:47], %[l16], s[40:43], s53 offen offset:3072\n" \
  "s_add_u32 s53, s58, 12288\n" \
  "buffer_load_dwordx4 a[48:51], %[l16], s[40:43], s53 offen offset:0\n" \
  "buffer_load_dwordx4 a[52:55], %[l16], s[40:43], s53 offen offset:1024\n" \
  "buffer_load_dwordx4 a[56:59], %[l16], s[40:43], s53 offen offset:2048\n" \
  "buffer_load_dwordx4 a[60:63], %[l16], s[40:43], s53 offen offset:3072\n" \
  "s_add_u32 s53, s58, 16384\n" \
  "buffer_load_dwordx4 a[64:67], %[l16], s[40:43], s53 offen offset:0\n" \
  "buffer_load_dwordx4 a[68:71], %[l16], s[40:43], s53 offen offset:1024\n" \
  "buffer_load_dwordx4 a[72:75], %[l16], s[40:43], s53 offen offset:2048\n" \
  "buffer_load_dwordx4 a[76:79], %[l16], s[40:43], s53 offen offset:3072\n" \
  "s_add_u32 s53, s59, 0\n" \
  "buffer_load_dwordx4 a[80:83], %[l16], s[40:43], s53 offen offset:0\n" \
  "buffer_load_dwordx4 a[84:87], %[l16], s[40:43], s53 offen offset:1024\n" \
  "buffer_load_dwordx4 a[88:91], %[l16], s[40:43], s53 offen offset:2048\n" \
  "buffer_load_dwordx4 a[92:95], %[l16], s[40:43], s53 offen offset:3072\n" \
  "s_add_u32 s53, s59, 4096\n" \
  "buffer_load_dwordx4 a[96:99], %[l16], s[40:43], s53 offen offset:0\n" \
  "buffer_load_dwordx4 a[100:103], %[l16], s[40:43], s53 offen offset:1024\n" \
  "buffer_load_dwordx4 a[104:107], %[l16], s[40:43], s53 offen offset:2048\n" \
  "buffer_load_dwordx4 a[108:111], %[l16], s[40:43], s53 offen offset:3072\n" \
  "s_add_u32 s53, s59, 8192\n" \
  "buffer_load_dwordx4 a[112:115], %[l16], s[40:43], s53 offen offset:0\n" \
  "buffer_load_dwordx4 a[116:119], %[l16], s[40:43], s53 offen offset:1024\n" \
  "buffer_load_dwordx4 a[120:123], %[l16], s[40:43], s53 offen offset:2048\n" \
  "buffer_load_dwordx4 a[124:127], %[l16], s[40:43], s53 offen offset:3072\n" \
  "s_add_u32 s53, s59, 12288\n" \
  "buffer_load_dwordx4 a[128:131], %[l16], s[40:43], s53 offen offset:0\n" \
  "buffer_load_dwordx4 a[132:135], %[l16], s[40:43], s53 offen offset:1024\n" \
  "buffer_load_dwordx4 a[136:139], %[l16], s[40:43], s53 offen offset:2048\n" \
  "buffer_load_dwordx4 a[140:143], %[l16], s[40:43], s53 offen offset:3072\n" \
  "s_add_u32 s53, s59, 16384\n" \
  "buffer_load_dwordx4 a[144:147], %[l16], s[40:43], s53 offen offset:0\n" \
  "buffer_load_dwordx4 a[148:151], %[l16], s[40:43], s53 offen offset:1024\n" \
  "buffer_load_dwordx4 a[152:155], %[l16], s[40:43], s53 offen offset:2048\n" \
  "buffer_load_dwordx4 a[156:159], %[l16], s[40:43], s53 offen offset:3072\n" \
  "s_add_u32 s53, s60, 0\n" \
  "buffer_load_dwordx4 a[160:163], %[l16], s[40:43], s53 offen offset:0\n" \
  "buffer_load_dwordx4 a[164:167], %[l16], s[40:43], s53 offen offset:1024\n" \
  "buffer_load_dwordx4 a[168:171], %[l16], s[40:43], s53 offen offset:2048\n" \
  "buffer_load_dwordx4 a[172:175], %[l16], s[40:43], s53 offen offset:3072\n" \
  "s_add_u32 s53, s60, 4096\n" \
  "buffer_load_dwordx4 a[176:179], %[l16], s[40:43], s53 offen offset:0\n" \
  "buffer_load_dwordx4 a[180:183], %[l16], s[40:43], s53 offen offset:1024\n" \
  "buffer_load_dwordx4 a[184:187], %[l16], s[40:43], s53 offen offset:2048\n" \
  "buffer_load_dwordx4 a[188:191], %[l16], s[40:43], s53 offen offset:3072\n" \
  "s_add_u32 s53, s60, 8192\n" \
  "buffer_load_dwordx4 a[192:195], %[l16], s[40:43], s53 offen offset:0\n" \
  "buffer_load_dwordx4 a[196:199], %[l16], s[40:43], s53 offen offset:1024\n" \
  "buffer_load_dwordx4 a[200:203], %[l16], s[40:43], s53 offen offset:2048\n" \
  "buffer_load_dwordx4 a[204:207], %[l16], s[40:43], s53 offen offset:3072\n" \
  "s_add_u32 s53, s60, 12288\n" \
  "buffer_load_dwordx4 a[208:211], %[l16], s[40:43], s53 offen offset:0\n" \
  "buffer_load_dwordx4 a[212:215], %[l16], s[40:43], s53 offen offset:1024\n" \
  "buffer_load_dwordx4 a[216:219], %[l16], s[40:43], s53 offen offset:2048\n" \
  "buffer_load_dwordx4 a[220:223], %[l16], s[40:43], s53 offen offset:3072\n" \
  "s_add_u32 s53, s60, 16384\n" \
  "buffer_load_dwordx4 a[224:227], %[l16], s[40:43], s53 offen offset:0\n" \
  "buffer_load_dwordx4 a[228:231], %[l16], s[40:43], s53 offen offset:1024\n" \
  "buffer_load_dwordx4 a[232:235], %[l16], s[40:43], s53 offen offset:2048\n" \
  "buffer_load_dwordx4 a[236:239], %[l16], s[40:43], s53 offen offset:3072\n" \
  "s_add_u32 s53, s61, 0\n" \
  "s_waitcnt vmcnt(56)\n" \
  "buffer_load_dwordx4 a[240:243], %[l16], s[40:43], s53 offen offset:0\n" \
  "buffer_load_dwordx4 a[244:247], %[l16], s[40:43], s53 offen offset:1024\n" \
  "buffer_load_dwordx4 a[248:251], %[l16], s[40:43], s53 offen offset:2048\n" \
  "buffer_load_dwordx4 a[252:255], %[l16], s[40:43], s53 offen offset:3072\n" \
  "s_add_u32 s53, s61, 4096\n" \
  "s_waitcnt vmcnt(56)\n" \
  "buffer_load_dwordx4 v[192:195], %[l16], s[40:43], s53 offen offset:0\n" \
  "buffer_load_dwordx4 v[196:199], %[l16], s[40:43], s53 offen offset:1024\n" \
  "buffer_load_dwordx4 v[200:203], %[l16], s[40:43], s53 offen offset:2048\n" \
  "buffer_load_dwordx4 v[204:207], %[l16], s[40:43], s53 offen offset:3072\n" \
  "s_add_u32 s53, s61, 8192\n" \
  "s_waitcnt vmcnt(56)\n" \
  "buffer_load_dwordx4 v[208:211], %[l16], s[40:43], s53 offen offset:0\n" \
  "buffer_load_dwordx4 v[212:215], %[l16], s[40:43], s53 offen offset:1024\n" \
  "buffer_load_dwordx4 v[216:219], %[l16], s[40:43], s53 offen offset:2048\n" \
  "buffer_load_dwordx4 v[220:223], %[l16], s[40:43], s53 offen offset:3072\n" \
  "s_add_u32 s53, s61, 12288\n" \
  "s_waitcnt vmcnt(56)\n" \
  "buffer_load_dwordx4 v[224:227], %[l16], s[40:43], s53 offen offset:0\n" \
  "buffer_load_dwordx4 v[228:231], %[l16], s[40:43], s53 offen offset:1024\n" \
  "buffer_load_dwordx4 v[232:235], %[l16], s[40:43], s53 offen offset:2048\n" \
  "buffer_load_dwordx4 v[236:239], %[l16], s[40:43], s53 offen offset:3072\n" \
  "s_add_u32 s53, s61, 16384\n" \
  "s_waitcnt vmcnt(56)\n" \
  "buffer_load_dwordx4 v[240:243], %[l16], s[40:43], s53 offen offset:0\n" \
  "buffer_load_dwordx4 v[244:247], %[l16], s[40:43], s53 offen offset:1024\n" \
  "buffer_load_dwordx4 v[248:251], %[l16], s[40:43], s53 offen offset:2048\n" \
  "buffer_load_dwordx4 v[252:255], %[l16], s[40:43], s53 offen offset:3072\n" \
  "s_lshl_b32 s44, %[cp], 16\n" \
  "s_lshl_b32 s55, %[w], 12\n" \
  "s_add_u32 s58, %[zs], s55\n" \
  "s_add_u32 s45, s55, 16384\n" \
  "s_lshl_b32 s59, %[id], 18\n" \
  "s_add_u32 s59, s59, 65011712\n" \
  "s_mov_b32 s60, 6307840\n" \
  "s_lshl_b32 s63, %[id], 9\n" \
  "s_lshl_b32 s55, %[w], 7\n" \
  "s_add_u32 s63, s63, s55\n" \
  "s_add_u32 s63, s63, 6340608\n" \
  "s_lshl_b32 s55, %[w], 10\n" \
  "v_add_u32 v187, s55, %[zmr]\n" \
  "s_add_u32 s55, s55, 10240\n" \
  "v_add_u32 v186, s55, %[zao]\n" \
  "s_waitcnt vmcnt(0)\n" \
  "s_mov_b32 s54, 6404096\n" \
  "v_lshlrev_b32 v180, 3, %[l16]\n" \
  "v_min_u32 v180, 1408, v180\n" \
  "Lbsel%=:\n" \
  "buffer_load_dword v181, v180, s[40:43], s54 offen sc1\n" \
  "s_sub_u32 s66, s66, 1\n" \
  "s_waitcnt vmcnt(0)\n" \
  "v_cmp_eq_u32 vcc, 0, v181\n" \
  "s_cmp_lt_i32 s66, 1\n" \
  "s_cbranch_scc1 Lbsok%=\n" \
  "s_cbranch_vccnz Lbsel%=\n" \
  "Lbsok%=:\n" \
  "v_cmp_eq_u32 vcc, 3, v181\n" \
  "s_nop 4\n" \
  "s_mov_b32 s65, vcc_lo\n" \
  "v_lshlrev_b32 v180, 14, v180\n" \
  "s_mov_b32 s54, 201333760\n" \
  "buffer_load_dword v181, v180, s[40:43], s54 offen sc1\n" \
  "s_waitcnt vmcnt(0)\n" \
  "v_mov_b32 v181, 0\n" \
  "s_and_b32 s51, s65, 1\n" \
  "s_lshl_b32 s51, s51, 13\n" \
  "s_add_u32 s51, s51, 201462784\n" \
  "s_and_b32 s55, %[id], 3\n" \
  "s_lshl_b32 s55, s55, 15\n" \
  "s_add_u32 s51, s51, s55\n" \
  "s_and_b32 s55, %[id], 12\n" \
  "s_lshl_b32 s55, s55, 15\n" \
  "s_add_u32 s51, s51, s55\n" \
  "v_and_b32 v188, 0x100, %[l16]\n" \
  "v_lshlrev_b32 v188, 6, v188\n" \
  "v_and_b32 v189, 0xf0, %[l16]\n" \
  "v_lshl_or_b32 v188, v189, 3, v188\n" \
  "s_mov_b32 s52, 0\n" \
  "s_cmp_lg_u32 %[w], 0\n" \
  "s_cselect_b32 s55, 0x80000000, 0\n" \
  "v_or_b32 v188, s55, v188\n" \
  "v_add_u32 v189, 0x4000, %[l16]\n" \
  "Lb0%=:\n" \
  "buffer_load_dword v181, %[co], s[40:43], s60 offen sc1\n" \
  "s_sub_u32 s66, s66, 1\n" \
  "s_waitcnt vmcnt(0)\n" \
  "v_cmp_eq_u32 vcc, 0, v181\n" \
  "s_cmp_lt_i32 s66, 1\n" \
  "s_cbranch_scc1 Lb1%=\n" \
  "s_cbranch_vccnz Lb0%=\n" \
  "Lb1%=:\n" \
  "s_mov_b32 m0, s45\n" \
  "s_nop 0\n" \
  "buffer_load_dwordx4 %[zxo], s[40:43], s58 offen offset:0 sc1 lds\n" \
  "buffer_load_dwordx4 %[zxo], s[40:43], s58 offen offset:1024 sc1 lds\n" \
  "buffer_load_dwordx4 %[zxo], s[40:43], s58 offen offset:2048 sc1 lds\n" \
  "buffer_load_dwordx4 %[zxo], s[40:43], s58 offen offset:3072 sc1 lds\n" \
  "s_waitcnt vmcnt(0)\n" \
  "s_mov_b32 s53, 0\n" \
  "s_branch Lbnext%=\n" \
  "Lbtop%=:\n" \
  "buffer_load_dwordx4 v[16:19], %[frag], s[40:43], s56 offen offset:0 sc1\n" \
  "buffer_load_dwordx4 v[20:23], %[frag], s[40:43], s56 offen offset:512 sc1\n" \
  "buffer_load_dwordx4 v[24:27], %[frag], s[40:43], s56 offen offset:1024 sc1\n" \
  "buffer_load_dwordx4 v[28:31], %[frag], s[40:43], s56 offen offset:1536 sc1\n" \
  "buffer_load_dwordx4 v[32:35], %[frag], s[40:43], s56 offen offset:2048 sc1\n" \
  "v_mov_b32 v96, 0\n" \
  "v_mov_b32 v97, 0\n" \
  "v_mov_b32 v98, 0\n" \
  "v_mov_b32 v99, 0\n" \
  "v_mov_b32 v100, 0\n" \
  "v_mov_b32 v101, 0\n" \
  "v_mov_b32 v102, 0\n" \
  "v_mov_b32 v103, 0\n" \
  "s_waitcnt vmcnt(4)\n" \
  "ds_write_b128 v182, v[16:19] offset:0\n" \
  "v_smfmac_f32_16x16x64_bf16 v[96:99], v[16:19], a[0:7], %[idx]\n" \
  "v_max3_u32 v180, v16, v17, v18\n" \
  "v_smfmac_f32_16x16x64_bf16 v[100:103], v[16:19], a[8:15], %[idx]\n" \
  "v_max3_u32 v180, v180, v19, v19\n" \
  "s_waitcnt vmcnt(3)\n" \
  "ds_write_b128 v182, v[20:23] offset:512\n" \
  "v_smfmac_f32_16x16x64_bf16 v[96:99], v[20:23], a[16:23], %[idx]\n" \
  "v_max3_u32 v180, v20, v21, v180\n" \
  "v_smfmac_f32_16x16x64_bf16 v[100:103], v[20:23], a[24:31], %[idx]\n" \
  "v_max3_u32 v180, v180, v23, v22\n" \
  "s_waitcnt vmcnt(2)\n" \
  "ds_write_b128 v182, v[24:27] offset:1024\n" \
  "v_smfmac_f32_16x16x64_bf16 v[96:99], v[24:27], a[32:39], %[idx]\n" \
  "v_max3_u32 v180, v24, v25, v180\n" \
  "v_smfmac_f32_16x16x64_bf16 v[100:103], v[24:27], a[40:47], %[idx]\n" \
  "v_max3_u32 v180, v180, v27, v26\n" \
  "s_waitcnt vmcnt(1)\n" \
  "ds_write_b128 v182, v[28:31] offset:1536\n" \
  "v_max3_u32 v180, v28, v29, v180\n" \
  "v_max3_u32 v180, v180, v31, v30\n" \
  "s_waitcnt vmcnt(0)\n" \
  "ds_write_b128 v182, v[32:35] offset:2048\n" \
  "v_max3_u32 v180, v32, v33, v180\n" \
  "v_max3_u32 v180, v180, v35, v34\n" \
  "v_cmp_eq_u32 vcc, -1, v180\n" \
  "s_cbranch_vccnz Lbretry%=\n" \
  "Lbok%=:\n" \
  "s_waitcnt lgkmcnt(0)\n" \
  "s_barrier\n" \
  "ds_read_b128 v[36:39], v183 offset:0\n" \
  "ds_read_b128 v[40:43], v183 offset:512\n" \
  "ds_read_b128 v[44:47], v183 offset:1024\n" \
  "v_smfmac_f32_16x16x64_bf16 v[96:99], v[28:31], a[48:55], %[idx]\n" \
  "ds_read_b128 v[48:51], v183 offset:1536\n" \
  "ds_read_b128 v[52:55], v183 offset:2048\n" \
  "ds_read_b128 v[56:59], v184 offset:0\n" \
  "v_smfmac_f32_16x16x64_bf16 v[100:103], v[28:31], a[56:63], %[idx]\n" \
  "ds_read_b128 v[60:63], v184 offset:512\n" \
  "ds_read_b128 v[64:67], v184 offset:1024\n" \
  "ds_read_b128 v[68:71], v184 offset:1536\n" \
  "v_smfmac_f32_16x16x64_bf16 v[96:99], v[32:35], a[64:71], %[idx]\n" \
  "ds_read_b128 v[72:75], v184 offset:2048\n" \
  "ds_read_b128 v[76:79], v185 offset:0\n" \
  "ds_read_b128 v[80:83], v185 offset:512\n" \
  "v_smfmac_f32_16x16x64_bf16 v[100:103], v[32:35], a[72:79], %[idx]\n" \
  "ds_read_b128 v[84:87], v185 offset:1024\n" \
  "ds_read_b128 v[88:91], v185 offset:1536\n" \
  "ds_read_b128 v[92:95], v185 offset:2048\n" \
  "s_waitcnt lgkmcnt(14)\n" \
  "v_smfmac_f32_16x16x64_bf16 v[96:99], v[36:39], a[80:87], %[idx]\n" \
  "v_smfmac_f32_16x16x64_bf16 v[100:103], v[36:39], a[88:95], %[idx]\n" \
  "s_waitcnt lgkmcnt(13)\n" \
  "v_smfmac_f32_16x16x64_bf16 v[96:99], v[40:43], a[96:103], %[idx]\n" \
  "v_mov_b32 v190, s62\n" \
  "v_smfmac_f32_16x16x64_bf16 v[100:103], v[40:43], a[104:111], %[idx]\n" \
  "buffer_store_dword v190, %[fo1], s[40:43], s63 offen sc1\n" \
  "s_waitcnt lgkmcnt(12)\n" \
  "v_smfmac_f32_16x16x64_bf16 v[96:99], v[44:47], a[112:119], %[idx]\n" \
  "v_smfmac_f32_16x16x64_bf16 v[100:103], v[44:47], a[120:127], %[idx]\n" \
  "s_waitcnt lgkmcnt(11)\n" \
  "v_smfmac_f32_16x16x64_bf16 v[96:99], v[48:51], a[128:135], %[idx]\n" \
  "v_smfmac_f32_16x16x64_bf16 v[100:103], v[48:51], a[136:143], %[idx]\n" \
  "s_waitcnt lgkmcnt(10)\n" \
  "v_smfmac_f32_16x16x64_bf16 v[96:99], v[52:55], a[144:151], %[idx]\n" \
  "v_smfmac_f32_16x16x64_bf16 v[100:103], v[52:55], a[152:159], %[idx]\n" \
  "s_waitcnt lgkmcnt(9)\n" \
  "v_smfmac_f32_16x16x64_bf16 v[96:99], v[56:59], a[160:167], %[idx]\n" \
  "v_smfmac_f32_16x16x64_bf16 v[100:103], v[56:59], a[168:175], %[idx]\n" \
  "s_waitcnt lgkmcnt(8)\n" \
  "v_smfmac_f32_16x16x64_bf16 v[96:99], v[60:63], a[176:183], %[idx]\n" \
  "v_smfmac_f32_16x16x64_bf16 v[100:103], v[60:63], a[184:191], %[idx]\n" \
  "s_waitcnt lgkmcnt(7)\n" \
  "v_smfmac_f32_16x16x64_bf16 v[96:99], v[64:67], a[192:199], %[idx]\n" \
  "v_smfmac_f32_16x16x64_bf16 v[100:103], v[64:67], a[200:207], %[idx]\n" \
  "s_waitcnt lgkmcnt(6)\n" \
  "v_smfmac_f32_16x16x64_bf16 v[96:99], v[68:71], a[208:215], %[idx]\n" \
  "v_smfmac_f32_16x16x64_bf16 v[100:103], v[68:71], a[216:223], %[idx]\n" \
  "s_waitcnt lgkmcnt(5)\n" \
  "v_smfmac_f32_16x16x64_bf16 v[96:99], v[72:75], a[224:231], %[idx]\n" \
  "v_smfmac_f32_16x16x64_bf16 v[100:103], v[72:75], a[232:239], %[idx]\n" \
  "s_waitcnt lgkmcnt(4)\n" \
  "v_smfmac_f32_16x16x64_bf16 v[96:99], v[76:79], a[240:247], %[idx]\n" \
  "v_smfmac_f32_16x16x64_bf16 v[100:103], v[76:79], a[248:255], %[idx]\n" \
  "s_waitcnt lgkmcnt(3)\n" \
  "v_smfmac_f32_16x16x64_bf16 v[96:99], v[80:83], v[192:199], %[idx]\n" \
  "v_smfmac_f32_16x16x64_bf16 v[100:103], v[80:83], v[200:207], %[idx]\n" \
  "buffer_load_dwordx4 v[108:111], v188, s[40:43], s51 offen sc1\n" \
  "s_waitcnt lgkmcnt(2)\n" \
  "v_smfmac_f32_16x16x64_bf16 v[96:99], v[84:87], v[208:215], %[idx]\n" \
  "s_waitcnt lgkmcnt(1)\n" \
  "v_smfmac_f32_16x16x64_bf16 v[96:99], v[88:91], v[224:231], %[idx]\n" \
  "s_waitcnt lgkmcnt(0)\n" \
  "v_smfmac_f32_16x16x64_bf16 v[96:99], v[92:95], v[240:247], %[idx]\n" \
  "v_smfmac_f32_16x16x64_bf16 v[100:103], v[84:87], v[216:223], %[idx]\n" \
  "v_smfmac_f32_16x16x64_bf16 v[100:103], v[88:91], v[232:239], %[idx]\n" \
  "v_cvt_pk_bf16_f32 v104, v96, v97\n" \
  "v_cvt_pk_bf16_f32 v105, v98, v99\n" \
  "v_smfmac_f32_16x16x64_bf16 v[100:103], v[92:95], v[248:255], %[idx]\n" \
  "ds_write_b64 v186, v[104:105]\n" \
  "s_nop 6\n" \
  "v_cvt_pk_bf16_f32 v106, v100, v101\n" \
  "v_cvt_pk_bf16_f32 v107, v102, v103\n" \
  "ds_write_b64 v186, v[106:107] offset:512\n" \
  "s_cmp_lg_u32 %[w], 0\n" \
  "s_waitcnt lgkmcnt(0)\n" \
  "s_barrier\n" \
  "s_cbranch_scc1 Lbarch%=\n" \
  "s_cmp_eq_u32 s62, 63\n" \
  "s_cbranch_scc1 Lbend%=\n" \
  "s_lshl_b32 s69, s69, 25\n" \
  "Lbtc%=:\n" \
  "s_waitcnt vmcnt(0)\n" \
  "v_min3_u32 v180, v108, v109, v110\n" \
  "v_max3_u32 v108, v108, v109, v110\n" \
  "v_min_u32 v180, v180, v111\n" \
  "v_max_u32 v108, v108, v111\n" \
  "v_cmp_gt_u32 vcc, s69, v180\n" \
  "s_nop 0\n" \
  "v_max_u32_dpp v108, v108, v108 row_ror:8 row_mask:0xf bank_mask:0xf\n" \
  "s_nop 1\n" \
  "v_max_u32_dpp v108, v108, v108 row_ror:4 row_mask:0xf bank_mask:0xf\n" \
  "s_cbranch_vccnz Lbtr%=\n" \
  "Lbtk%=:\n" \
  "s_nop 0\n" \
  "v_max_u32_dpp v108, v108, v108 row_ror:2 row_mask:0xf bank_mask:0xf\n" \
  "s_nop 1\n" \
  "v_max_u32_dpp v108, v108, v108 row_ror:1 row_mask:0xf bank_mask:0xf\n" \
  "s_nop 0\n" \
  "v_readlane_b32 s52, v108, 0\n" \
  "v_readlane_b32 s55, v108, 16\n" \
  "s_nop 3\n" \
  "s_max_u32 s52, s52, s55\n" \
  "s_add_u32 s52, s52, %[d1]\n" \
  "Lbu1%=:\n" \
  "s_memrealtime s[88:89]\n" \
  "s_waitcnt lgkmcnt(0)\n" \
  "s_sub_u32 s55, s52, s88\n" \
  "s_and_b32 s55, s55, 0x1ffffff\n" \
  "s_sub_u32 s55, s55, 1\n" \
  "s_cmp_lt_u32 s55, 400\n" \
  "s_cbranch_scc1 Lbu1%=\n" \
  "buffer_load_dwordx4 v[16:19], %[pxo], s[40:43], s61 offen offset:0 sc1\n" \
  "buffer_load_dwordx4 v[20:23], %[pxo], s[40:43], s61 offen offset:1280 sc1\n" \
  "buffer_load_dwordx4 v[24:27], %[pxo], s[40:43], s61 offen offset:2560 sc1\n" \
  "buffer_load_dwordx4 v[28:31], %[pxo], s[40:43], s61 offen offset:3840 sc1\n" \
  "buffer_load_dwordx4 v[32:35], %[pmo], s[40:43], s61 offen sc1\n" \
  "buffer_load_dwordx4 v[36:39], %[pxo], s[40:43], s46 offen offset:0 sc1\n" \
  "buffer_load_dwordx4 v[40:43], %[pxo], s[40:43], s46 offen offset:1280 sc1\n" \
  "buffer_load_dwordx4 v[44:47], %[pxo], s[40:43], s46 offen offset:2560 sc1\n" \
  "buffer_load_dwordx4 v[48:51], %[pxo], s[40:43], s46 offen offset:3840 sc1\n" \
  "buffer_load_dwordx4 v[52:55], %[pmo], s[40:43], s46 offen sc1\n" \
  "buffer_load_dwordx4 v[56:59], %[pxo], s[40:43], s47 offen offset:0 sc1\n" \
  "buffer_load_dwordx4 v[60:63], %[pxo], s[40:43], s47 offen offset:1280 sc1\n" \
  "buffer_load_dwordx4 v[64:67], %[pxo], s[40:43], s47 offen offset:2560 sc1\n" \
  "buffer_load_dwordx4 v[68:71], %[pxo], s[40:43], s47 offen offset:3840 sc1\n" \
  "buffer_load_dwordx4 v[72:75], %[pmo], s[40:43], s47 offen sc1\n" \
  "buffer_load_dwordx4 v[76:79], %[pxo], s[40:43], s48 offen offset:0 sc1\n" \
  "buffer_load_dwordx4 v[80:83], %[pxo], s[40:43], s48 offen offset:1280 sc1\n" \
  "buffer_load_dwordx4 v[84:87], %[pxo], s[40:43], s48 offen offset:2560 sc1\n" \
  "buffer_load_dwordx4 v[88:91], %[pxo], s[40:43], s48 offen offset:3840 sc1\n" \
  "buffer_load_dwordx4 v[92:95], %[pmo], s[40:43], s48 offen sc1\n" \
  "s_barrier\n" \
  "ds_read_b128 v[96:99], v189 offset:0\n" \
  "ds_read_b128 v[100:103], v189 offset:1024\n" \
  "ds_read_b128 v[104:107], v189 offset:2048\n" \
  "ds_read_b128 v[108:111], v189 offset:3072\n" \
  "ds_read_b128 v[112:115], %[zmr] offset:10240\n" \
  "ds_read_b128 v[116:119], v189 offset:4096\n" \
  "ds_read_b128 v[120:123], v189 offset:5120\n" \
  "ds_read_b128 v[124:127], v189 offset:6144\n" \
  "ds_read_b128 v[128:131], v189 offset:7168\n" \
  "ds_read_b128 v[132:135], %[zmr] offset:11264\n" \
  "ds_read_b128 v[136:139], v189 offset:8192\n" \
  "ds_read_b128 v[140:143], v189 offset:9216\n" \
  "ds_read_b128 v[144:147], v189 offset:10240\n" \
  "ds_read_b128 v[148:151], v189 offset:11264\n" \
  "ds_read_b128 v[152:155], %[zmr] offset:12288\n" \
  "s_waitcnt lgkmcnt(14)\n" \
  "ds_read_b128 v[156:159], v189 offset:12288\n" \
  "s_waitcnt lgkmcnt(14)\n" \
  "ds_read_b128 v[160:163], v189 offset:13312\n" \
  "s_waitcnt lgkmcnt(14)\n" \
  "ds_read_b128 v[164:167], v189 offset:14336\n" \
  "s_waitcnt lgkmcnt(14)\n" \
  "ds_read_b128 v[168:171], v189 offset:15360\n" \
  "s_waitcnt lgkmcnt(14)\n" \
  "ds_read_b128 v[172:175], %[zmr] offset:13312\n" \
  "s_waitcnt vmcnt(19)\n" \
  "v_mfma_f32_16x16x32_bf16 v[176:179], v[96:99], v[16:19], 0\n" \
  "s_waitcnt vmcnt(18)\n" \
  "v_mfma_f32_16x16x32_bf16 v[176:179], v[100:103], v[20:23], v[176:179]\n" \
  "s_waitcnt vmcnt(17)\n" \
  "v_mfma_f32_16x16x32_bf16 v[176:179], v[104:107], v[24:27], v[176:179]\n" \
  "s_waitcnt vmcnt(16)\n" \
  "v_mfma_f32_16x16x32_bf16 v[176:179], v[108:111], v[28:31], v[176:179]\n" \
  "s_waitcnt vmcnt(15)\n" \
  "v_mfma_f32_16x16x32_bf16 v[176:179], v[112:115], v[32:35], v[176:179]\n" \
  "s_waitcnt vmcnt(14) lgkmcnt(14)\n" \
  "v_mfma_f32_16x16x32_bf16 v[96:99], v[116:119], v[36:39], 0\n" \
  "s_waitcnt vmcnt(13) lgkmcnt(13)\n" \
  "v_mfma_f32_16x16x32_bf16 v[96:99], v[120:123], v[40:43], v[96:99]\n" \
  "s_waitcnt vmcnt(12) lgkmcnt(12)\n" \
  "v_mfma_f32_16x16x32_bf16 v[96:99], v[124:127], v[44:47], v[96:99]\n" \
  "s_waitcnt vmcnt(11) lgkmcnt(11)\n" \
  "v_mfma_f32_16x16x32_bf16 v[96:99], v[128:131], v[48:51], v[96:99]\n" \
  "s_waitcnt vmcnt(10) lgkmcnt(10)\n" \
  "v_mfma_f32_16x16x32_bf16 v[96:99], v[132:135], v[52:55], v[96:99]\n" \
  "s_waitcnt vmcnt(9) lgkmcnt(9)\n" \
  "v_mfma_f32_16x16x32_bf16 v[116:119], v[136:139], v[56:59], 0\n" \
  "s_waitcnt vmcnt(8) lgkmcnt(8)\n" \
  "v_mfma_f32_16x16x32_bf16 v[116:119], v[140:143], v[60:63], v[116:119]\n" \
  "s_waitcnt vmcnt(7) lgkmcnt(7)\n" \
  "v_mfma_f32_16x16x32_bf16 v[116:119], v[144:147], v[64:67], v[116:119]\n" \
  "v_pk_add_f32 v[100:101], v[176:177], v[96:97]\n" \
  "v_pk_add_f32 v[102:103], v[178:179], v[98:99]\n" \
  "s_waitcnt vmcnt(6) lgkmcnt(6)\n" \
  "v_mfma_f32_16x16x32_bf16 v[116:119], v[148:151], v[68:71], v[116:119]\n" \
  "s_waitcnt vmcnt(5) lgkmcnt(5)\n" \
  "v_mfma_f32_16x16x32_bf16 v[116:119], v[152:155], v[72:75], v[116:119]\n" \
  "s_waitcnt vmcnt(4) lgkmcnt(4)\n" \
  "v_mfma_f32_16x16x32_bf16 v[136:139], v[156:159], v[76:79], 0\n" \
  "s_waitcnt vmcnt(3) lgkmcnt(3)\n" \
  "v_mfma_f32_16x16x32_bf16 v[136:139], v[160:163], v[80:83], v[136:139]\n" \
  "s_waitcnt vmcnt(2) lgkmcnt(2)\n" \
  "v_mfma_f32_16x16x32_bf16 v[136:139], v[164:167], v[84:87], v[136:139]\n" \
  "v_pk_add_f32 v[100:101], v[100:101], v[116:117]\n" \
  "v_pk_add_f32 v[102:103], v[102:103], v[118:119]\n" \
  "s_waitcnt vmcnt(1) lgkmcnt(1)\n" \
  "v_mfma_f32_16x16x32_bf16 v[136:139], v[168:171], v[88:91], v[136:139]\n" \
  "s_waitcnt vmcnt(0) lgkmcnt(0)\n" \
  "v_mfma_f32_16x16x32_bf16 v[136:139], v[172:175], v[92:95], v[136:139]\n" \
  "Lbsum%=:\n" \
  "s_nop 7\n" \
  "v_pk_add_f32 v[100:101], v[100:101], v[136:137]\n" \
  "v_pk_add_f32 v[102:103], v[102:103], v[138:139]\n" \
  "s_nop 1\n" \
  "v_permlane32_swap_b32 v100, v102\n" \
  "v_permlane32_swap_b32 v101, v103\n" \
  "s_nop 0\n" \
  "v_add_f32_dpp v104, v102, v100 row_ror:8 row_mask:0xf bank_mask:0x3\n" \
  "v_add_f32_dpp v104, v100, v102 row_ror:8 row_mask:0xf bank_mask:0xc\n" \
  "v_add_f32_dpp v105, v103, v101 row_ror:8 row_mask:0xf bank_mask:0x3\n" \
  "v_add_f32_dpp v105, v101, v103 row_ror:8 row_mask:0xf bank_mask:0xc\n" \
  "v_cmp_u_f32 vcc, v104, v105\n" \
  "v_cvt_pk_bf16_f32 v106, v104, v105\n" \
  "s_cbranch_vccnz Lbslow%=\n" \
  "Lbpok%=:\n" \
  "buffer_store_dword v106, %[mso], s[40:43], s64 offen sc1\n" \
  "buffer_store_dword v106, %[mso], s[40:43], s49 offen sc1\n" \
  "s_add_u32 s53, s52, %[d2]\n" \
  "s_branch Lbjoin%=\n" \
  "Lbarch%=:\n" \
  "ds_read_b128 v[16:19], v187 offset:10240\n" \
  "ds_read_b128 v[20:23], %[zmr] offset:10240\n" \
  "s_cmp_eq_u32 s62, 63\n" \
  "s_cbranch_scc1 Lblast%=\n" \
  "v_cmp_ge_u32 vcc, s69, v181\n" \
  "s_cbranch_vccnz Lbcnt%=\n" \
  "Lbgo%=:\n" \
  "s_mov_b32 m0, s45\n" \
  "s_cmp_lg_u32 %[w], 1\n" \
  "s_waitcnt lgkmcnt(0)\n" \
  "s_barrier\n" \
  "buffer_store_dwordx4 v[16:19], v187, s[40:43], s59 offen sc1\n" \
  "buffer_store_dwordx4 v[20:23], %[zmo2], s[40:43], s59 offen sc1\n" \
  "buffer_load_dwordx4 %[zxo], s[40:43], s58 offen offset:0 sc1 lds\n" \
  "buffer_load_dwordx4 %[zxo], s[40:43], s58 offen offset:1024 sc1 lds\n" \
  "buffer_load_dwordx4 %[zxo], s[40:43], s58 offen offset:2048 sc1 lds\n" \
  "buffer_load_dwordx4 %[zxo], s[40:43], s58 offen offset:3072 sc1 lds\n" \
  "s_cbranch_scc1 Lbjoin%=\n" \
  "s_sub_u32 m0, s45, 4096\n" \
  "s_sub_u32 s54, s58, 4096\n" \
  "buffer_load_dwordx4 %[zxo], s[40:43], s54 offen offset:0 sc1 lds\n" \
  "buffer_load_dwordx4 %[zxo], s[40:43], s54 offen offset:1024 sc1 lds\n" \
  "buffer_load_dwordx4 %[zxo], s[40:43], s54 offen offset:2048 sc1 lds\n" \
  "buffer_load_dwordx4 %[zxo], s[40:43], s54 offen offset:3072 sc1 lds\n" \
  "Lbjoin%=:\n" \
  "s_add_u32 s62, s62, 1\n" \
  "s_add_u32 s59, s59, 4096\n" \
  "v_xor_b32 v189, 0xc000, v189\n" \
  "Lbnext%=:\n" \
  "s_lshr_b32 s55, s62, 4\n" \
  "s_lshr_b32 s55, s65, s55\n" \
  "s_and_b32 s55, s55, 1\n" \
  "s_lshl_b32 s55, s55, 13\n" \
  "s_add_u32 s55, s55, s57\n" \
  "s_lshl_b32 s56, s62, 17\n" \
  "s_add_u32 s56, s56, s55\n" \
  "s_add_u32 s56, s56, 201326592\n" \
  "s_add_u32 s54, s62, 1\n" \
  "s_lshr_b32 s55, s54, 4\n" \
  "s_lshr_b32 s55, s65, s55\n" \
  "s_and_b32 s55, s55, 1\n" \
  "s_lshl_b32 s55, s55, 13\n" \
  "s_add_u32 s55, s55, %[mo]\n" \
  "s_lshl_b32 s64, s54, 17\n" \
  "s_add_u32 s64, s64, s55\n" \
  "s_add_u32 s64, s64, 201326592\n" \
  "s_add_u32 s49, s64, 65536\n" \
  "s_lshr_b32 s55, s62, 3\n" \
  "s_add_u32 s55, s55, 4\n" \
  "s_lshr_b32 s55, s65, s55\n" \
  "s_and_b32 s55, s55, 1\n" \
  "s_lshl_b32 s55, s55, 13\n" \
  "s_add_u32 s55, s55, s44\n" \
  "s_lshl_b32 s61, s62, 18\n" \
  "s_add_u32 s61, s61, s55\n" \
  "s_add_u32 s61, s61, 209715200\n" \
  "s_add_u32 s46, s61, 16384\n" \
  "s_add_u32 s47, s61, 32768\n" \
  "s_add_u32 s48, s61, 49152\n" \
  "s_add_u32 s58, s58, 16384\n" \
  "s_xor_b32 s45, s45, 0xc000\n" \
  "s_add_u32 s69, s62, 1\n" \
  "s_cmp_lg_u32 %[w], 0\n" \
  "s_cbranch_scc1 Lbgt%=\n" \
  "Lbu2%=:\n" \
  "s_memrealtime s[88:89]\n" \
  "s_waitcnt lgkmcnt(0)\n" \
  "s_sub_u32 s55, s53, s88\n" \
  "s_and_b32 s55, s55, 0x1ffffff\n" \
  "s_sub_u32 s55, s55, 1\n" \
  "s_cmp_lt_u32 s55, 400\n" \
  "s_cbranch_scc1 Lbu2%=\n" \
  "Lbgt%=:\n" \
  "s_barrier\n" \
  "s_branch Lbtop%=\n" \
  "Lbtr%=:\n" \
  "s_add_u32 s68, s68, 1\n" \
  "s_sub_u32 s66, s66, 1\n" \
  "s_cmp_gt_i32 s66, 0\n" \
  "s_cbranch_scc0 Lbtk%=\n" \
  "s_sleep 2\n" \
  "buffer_load_dwordx4 v[108:111], v188, s[40:43], s51 offen sc1\n" \
  "s_branch Lbtc%=\n" \
  "Lbretry%=:\n" \
  "s_add_u32 s67, s67, 1\n" \
  "s_sub_u32 s66, s66, 1\n" \
  "s_cmp_gt_i32 s66, 0\n" \
  "s_cbranch_scc1 Lbtop%=\n" \
  "s_branch Lbok%=\n" \
  "Lbcnt%=:\n" \
  "buffer_load_dword v181, %[co], s[40:43], s60 offen sc1\n" \
  "s_sub_u32 s66, s66, 1\n" \
  "s_waitcnt vmcnt(0)\n" \
  "v_cmp_ge_u32 vcc, s69, v181\n" \
  "s_cbranch_vccz Lbgo%=\n" \
  "s_cmp_gt_i32 s66, 0\n" \
  "s_cbranch_scc1 Lbcnt%=\n" \
  "s_branch Lbgo%=\n" \
  "Lbslow%=:\n" \
  "s_mov_b32 s50, 0\n" \
  "s_add_u32 s68, s68, 1\n" \
  "s_sub_u32 s66, s66, 1\n" \
  "s_cmp_gt_i32 s66, 0\n" \
  "s_cbranch_scc0 Lbpok%=\n" \
  "v_cmp_u_f32 vcc, v176, v176\n" \
  "s_cbranch_vccz Lbs0%=\n" \
  "v_max_u32 v180, v16, v17\n" \
  "v_max3_u32 v180, v180, v18, v19\n" \
  "v_max3_u32 v180, v180, v20, v21\n" \
  "v_max3_u32 v180, v180, v22, v23\n" \
  "v_max3_u32 v180, v180, v24, v25\n" \
  "v_max3_u32 v180, v180, v26, v27\n" \
  "v_max3_u32 v180, v180, v28, v29\n" \
  "v_max3_u32 v180, v180, v30, v31\n" \
  "v_max3_u32 v180, v180, v32, v33\n" \
  "v_max3_u32 v180, v180, v34, v35\n" \
  "v_cmp_eq_u32 vcc, -1, v180\n" \
  "s_cbranch_vccz Lbs0%=\n" \
  "s_or_b32 s50, s50, 1\n" \
  "buffer_load_dwordx4 v[16:19], %[pxo], s[40:43], s61 offen offset:0 sc1\n" \
  "buffer_load_dwordx4 v[20:23], %[pxo], s[40:43], s61 offen offset:1280 sc1\n" \
  "buffer_load_dwordx4 v[24:27], %[pxo], s[40:43], s61 offen offset:2560 sc1\n" \
  "buffer_load_dwordx4 v[28:31], %[pxo], s[40:43], s61 offen offset:3840 sc1\n" \
  "buffer_load_dwordx4 v[32:35], %[pmo], s[40:43], s61 offen sc1\n" \
  "Lbs0%=:\n" \
  "v_cmp_u_f32 vcc, v96, v96\n" \
  "s_cbranch_vccz Lbs1%=\n" \
  "v_max_u32 v180, v36, v37\n" \
  "v_max3_u32 v180, v180, v38, v39\n" \
  "v_max3_u32 v180, v180, v40, v41\n" \
  "v_max3_u32 v180, v180, v42, v43\n" \
  "v_max3_u32 v180, v180, v44, v45\n" \
  "v_max3_u32 v180, v180, v46, v47\n" \
  "v_max3_u32 v180, v180, v48, v49\n" \
  "v_max3_u32 v180, v180, v50, v51\n" \
  "v_max3_u32 v180, v180, v52, v53\n" \
  "v_max3_u32 v180, v180, v54, v55\n" \
  "v_cmp_eq_u32 vcc, -1, v180\n" \
  "s_cbranch_vccz Lbs1%=\n" \
  "s_or_b32 s50, s50, 2\n" \
  "buffer_load_dwordx4 v[36:39], %[pxo], s[40:43], s46 offen offset:0 sc1\n" \
  "buffer_load_dwordx4 v[40:43], %[pxo], s[40:43], s46 offen offset:1280 sc1\n" \
  "buffer_load_dwordx4 v[44:47], %[pxo], s[40:43], s46 offen offset:2560 sc1\n" \
  "buffer_load_dwordx4 v[48:51], %[pxo], s[40:43], s46 offen offset:3840 sc1\n" \
  "buffer_load_dwordx4 v[52:55], %[pmo], s[40:43], s46 offen sc1\n" \
  "Lbs1%=:\n" \
  "v_cmp_u_f32 vcc, v116, v116\n" \
  "s_cbranch_vccz Lbs2%=\n" \
  "v_max_u32 v180, v56, v57\n" \
  "v_max3_u32 v180, v180, v58, v59\n" \
  "v_max3_u32 v180, v180, v60, v61\n" \
  "v_max3_u32 v180, v180, v62, v63\n" \
  "v_max3_u32 v180, v180, v64, v65\n" \
  "v_max3_u32 v180, v180, v66, v67\n" \
  "v_max3_u32 v180, v180, v68, v69\n" \
  "v_max3_u32 v180, v180, v70, v71\n" \
  "v_max3_u32 v180, v180, v72, v73\n" \
  "v_max3_u32 v180, v180, v74, v75\n" \
  "v_cmp_eq_u32 vcc, -1, v180\n" \
  "s_cbranch_vccz Lbs2%=\n" \
  "s_or_b32 s50, s50, 4\n" \
  "buffer_load_dwordx4 v[56:59], %[pxo], s[40:43], s47 offen offset:0 sc1\n" \
  "buffer_load_dwordx4 v[60:63], %[pxo], s[40:43], s47 offen offset:1280 sc1\n" \
  "buffer_load_dwordx4 v[64:67], %[pxo], s[40:43], s47 offen offset:2560 sc1\n" \
  "buffer_load_dwordx4 v[68:71], %[pxo], s[40:43], s47 offen offset:3840 sc1\n" \
  "buffer_load_dwordx4 v[72:75], %[pmo], s[40:43], s47 offen sc1\n" \
  "Lbs2%=:\n" \
  "v_cmp_u_f32 vcc, v136, v136\n" \
  "s_cbranch_vccz Lbs3%=\n" \
  "v_max_u32 v180, v76, v77\n" \
  "v_max3_u32 v180, v180, v78, v79\n" \
  "v_max3_u32 v180, v180, v80, v81\n" \
  "v_max3_u32 v180, v180, v82, v83\n" \
  "v_max3_u32 v180, v180, v84, v85\n" \
  "v_max3_u32 v180, v180, v86, v87\n" \
  "v_max3_u32 v180, v180, v88, v89\n" \
  "v_max3_u32 v180, v180, v90, v91\n" \
  "v_max3_u32 v180, v180, v92, v93\n" \
  "v_max3_u32 v180, v180, v94, v95\n" \
  "v_cmp_eq_u32 vcc, -1, v180\n" \
  "s_cbranch_vccz Lbs3%=\n" \
  "s_or_b32 s50, s50, 8\n" \
  "buffer_load_dwordx4 v[76:79], %[pxo], s[40:43], s48 offen offset:0 sc1\n" \
  "buffer_load_dwordx4 v[80:83], %[pxo], s[40:43], s48 offen offset:1280 sc1\n" \
  "buffer_load_dwordx4 v[84:87], %[pxo], s[40:43], s48 offen offset:2560 sc1\n" \
  "buffer_load_dwordx4 v[88:91], %[pxo], s[40:43], s48 offen offset:3840 sc1\n" \
  "buffer_load_dwordx4 v[92:95], %[pmo], s[40:43], s48 offen sc1\n" \
  "Lbs3%=:\n" \
  "s_cmp_eq_u32 s50, 0\n" \
  "s_cbranch_scc1 Lbpok%=\n" \
  "s_bitcmp1_b32 s50, 0\n" \
  "s_cbranch_scc0 Lbt0%=\n" \
  "s_nop 7\n" \
  "ds_read_b128 v[156:159], v189 offset:0\n" \
  "ds_read_b128 v[160:163], v189 offset:1024\n" \
  "ds_read_b128 v[164:167], v189 offset:2048\n" \
  "ds_read_b128 v[168:171], v189 offset:3072\n" \
  "ds_read_b128 v[172:175], %[zmr] offset:10240\n" \
  "s_waitcnt vmcnt(0) lgkmcnt(0)\n" \
  "v_mfma_f32_16x16x32_bf16 v[176:179], v[156:159], v[16:19], 0\n" \
  "v_mfma_f32_16x16x32_bf16 v[176:179], v[160:163], v[20:23], v[176:179]\n" \
  "v_mfma_f32_16x16x32_bf16 v[176:179], v[164:167], v[24:27], v[176:179]\n" \
  "v_mfma_f32_16x16x32_bf16 v[176:179], v[168:171], v[28:31], v[176:179]\n" \
  "v_mfma_f32_16x16x32_bf16 v[176:179], v[172:175], v[32:35], v[176:179]\n" \
  "Lbt0%=:\n" \
  "s_bitcmp1_b32 s50, 1\n" \
  "s_cbranch_scc0 Lbt1%=\n" \
  "s_nop 7\n" \
  "ds_read_b128 v[156:159], v189 offset:4096\n" \
  "ds_read_b128 v[160:163], v189 offset:5120\n" \
  "ds_read_b128 v[164:167], v189 offset:6144\n" \
  "ds_read_b128 v[168:171], v189 offset:7168\n" \
  "ds_read_b128 v[172:175], %[zmr] offset:11264\n" \
  "s_waitcnt vmcnt(0) lgkmcnt(0)\n" \
  "v_mfma_f32_16x16x32_bf16 v[96:99], v[156:159], v[36:39], 0\n" \
  "v_mfma_f32_16x16x32_bf16 v[96:99], v[160:163], v[40:43], v[96:99]\n" \
  "v_mfma_f32_16x16x32_bf16 v[96:99], v[164:167], v[44:47], v[96:99]\n" \
  "v_mfma_f32_16x16x32_bf16 v[96:99], v[168:171], v[48:51], v[96:99]\n" \
  "v_mfma_f32_16x16x32_bf16 v[96:99], v[172:175], v[52:55], v[96:99]\n" \
  "Lbt1%=:\n" \
  "s_bitcmp1_b32 s50, 2\n" \
  "s_cbranch_scc0 Lbt2%=\n" \
  "s_nop 7\n" \
  "ds_read_b128 v[156:159], v189 offset:8192\n" \
  "ds_read_b128 v[160:163], v189 offset:9216\n" \
  "ds_read_b128 v[164:167], v189 offset:10240\n" \
  "ds_read_b128 v[168:171], v189 offset:11264\n" \
  "ds_read_b128 v[172:175], %[zmr] offset:12288\n" \
  "s_waitcnt vmcnt(0) lgkmcnt(0)\n" \
  "v_mfma_f32_16x16x32_bf16 v[116:119], v[156:159], v[56:59], 0\n" \
  "v_mfma_f32_16x16x32_bf16 v[116:119], v[160:163], v[60:63], v[116:119]\n" \
  "v_mfma_f32_16x16x32_bf16 v[116:119], v[164:167], v[64:67], v[116:119]\n" \
  "v_mfma_f32_16x16x32_bf16 v[116:119], v[168:171], v[68:71], v[116:119]\n" \
  "v_mfma_f32_16x16x32_bf16 v[116:119], v[172:175], v[72:75], v[116:119]\n" \
  "Lbt2%=:\n" \
  "s_bitcmp1_b32 s50, 3\n" \
  "s_cbranch_scc0 Lbt3%=\n" \
  "s_nop 7\n" \
  "ds_read_b128 v[156:159], v189 offset:12288\n" \
  "ds_read_b128 v[160:163], v189 offset:13312\n" \
  "ds_read_b128 v[164:167], v189 offset:14336\n" \
  "ds_read_b128 v[168:171], v189 offset:15360\n" \
  "ds_read_b128 v[172:175], %[zmr] offset:13312\n" \
  "s_waitcnt vmcnt(0) lgkmcnt(0)\n" \
  "v_mfma_f32_16x16x32_bf16 v[136:139], v[156:159], v[76:79], 0\n" \
  "v_mfma_f32_16x16x32_bf16 v[136:139], v[160:163], v[80:83], v[136:139]\n" \
  "v_mfma_f32_16x16x32_bf16 v[136:139], v[164:167], v[84:87], v[136:139]\n" \
  "v_mfma_f32_16x16x32_bf16 v[136:139], v[168:171], v[88:91], v[136:139]\n" \
  "v_mfma_f32_16x16x32_bf16 v[136:139], v[172:175], v[92:95], v[136:139]\n" \
  "Lbt3%=:\n" \
  "s_nop 7\n" \
  "s_nop 7\n" \
  "v_pk_add_f32 v[100:101], v[176:177], v[96:97]\n" \
  "v_pk_add_f32 v[102:103], v[178:179], v[98:99]\n" \
  "v_pk_add_f32 v[100:101], v[100:101], v[116:117]\n" \
  "v_pk_add_f32 v[102:103], v[102:103], v[118:119]\n" \
  "s_branch Lbsum%=\n" \
  "Lblast%=:\n" \
  "s_waitcnt lgkmcnt(1)\n" \
  "buffer_store_dwordx4 v[16:19], v187, s[40:43], s59 offen sc1\n" \
  "s_waitcnt lgkmcnt(0)\n" \
  "buffer_store_dwordx4 v[20:23], %[zmo2], s[40:43], s59 offen sc1\n" \
  "v_mov_b32 v190, s69\n" \
  "s_waitcnt vmcnt(0)\n" \
  "buffer_store_dword v190, %[fo], s[40:43], s63 offen sc1\n" \
  "Lbend%=:\n" \
  "s_waitcnt vmcnt(0) lgkmcnt(0)\n"

struct ProArgs { const us16* x; const us16* mem0; const us16* Wv; const us16* Wo; char* wb; const us16* Wq; const us16* Wk; };
#define NPOIS (63 * 48)
#define NPW 128
#define WT 3
#define PST(v, o) __builtin_amdgcn_raw_buffer_store_b128(v, rs, o, 0, (WT & 1) ? SC1 : 0)
#define UST(v, o) __builtin_amdgcn_raw_buffer_store_b128(v, rs, o, 0, (WT & 2) ? SC1 : 0)
#define PF_W 5

__global__ void __launch_bounds__(256) k_pro(ProArgs p) {
  extern __shared__ __attribute__((aligned(16))) char smem[];
  LAS char* lds = (LAS char*)smem;
  const int b = blockIdx.x, tid = threadIdx.x, lane = tid & 63, w = __builtin_amdgcn_readfirstlane(tid >> 6), g = lane >> 4, t = lane & 15;
  p.wb = page_up(p.wb);
  const rsrc_t rs = __builtin_amdgcn_make_buffer_rsrc(p.wb, 0, 0x0ff00000, 0x00020000);
  if (b < 256) {
    const int h = (b & 7) * 4 + ((b >> 3) & 3), dh = (b >> 5) & 1, cq = b >> 6;
    for (int i = w; i < 40; i += 4) {
      const int tg = i >> 3, r = (i & 7) * 16 + (lane >> 2);
      glds(p.Wo + (size_t)(h * 128 + r) * DM + cq * 160 + tg * 32 + (lane & 3) * 8, i * 1024);
    }
    u32x4 a[5][4];
#pragma unroll
    for (int d = 0; d < 5; ++d)
#pragma unroll
      for (int k = 0; k < 4; ++k) a[d][k] = *(const u32x4*)(p.Wv + (size_t)(dh * 320 + (w * 5 + d) * 16 + t) * 4096 + h * 128 + k * 32 + g * 8);
    wait_vm<0>();
    bar();
    LAS char* rp = lds + (8 * g + (t >> 2)) * 64 + (t & 3) * 16;
    const unsigned ub = OFF_UW + (h >> 3) * 81920 + ((h >> 2) & 1) * 2048 + g * 256 + (h & 3) * 64 + (t & 3) * 16 + (t >> 2) * 327680;
#pragma unroll 1
    for (int tg = 0; tg < 5; ++tg) {
      u32x4 bx[4], by[4];
#pragma unroll
      for (int k = 0; k < 4; ++k) {
        u32x2 x0 = trd(rp + tg * 8192 + k * 2048), x1 = trd(rp + tg * 8192 + k * 2048 + 256), y0 = trd(rp + tg * 8192 + k * 2048 + 8), y1 = trd(rp + tg * 8192 + k * 2048 + 264);
        bx[k] = (u32x4){x0.x, x0.y, x1.x, x1.y}; by[k] = (u32x4){y0.x, y0.y, y1.x, y1.y};
      }
#pragma unroll
      for (int d = 0; d < 5; ++d) {
        f32x4 X = {0.f, 0.f, 0.f, 0.f}, Y = X;
#pragma unroll
        for (int k = 0; k < 4; ++k) { X = mma32(a[d][k], bx[k], X); Y = mma32(a[d][k], by[k], Y); }
        const int dt = dh * 20 + w * 5 + d;
        UST(((u32x4){pk(X[0], X[1]), pk(Y[0], Y[1]), pk(X[2], X[3]), pk(Y[2], Y[3])}), ub + (cq * 5 + tg) * 4 * 327680 + (dt >> 1) * 4096 + (dt & 1) * 1024);
      }
    }
    return;
  }
  if (b < 384) {
    LAS float* red = (LAS float*)lds;
    const int r0 = (b - 256) * 16;
    u32x4 v[5]; float s = 0.f;
    for (int i = 0; i < 5; ++i) {
      v[i] = *(const u32x4*)(p.x + (size_t)(r0 + t) * DM + (5 * w + i) * 32 + 8 * g);
      for (int k = 0; k < 4; ++k) s += bflo(v[i][k]) + bfhi(v[i][k]);
    }
    s = sum32(sum16(s));
    if (g == 0) red[w * 16 + t] = s;
    bar();
    const float mean = (red[t] + red[16 + t] + red[32 + t] + red[48 + t]) * (1.f / DM);
    float q = 0.f;
    for (int i = 0; i < 5; ++i) for (int k = 0; k < 4; ++k) { float a = bflo(v[i][k]) - mean, c = bfhi(v[i][k]) - mean; q += a * a + c * c; }
    q = sum32(sum16(q));
    if (g == 0) red[64 + w * 16 + t] = q;
    bar();
    const float rstd = 1.f / sqrtf((red[64 + t] + red[80 + t] + red[96 + t] + red[112 + t]) * (1.f / DM) + 1e-5f);
    for (int i = 0; i < 5; ++i) {
      u32x4 o; for (int k = 0; k < 4; ++k) o[k] = pk((bflo(v[i][k]) - mean) * rstd, (bfhi(v[i][k]) - mean) * rstd);
      PST(o, OFF_XN + (((b - 256) * 20 + 5 * w + i) * 64 + lane) * 16);
    }
    return;
  }
  if (b >= 393) {
    {
      u32x4 pf[2560 / NPW];
#pragma unroll
      for (int k = 0; k < 2560 / NPW; ++k) pf[k] = *(const u32x4*)((const char*)(k & 1 ? p.Wq : p.Wk) + ((b - 393) * (1280 / NPW) + (k >> 1)) * 4096 + tid * 16);
#pragma unroll
      for (int k = 0; k < 2560 / NPW; ++k) asm volatile("" :: "v"(pf[k]));
    }
    const u32x4 po = {POISON, POISON, POISON, POISON};
    for (int u = b - 393; u < NPOIS; u += NPW) {
      const unsigned o = u < 1008 ? OFF_M + 131072u + (unsigned)u * 8192u : OFF_P + (unsigned)(u - 1008) * 8192u;
      PST(po, o + tid * 16);
      if (tid < 64) PST(po, o + 4096 + tid * 16);
    }
    return;
  }
  if (b == 384) {
    for (int i = tid; i < 640; i += 256) {
      const int ks = i >> 5, gg = (i >> 3) & 3, row = i & 7;
      u32x4 v = *(const u32x4*)(p.mem0 + row * DM + ks * 32 + gg * 8);
      for (int k = 0; k < 4; ++k) v[k] = min(v[k], POISON - 1);
      const unsigned o = (ks < 10 ? ks * 512 : 16384 + (ks - 10) * 512) + gg * 128 + row * 16;
      for (int c = 0; c < 8; ++c) *(u32x4*)(p.wb + OFF_M + o + (c >> 1) * 32768 + (c & 1) * 8192) = v;
    }
  } else {
    for (int i = (b - 385) * 256 + tid; i < (int)F_END / 16; i += 8 * 256) *(u32x4*)(p.wb + OFF_FLAG + i * 16) = (u32x4){0u, 0u, 0u, 0u};
    const int i = (b - 385) * 256 + tid;
    for (int k = 0; k < 4; ++k) *(u32x4*)(p.wb + OFF_M + (k + 1) * 131072 + (i >> 7) * 8192 + 5120 + (i & 127) * 16) = (u32x4){0u, 0u, 0u, 0u};
  }
}

DEVI void role_t1(rsrc_t rs, int h) {
  const int lane = lane_id(), g = lane >> 4, n = lane & 15;
  const unsigned l16 = lane * 16, kmo = g * 128 + (n & 7) * 16;
  const unsigned fq = F_QK + (h * 4 + (lane & 3)) * 128, fa = F_A + (h * 4 + max(lane & 3, 1)) * 128;
  const unsigned pmo = g < 2 ? (32 + (h >> 2)) * 1024 + (h & 3) * 256 + n * 16 + g * 8 : OOB;
  const f32x4 z4 = {0.f, 0.f, 0.f, 0.f};
  const float cs = 0.08838834764831845f * 1.4426950408889634f;
  const float NEG = -__builtin_inff();
  int budget = SPIN_MAX;
  unsigned cq = 0, ca = 0;
  u32x4 qa[2][4], ka[2][4], qb[2][4], kb[2][4];
  auto load_qk = [&](int s, u32x4 (&q)[2][4], u32x4 (&k)[2][4]) {
    const unsigned so = OFF_Q + (h * 64 + s) * 8192;
#pragma unroll
    for (int ks = 0; ks < 4; ++ks)
#pragma unroll
      for (int T = 0; T < 2; ++T) { q[T][ks] = ld16(rs, l16 + (T * 4 + ks) * 1024, so); k[T][ks] = ld16(rs, l16 + (T * 4 + ks) * 1024, so + 16 * MB); }
  };
  auto step = [&](int s, u32x4 (&q)[2][4], u32x4 (&k)[2][4]) {
    f32x4 s0[2] = {z4, z4}, s1 = z4, sm[2] = {z4, z4};
#pragma unroll
    for (int ks = 0; ks < 4; ++ks) { s0[0] = mma32(k[0][ks], q[0][ks], s0[0]); s0[1] = mma32(k[0][ks], q[1][ks], s0[1]); s1 = mma32(k[1][ks], q[1][ks], s1); }
    fence();
    pollc(rs, fa, s + 1, ca, budget);
    u32x4 km[4];
#pragma unroll
    for (int ks = 0; ks < 4; ++ks) km[ks] = ld16(rs, kmo + ks * 512, OFF_KM + (h * 64 + s) * 2048);
#pragma unroll
    for (int ks = 0; ks < 4; ++ks) { sm[0] = mma32(km[ks], q[0][ks], sm[0]); sm[1] = mma32(km[ks], q[1][ks], sm[1]); }
    pollc(rs, fq, min(s + 3, NSEG), cq, budget);
#pragma unroll
    for (int qt = 0; qt < 2; ++qt) {
      f32x4 a = s0[qt], c = s1, m = sm[qt];
      float mx = NEG;
#pragma unroll
      for (int r = 0; r < 4; ++r) {
        if (g >= 2) m[r] = NEG;
        const bool vis = 4 * g + r <= n;
        if (qt == 0) { if (!vis) a[r] = NEG; c[r] = NEG; } else if (!vis) c[r] = NEG;
        mx = fmaxf(mx, fmaxf(m[r], fmaxf(a[r], c[r])));
      }
      mx = max32(max16(mx));
      const float mc = -mx * cs; float l = 0.f;
#pragma unroll
      for (int r = 0; r < 4; ++r) {
        m[r] = __builtin_amdgcn_exp2f(fmaf(m[r], cs, mc)); a[r] = __builtin_amdgcn_exp2f(fmaf(a[r], cs, mc)); c[r] = qt ? __builtin_amdgcn_exp2f(fmaf(c[r], cs, mc)) : 0.f;
        l += m[r] + a[r] + c[r];
      }
      l = sum32(sum16(l));
      const float il = __builtin_amdgcn_rcpf(l);
      const unsigned so = OFF_PT + (s * 2 + qt) * 40960;
      st16(pk8(a * il, c * il), rs, l16 + h * 1024, so);
      __builtin_amdgcn_raw_buffer_store_b64(pk4(m * il), rs, pmo, so, SC1);
    }
    fence();
    load_qk(min(s + 2, NSEG - 1), q, k);
    fence();
    wait_vm<16>();
    __builtin_amdgcn_raw_buffer_store_b32(s + 1, rs, lane == 0 ? F_T1 + h * 128 : OOB, OFF_FLAG, SC1);
  };
  pollc(rs, fq, 1, cq, budget);
  load_qk(0, qa, ka);
  pollc(rs, fq, 2, cq, budget);
  load_qk(1, qb, kb);
  for (int s = 0; s < NSEG; s += 2) { step(s, qa, ka); step(s + 1, qb, kb); }
}

DEVI void role_t2(LAS char* lds, rsrc_t rs, const us16* x, int cg, int s0, int w) {
  const int lane = lane_id(), g = lane >> 4, n = lane & 15;
  const unsigned l16 = lane * 16;
  const unsigned zo = (n >> 3) * MB + g * 128 + (n & 7) * 16 + w * 4096, zmo = (n >> 3) * 262144 + g * 128 + (n & 7) * 16 + w * 1024;
  const unsigned fz = F_VZ + ((cg >> 1) * 32 + 8 * w + (lane & 7)) * 128;
  const unsigned fp = lane < 8 ? F_T1 + (8 * w + lane) * 128 : F_B + ((cg * 10 + min(lane - 8, 9)) * 4 + max(w, 1)) * 128;
  int budget = SPIN_MAX, par = 0;
  u32x4 z[8][5];
  auto load_z = [&](int s) {
    poll(rs, fz, s + 1, budget);
    const unsigned so = OFF_Z + cg * 10 * MB + s * 16384;
#pragma unroll
    for (int mt = 0; mt < 5; ++mt)
#pragma unroll
      for (int i = 0; i < 8; ++i) z[i][mt] = ld16(rs, zo + i * 512, so + mt * 2 * MB);
  };
  load_z(s0);
  for (int s = s0; s < NSEG; s += 4, par ^= 1) {
    u32x2 xr[5];
    const us16* xp = x + (size_t)(s * 32 + (w & 1) * 16 + n) * DM + cg * 80 + 4 * g;
#pragma unroll
    for (int mt = 0; mt < 5; ++mt) xr[mt] = *(const u32x2*)(xp + mt * 16);
    poll(rs, fp, s + 1, budget);
    u32x4 pt[10][2], zm[2][5];
#pragma unroll
    for (int q = 0; q < 2; ++q)
#pragma unroll
      for (int mt = 0; mt < 5; ++mt) zm[q][mt] = ld16(rs, zmo + q * 512, OFF_ZM + ((cg * 10 + mt * 2) * 64 + s) * 4096);
#pragma unroll
    for (int i = 0; i < 10; ++i)
#pragma unroll
      for (int T = 0; T < 2; ++T) pt[i][T] = ld16(rs, l16 + (i < 2 ? 32 + i : i - 2) * 1024, OFF_PT + (s * 2 + T) * 40960 + (i < 2 ? 2 : 8) * w * 1024);
    fence();
    f32x4 acc[5][2];
#pragma unroll
    for (int mt = 0; mt < 5; ++mt) { acc[mt][0] = (f32x4){0.f, 0.f, 0.f, 0.f}; acc[mt][1] = acc[mt][0]; }
#pragma unroll
    for (int i = 0; i < 10; ++i)
#pragma unroll
      for (int mt = 0; mt < 5; ++mt)
#pragma unroll
        for (int T = 0; T < 2; ++T) acc[mt][T] = mma32(i < 2 ? zm[i][mt] : z[i - 2][mt], pt[i][T], acc[mt][T]);
    LAS char* rb = lds + par * 40960 + l16;
#pragma unroll
    for (int mt = 0; mt < 5; ++mt)
#pragma unroll
      for (int T = 0; T < 2; ++T) *(LAS f32x4*)(rb + (w * 10 + T * 5 + mt) * 1024) = acc[mt][T];
    bar();
    if (w < 2) {
      u32x2 hv[5]; float sum = 0.f;
#pragma unroll
      for (int mt = 0; mt < 5; ++mt) {
        LAS char* q = rb + (w * 5 + mt) * 1024;
        const u32x2 at = pk4((*(LAS f32x4*)q + *(LAS f32x4*)(q + 10240)) + (*(LAS f32x4*)(q + 20480) + *(LAS f32x4*)(q + 30720)));
        hv[mt].x = pk(bflo(xr[mt].x) + bflo(at.x), bfhi(xr[mt].x) + bfhi(at.x)); hv[mt].y = pk(bflo(xr[mt].y) + bflo(at.y), bfhi(xr[mt].y) + bfhi(at.y));
        sum += (bflo(hv[mt].x) + bfhi(hv[mt].x)) + (bflo(hv[mt].y) + bfhi(hv[mt].y));
      }
      const unsigned ho = OFF_H + ((s * 2 + w) * 20 + (cg * 5 >> 1)) * 1024 + l16;
      if (cg & 1) {
        __builtin_amdgcn_raw_buffer_store_b64(hv[0], rs, ho + 8, 0, SC1);
        st16((u32x4){hv[1].x, hv[1].y, hv[2].x, hv[2].y}, rs, ho, 1024); st16((u32x4){hv[3].x, hv[3].y, hv[4].x, hv[4].y}, rs, ho, 2048);
      } else {
        st16((u32x4){hv[0].x, hv[0].y, hv[1].x, hv[1].y}, rs, ho, 0); st16((u32x4){hv[2].x, hv[2].y, hv[3].x, hv[3].y}, rs, ho, 1024);
        __builtin_amdgcn_raw_buffer_store_b64(hv[4], rs, ho + 2048, 0, SC1);
      }
      const float mean = sum32(sum16(sum)) * (1.f / 80.f);
      float q2 = 0.f;
#pragma unroll
      for (int mt = 0; mt < 5; ++mt) {
        const float a = bflo(hv[mt].x) - mean, b = bfhi(hv[mt].x) - mean, c = bflo(hv[mt].y) - mean, d = bfhi(hv[mt].y) - mean;
        q2 += (a * a + b * b) + (c * c + d * d);
      }
      q2 = sum32(sum16(q2));
      __builtin_amdgcn_raw_buffer_store_b64((u32x2){__float_as_uint(mean), __float_as_uint(q2)}, rs, g == 0 ? n * 8 : OOB, OFF_ST + ((s * 2 + w) * 8 + cg) * 128, SC1);
      publish(rs, lane == 0 ? F_H + (w * 8 + cg) * 256 + s * 4 : OOB, 1);
    }
    if (s + 4 < NSEG) load_z(s + 4);
  }
}

DEVI void role_f1(LAS char* lds, rsrc_t rs, const char* wb, const us16* w1, int f, int w) {
  const int lane = lane_id(), g = lane >> 4, n = lane & 15;
  const unsigned l16 = lane * 16;
  const int kb = f * 4 + w, kbc = min(kb, 53);
  u32x4 wf[20][2];
  {
    LAS char* rp = lds + w * 20480 + (4 * g + (n >> 2)) * 64 + (n & 3) * 8;
#pragma unroll
    for (int hf = 0; hf < 2; ++hf) {
#pragma unroll
      for (int i = 0; i < 20; ++i) glds(w1 + (size_t)(hf * 320 + i * 16 + (lane >> 2)) * DFF + kbc * 32 + (lane & 3) * 8, w * 20480 + i * 1024);
      wait_vm<0>();
#pragma unroll
      for (int ks = 0; ks < 10; ++ks)
#pragma unroll
        for (int t = 0; t < 2; ++t) { u32x2 lo = trd(rp + ks * 2048 + t * 32), hi = trd(rp + ks * 2048 + t * 32 + 1024); wf[hf * 10 + ks][t] = (u32x4){lo.x, lo.y, hi.x, hi.y}; }
      asm volatile("s_waitcnt lgkmcnt(0)" ::: "memory");
    }
  }
  f32x4 cs[2] = {{0.f, 0.f, 0.f, 0.f}, {0.f, 0.f, 0.f, 0.f}};
  {
    const u32x4 ones = {0x3f803f80u, 0x3f803f80u, 0x3f803f80u, 0x3f803f80u};
#pragma unroll
    for (int ks = 0; ks < 20; ++ks) { cs[0] = mma32(wf[ks][0], ones, cs[0]); cs[1] = mma32(wf[ks][1], ones, cs[1]); }
  }
  bar();
  const unsigned fh = F_H + n * 256;
  const char* hsrc = wb + OFF_H + w * 10240 + l16;
  const char* ssrc = wb + OFF_ST + (w & 1) * 1024 + l16;
  int budget = SPIN_MAX, issued = 0, slot = 0, islot = 0, rdy = 0, nb = 0, done = 0, k1 = 0;
  bool dfr = false;
  unsigned n0 = 0, n1 = 0;
  auto consume = [&]() {
    asm volatile("" : "+v"(n0), "+v"(n1) :: "memory");
    if (__all(n0 != 0)) rdy = max(rdy, __all(n1 != 0) ? nb + 2 : nb + 1);
  };
  const unsigned fo = lane == 0 ? F_F1 + kb * 128 : OOB;
  auto issue = [&]() {
#pragma unroll
    for (int i = 0; i < 10; ++i) glds_sc1(hsrc + (size_t)issued * 40960 + i * 1024, islot + w * 10240 + i * 1024);
    glds_sc1(ssrc + issued * 2048, islot + 40960 + (w & 1) * 1024);
    ++issued; islot = islot == 2 * 43008 ? 0 : islot + 43008;
  };
  for (int s = 0; s < NSEG; ++s) {
    if (issued == s) { poll(rs, fh + s * 4, 1, budget); issue(); }
    int pubv = s;
    if (issued == s + 1) wait_vm<0>(); else if (k1 == 2) wait_vm<11>(); else { wait_vm<13>(); pubv = s - 1; }
    if (dfr) consume();
    if (pubv > done) { __builtin_amdgcn_raw_buffer_store_b32(pubv, rs, fo, OFF_FLAG, SC1); done = pubv; }
    bar();
    nb = max(rdy, issued);
    asm volatile("buffer_load_dword %0, %2, %4, %5 offen sc1\n buffer_load_dword %1, %3, %4, %5 offen sc1" : "=&v"(n0), "=&v"(n1)
                 : "v"(fh + min(nb, NSEG - 1) * 4), "v"(fh + min(nb + 1, NSEG - 1) * 4), "s"(rs), "s"(OFF_FLAG) : "memory");
    LAS char* hb = lds + slot + l16;
    u32x2 sn[2][2];
#pragma unroll
    for (int T = 0; T < 2; ++T)
#pragma unroll
      for (int i = 0; i < 2; ++i) sn[T][i] = *(LAS u32x2*)(lds + slot + 40960 + T * 1024 + (2 * g + i) * 128 + n * 8);
    f32x4 acc[2][2];
    acc[0][0] = (f32x4){0.f, 0.f, 0.f, 0.f}; acc[0][1] = acc[0][0]; acc[1][0] = acc[0][0]; acc[1][1] = acc[0][0];
    u32x4 bf[4][2];
#pragma unroll
    for (int ks = 0; ks < 3; ++ks) { bf[ks][0] = *(LAS u32x4*)(hb + ks * 1024); bf[ks][1] = *(LAS u32x4*)(hb + (20 + ks) * 1024); }
    float rstd[2], mr[2];
#pragma unroll
    for (int ks = 0; ks < 20; ++ks) {
      if (ks + 3 < 20) { bf[(ks + 3) & 3][0] = *(LAS u32x4*)(hb + (ks + 3) * 1024); bf[(ks + 3) & 3][1] = *(LAS u32x4*)(hb + (23 + ks) * 1024); }
      __builtin_amdgcn_sched_barrier(0);
      acc[0][0] = mma32(wf[ks][0], bf[ks & 3][0], acc[0][0]); acc[1][0] = mma32(wf[ks][1], bf[ks & 3][0], acc[1][0]);
      acc[0][1] = mma32(wf[ks][0], bf[ks & 3][1], acc[0][1]); acc[1][1] = mma32(wf[ks][1], bf[ks & 3][1], acc[1][1]);
      if (ks == 2 || ks == 6) {
        const int T = ks == 6;
        const float m0 = __uint_as_float(sn[T][0].x), m1 = __uint_as_float(sn[T][1].x);
        const float mean = sum32(sum16(m0 + m1)) * 0.125f;
        const float d0 = m0 - mean, d1 = m1 - mean;
        const float var = sum32(sum16((__uint_as_float(sn[T][0].y) + __uint_as_float(sn[T][1].y)) + 80.f * (d0 * d0 + d1 * d1))) * (1.f / DM);
        rstd[T] = 1.f / sqrtf(var + 1e-5f); mr[T] = -mean * rstd[T];
      }
      __builtin_amdgcn_sched_barrier(0);
    }
#pragma unroll
    for (int T = 0; T < 2; ++T) {
      f32x4 a, c;
#pragma unroll
      for (int r = 0; r < 4; ++r) { a[r] = fmaxf(fmaf(acc[0][T][r], rstd[T], mr[T] * cs[0][r]), 0.f); c[r] = fmaxf(fmaf(acc[1][T][r], rstd[T], mr[T] * cs[1][r]), 0.f); }
      st16(pk8(a, c), rs, kb < 54 ? l16 : OOB, OFF_U2 + ((s * 2 + T) * 54 + kbc) * 1024);
    }
    fence();
    dfr = rdy > issued;
    if (!dfr) { wait_vm<2>(); consume(); }
    k1 = 0;
    if (issued < min(min(rdy, s + 3), NSEG)) { issue(); k1 = 1; if (issued < min(min(rdy, s + 3), NSEG)) { issue(); k1 = 2; } }
    if (k1 == 0) { publish(rs, fo, s + 1); done = s + 1; }
    slot = slot == 2 * 43008 ? 0 : slot + 43008;
  }
}

DEVI void role_cal(rsrc_t rs, int pg) {
  const int lane = lane_id();
  const unsigned base = OFF_M + pg * PAGE;
  __builtin_amdgcn_raw_buffer_store_b32(0u, rs, lane < 16 ? (lane & 1) * 8192 + 6144 + (lane >> 1) * 128 : OOB, base, SC1);
  wait_vm<0>();
  unsigned best[2] = {1u << 30, 1u << 30};
  for (int r = 0; r < 16; ++r) {
    unsigned v;
    const unsigned long long c0 = __builtin_amdgcn_s_memtime();
    asm volatile("buffer_load_dword %0, %1, %2, %3 offen sc1\n s_waitcnt vmcnt(0)" : "=v"(v) : "v"((r & 1) * 8192 + 6144 + (r >> 1) * 128), "s"(rs), "s"(base) : "memory");
    const unsigned long long c1 = __builtin_amdgcn_s_memtime();
    best[r & 1] = min(best[r & 1], (unsigned)(c1 - c0));
  }
  __builtin_amdgcn_raw_buffer_store_b32(best[1] < best[0] ? 3u : 2u, rs, lane == 0 ? F_SEL + pg * 128 : OOB, OFF_FLAG, SC1);
}

DEVI void role_f2(LAS char* lds, rsrc_t rs, const us16* w2, us16* out, int cg, int par0, int w) {
  const int lane = lane_id(), g = lane >> 4, n = lane & 15;
  const unsigned l16 = lane * 16;
  u32x4 wf[14][4];
  {
    LAS char* rp = lds + w * 28672 + (4 * g + (n >> 2)) * 64 + (n & 3) * 8;
#pragma unroll
    for (int hf = 0; hf < 2; ++hf) {
#pragma unroll
      for (int i = 0; i < 28; ++i) glds(w2 + (size_t)(min(14 * w + (i >> 1), 53) * 32 + (i & 1) * 16 + (lane >> 2)) * DM + cg * 64 + hf * 32 + (lane & 3) * 8, w * 28672 + i * 1024);
      wait_vm<0>();
#pragma unroll
      for (int i = 0; i < 14; ++i)
#pragma unroll
        for (int t = 0; t < 2; ++t) {
          u32x2 lo = trd(rp + i * 2048 + t * 32), hi = trd(rp + i * 2048 + t * 32 + 1024);
          const bool ok = i < 12 || w < 3;
          wf[i][hf * 2 + t] = ok ? (u32x4){lo.x, lo.y, hi.x, hi.y} : (u32x4){0u, 0u, 0u, 0u};
        }
      asm volatile("s_waitcnt lgkmcnt(0)" ::: "memory");
    }
  }
  bar();
  const unsigned ff = F_F1 + min(14 * w + min(lane & 15, 13), 53) * 128;
  int budget = SPIN_MAX, par = 0;
  for (int s = par0; s < NSEG; s += 2, par ^= 1) {
    poll(rs, ff, s + 1, budget);
    u32x4 u[14][2];
#pragma unroll
    for (int i = 0; i < 14; ++i)
#pragma unroll
      for (int T = 0; T < 2; ++T) u[i][T] = ld16(rs, l16, OFF_U2 + ((s * 2 + T) * 54 + min(14 * w + i, 53)) * 1024);
    const u32x4 hv = ld16(rs, l16, OFF_H + ((s * 2 + (w & 1)) * 20 + cg * 2 + (w >> 1)) * 1024);
    fence();
    f32x4 acc[4][2];
#pragma unroll
    for (int mt = 0; mt < 4; ++mt) { acc[mt][0] = (f32x4){0.f, 0.f, 0.f, 0.f}; acc[mt][1] = acc[mt][0]; }
#pragma unroll
    for (int i = 0; i < 14; ++i)
#pragma unroll
      for (int mt = 0; mt < 4; ++mt)
#pragma unroll
        for (int T = 0; T < 2; ++T) acc[mt][T] = mma32(wf[i][mt], u[i][T], acc[mt][T]);
    LAS char* rb = lds + par * 32768 + l16;
#pragma unroll
    for (int mt = 0; mt < 4; ++mt)
#pragma unroll
      for (int T = 0; T < 2; ++T) *(LAS f32x4*)(rb + (w * 8 + mt * 2 + T) * 1024) = acc[mt][T];
    bar();
    f32x4 lo = {0.f, 0.f, 0.f, 0.f}, hi = lo;
#pragma unroll
    for (int q = 0; q < 4; ++q) { lo += *(LAS f32x4*)(rb + (q * 8 + (w >> 1) * 4 + (w & 1)) * 1024); hi += *(LAS f32x4*)(rb + (q * 8 + (w >> 1) * 4 + 2 + (w & 1)) * 1024); }
    const u32x4 at = pk8(lo, hi);
    us16* op = out + (size_t)(s * 32 + (w & 1) * 16 + n) * DM + cg * 64 + (w >> 1) * 32 + 4 * g;
    *(u32x2*)op = (u32x2){pk(bflo(hv.x) + bflo(at.x), bfhi(hv.x) + bfhi(at.x)), pk(bflo(hv.y) + bflo(at.y), bfhi(hv.y) + bfhi(at.y))};
    *(u32x2*)(op + 16) = (u32x2){pk(bflo(hv.z) + bflo(at.z), bfhi(hv.z) + bfhi(at.z)), pk(bflo(hv.w) + bflo(at.w), bfhi(hv.w) + bfhi(at.w))};
  }
}

struct MegaArgs { const us16* x; const us16* Wq; const us16* Wk; const us16* Wv; const us16* Wo; const us16* w1; const us16* w2; us16* out; char* wb; int a1, a2, b1, b2; unsigned mask, pad; };

#define R8(p, a) p #a "0", p #a "1", p #a "2", p #a "3", p #a "4", p #a "5", p #a "6", p #a "7", p #a "8", p #a "9"
#define CL_COMMON \
  "v16", "v17", "v18", "v19", R8("v", 2), R8("v", 3), R8("v", 4), R8("v", 5), R8("v", 6), R8("v", 7), R8("v", 8), R8("v", 9), R8("v", 10), R8("v", 11), R8("v", 12), \
  R8("v", 13), R8("v", 14), R8("v", 15), R8("v", 16), R8("v", 17), R8("v", 18), R8("v", 19), R8("v", 20), R8("v", 21), R8("v", 22), R8("v", 23), R8("v", 24), \
  "v250", "v251", "v252", "v253", "v254", "v255", \
  R8("a", ), R8("a", 1), R8("a", 2), R8("a", 3), R8("a", 4), R8("a", 5), R8("a", 6), R8("a", 7), R8("a", 8), R8("a", 9), R8("a", 10), R8("a", 11), R8("a", 12), \
  R8("a", 13), R8("a", 14), R8("a", 15), R8("a", 16), R8("a", 17), R8("a", 18), R8("a", 19), R8("a", 20), R8("a", 21), R8("a", 22), R8("a", 23), R8("a", 24), \
  "a250", "a251", "a252", "a253", "a254", "a255", \
  R8("s", 4), R8("s", 5), R8("s", 6), R8("s", 7), R8("s", 8), R8("s", 9), "vcc", "scc", "memory"

#define BDLY 500
#define FDLY 800
DEVI void pause(unsigned ticks) {
  const unsigned t0 = (unsigned)__builtin_amdgcn_s_memrealtime();
  for (int k = 0; k < 4096 && (unsigned)__builtin_amdgcn_s_memrealtime() - t0 < ticks; ++k) __builtin_amdgcn_s_sleep(4);
}
__global__ void __launch_bounds__(256, 1) k_mega(MegaArgs p) {
  extern __shared__ __attribute__((aligned(16))) char smem[];
  LAS char* lds = (LAS char*)smem;
  const int b = blockIdx.x, w = __builtin_amdgcn_readfirstlane(threadIdx.x >> 6);
  p.wb = page_up(p.wb);
  const int r = (b >> 3) * 4 + (b & 3);
  const bool nearx = (b & 7) < 4;
  const int xcd = b & 7, i = b >> 3;
  const int qv = nearx ? (r == 120 || r == 121 ? r - 58 : -1) : xcd == 4 ? (i >= 18 ? i - 18 : -1) : xcd == 5 ? (i >= 18 ? i - 4 : -1) : xcd == 6 ? (i >= 22 ? i + 6 : -1) : (i >= 8 ? i + 30 : -1);
#define LANE_CONSTS \
  const int lane = lane_id(), G = lane >> 4, n = lane & 15, eta = (lane >> 3) & 1, r8 = lane & 7, q4 = n >> 2, p4 = n & 3; \
  const unsigned l16 = lane * 16, wo = (lane >> 2) * 8192 + (lane & 3) * 16, ring = 40960 + w * 28672; \
  const unsigned frag = G * 128 + r8 * 16, idx = eta ? 0xEEEEu : 0x4444u, fo0 = lane == 0 ? 0u : OOB, tr = ring + (8 * G + q4) * 64 + p4 * 8; \
  (void)wo; (void)frag; (void)idx; (void)fo0; (void)tr;
  const rsrc_t rs = __builtin_amdgcn_make_buffer_rsrc(p.wb, 0, 0x0ff00000, 0x00020000);
  if (nearx && r < 112) {
    if (r < 32) {
      if (!(p.mask & 1)) return;
      LANE_CONSTS
      const unsigned qim = 22528 + w * 2048 + (4 * G + q4) * 32 + p4 * 8;
      asm volatile(ASM_RA ::
        [ws] "s"(p.wb), [wa] "s"(p.Wq), [wb] "s"(p.Wk), [id] "s"(r), [w] "s"(w), [cp] "s"(b & 3), [d1] "s"(p.a1), [ca] "s"(p.a2), [qf] "v"(w == 1 && (lane & 1) ? 0u : 128u),
        [frag] "v"(frag), [idx] "v"(idx), [wo] "v"(wo), [tr] "v"(ring + (4 * G + (q4 & 1)) * 64 + (q4 >> 1) * 32 + p4 * 8),
        [trb] "v"((4 * G + q4) * 32 + (p4 & 1) * 8), [dw] "v"(n * 32 + G * 8), [q0] "v"(p4 < 2 ? qim : 30720u), [q1] "v"(p4 < 2 ? 30720u : qim),
        [msk] "v"(G < 2 ? 1.f : 0.f), [pox] "v"(!eta ? frag : G == 0 ? 512u + r8 * 16 : OOB), [kmo] "v"(eta ? OOB : frag + w * 512), [kmo2] "v"(eta || w != 1 ? OOB : frag),
        [l16] "v"(l16), [fo] "v"(fo0) : CL_COMMON);
      __builtin_amdgcn_endpgm();
    } else {
      if (!(p.mask & 2)) return;
      pause(BDLY);
      LANE_CONSTS
      const int c = r - 32, ks = c >> 2;
      const unsigned mo = (ks < 10 ? ks * 512 : 16384 + (ks - 10) * 512) + (c & 3) * 128;
      const unsigned zmr = (4 * eta + G) * 128 + r8 * 16;
      asm volatile(ASM_RB ::
        [ws] "s"(p.wb), [id] "s"(c), [w] "s"(w), [cp] "s"(b & 3), [mo] "s"(mo), [zs] "s"(OFF_Z + c * MB), [d1] "s"(p.b1), [d2] "s"(p.b2),
        [frag] "v"(frag), [idx] "v"(idx), [l16] "v"(l16), [zao] "v"(q4 * 128 + (4 * (G >> 1) + p4) * 16 + (G & 1) * 8), [zmr] "v"(zmr), [zmo2] "v"(w == 1 ? zmr : OOB),
        [co] "v"(((c / 20) * 32 + 8 * (w == 1 && eta ? 0 : w) + r8) * 128), [zxo] "v"(eta * 512 + frag), [pxo] "v"(eta * 640 + frag), [pmo] "v"((4 * eta + G) * 640 + 512 + r8 * 16),
        [mso] "v"(eta * 32768 + r8 * 16 + (G & 1) * 8 + (G >> 1) * 4), [fo] "v"(fo0), [fo1] "v"(w == 0 ? OOB : fo0) : CL_COMMON);
      __builtin_amdgcn_endpgm();
    }
    return;
  }
  if (qv >= 0) {
    const int h = qv >> 1;
    if (!(qv & 1)) {
      if (!(p.mask & 4)) return;
      LANE_CONSTS
      asm volatile(ASM_QK :: [ws] "s"(p.wb), [wa] "s"(p.Wq), [wb] "s"(p.Wk), [h] "s"(h), [w] "s"(w),
        [wo] "v"(wo), [tr] "v"(tr), [l16] "v"(l16), [fo] "v"(lane == 0 ? F_QK + (h * 4 + w) * 128 : OOB) : CL_COMMON);
      __builtin_amdgcn_endpgm();
    } else {
      if (!(p.mask & 8)) return;
      LANE_CONSTS
      asm volatile(ASM_VZ :: [ws] "s"(p.wb), [wa] "s"(p.Wv), [wb] "s"(p.Wo), [h] "s"(h), [w] "s"(w),
        [wo] "v"(wo), [wo2] "v"((lane >> 2) * 1280 + (lane & 3) * 16), [tr] "v"(tr), [tr2] "v"(ring + (4 * G + q4) * 64 + p4 * 8), [l16] "v"(l16),
        [zo] "v"(eta * MB + frag), [fo] "v"(lane == 0 ? F_VZ + (w * 32 + h) * 128 : OOB) : CL_COMMON);
      __builtin_amdgcn_endpgm();
    }
    return;
  }
  if (nearx && r >= 122 && r < 125) { role_cal(rs, (r - 122) * 4 + w); return; }
  if (!(p.mask & 16)) return;
  if (nearx) { if (r < 120) role_t1(rs, (r - 112) * 4 + w); }
  else if (i < 8) role_t2(lds, rs, p.x, i, xcd - 4, w);
  else if (pause(FDLY), xcd == 6) role_f1(lds, rs, p.wb, p.w1, i - 8, w);
  else role_f2(lds, rs, p.w2, p.out, i - 8, xcd - 4, w);
}

#define D_A1 46
#define D_A2 1700
#define D_B1 12
#define D_B2 66
#define ROLE_MASK 31
extern "C" void kernel_launch(void* const* d_in, const int* in_sizes, int n_in, void* d_out, int out_size, void* d_ws, size_t ws_size,
                              hipStream_t stream) {
  char* wb = (char*)d_ws;
  (void)hipFuncSetAttribute((const void*)k_mega, hipFuncAttributeMaxDynamicSharedMemorySize, LDS_BYTES);
  ProArgs pa = {(const us16*)d_in[0], (const us16*)d_in[1], (const us16*)d_in[4], (const us16*)d_in[5], wb, (const us16*)d_in[2], (const us16*)d_in[3]};
  k_pro<<<393 + NPW, 256, 40960, stream>>>(pa);
  MegaArgs ma = {(const us16*)d_in[0], (const us16*)d_in[2], (const us16*)d_in[3], (const us16*)d_in[4], (const us16*)d_in[5], (const us16*)d_in[8], (const us16*)d_in[9],
                 (us16*)d_out, wb, D_A1, D_A2, D_B1, D_B2, ROLE_MASK, 0};
  k_mega<<<256, 256, LDS_BYTES, stream>>>(ma);
}
```
